```python
import torch

def _softmax(x, axis=-1):
    return torch.softmax(x, dim=axis)
def _gelu(x):
    return torch.nn.functional.gelu(x, approximate="tanh")
def _silu(x):
    return torch.nn.functional.silu(x)
def _sigmoid(x):
    return torch.sigmoid(x)
def _layernorm(x, axis=-1, eps=1e-5):
    m = x.mean(dim=axis, keepdim=True)
    v = x.var(dim=axis, keepdim=True, unbiased=False)
    return (x - m) / torch.sqrt(v + eps)
def _scatter_add(tgt, idx, src, axis=0):
    return tgt.clone().index_add_(axis, idx, src)
def _segment_sum(src, seg, n):
    out = torch.zeros((n,) + src.shape[1:], dtype=src.dtype)
    return out.index_add_(0, seg, src)
def _pad(x, pw):
    flat = tuple(v for pair in reversed(pw) for v in pair)
    return torch.nn.functional.pad(x, flat)

kernel_name = 'gated_mix__spectral_gfnet__ssm_conv_s5__prompt'

def setup_inputs():

    _w = (lambda a: (lambda t: t.to(torch.bfloat16) if t.is_floating_point() else t)(torch.as_tensor(a)))
    def _A_setup():
        B, S, D = 1, 2048, 640
        Df = D // 2 + 1
        return {
            "x": _w((lambda *s: torch.randn(*s, dtype=torch.bfloat16) * 0.05)(B, S, D)),
            "w_r": _w((lambda *s: torch.randn(*s, dtype=torch.bfloat16) * 0.8)(S, Df) * 0.02),
            "w_i": _w((lambda *s: torch.randn(*s, dtype=torch.bfloat16) * 0.8)(S, Df) * 0.02),
        }
    _A_in = _A_setup()

    def _B_setup():
        B, S, D = 1, 2048, 640
        P = D
        Lambda_re = -0.5 * (lambda _x: torch.exp(torch.as_tensor(_x)))((lambda *s: torch.randn(*s, dtype=torch.bfloat16) * 0.2)(P))
        Lambda_im = (lambda *s: torch.randn(*s, dtype=torch.bfloat16) * 0.2)(P) * 1.0
        B_re = (lambda *s: torch.randn(*s, dtype=torch.bfloat16) * 0.2)(P, D) * (D ** -0.5)
        B_im = (lambda *s: torch.randn(*s, dtype=torch.bfloat16) * 0.2)(P, D) * (D ** -0.5)
        C_re = (lambda *s: torch.randn(*s, dtype=torch.bfloat16) * 0.2)(D, P) * (P ** -0.5)
        C_im = (lambda *s: torch.randn(*s, dtype=torch.bfloat16) * 0.2)(D, P) * (P ** -0.5)
        log_dt = (lambda *s: torch.randn(*s, dtype=torch.bfloat16) * 0.2)(P) * 0.3 - 2.0
        Dskip = (lambda *s: torch.randn(*s, dtype=torch.bfloat16) * 0.05)(D)
        return {
            "x": _w((lambda *s: torch.randn(*s, dtype=torch.bfloat16) * 0.05)(B, S, D)),
            "Lambda_re": _w(Lambda_re),
            "Lambda_im": _w(Lambda_im),
            "B_re": _w(B_re),
            "B_im": _w(B_im),
            "C_re": _w(C_re),
            "C_im": _w(C_im),
            "log_dt": _w(log_dt),
            "Dskip": _w(Dskip),
        }
    _B_in = _B_setup()

    return {
        "A_x": _A_in["x"],
        "A_w_r": _A_in["w_r"],
        "A_w_i": _A_in["w_i"],
        "B_x": _B_in["x"],
        "B_Lambda_re": _B_in["Lambda_re"],
        "B_Lambda_im": _B_in["Lambda_im"],
        "B_B_re": _B_in["B_re"],
        "B_B_im": _B_in["B_im"],
        "B_C_re": _B_in["C_re"],
        "B_C_im": _B_in["C_im"],
        "B_log_dt": _B_in["log_dt"],
        "B_Dskip": _B_in["Dskip"],
        "gate_w": _w((lambda *s: torch.randn(*s, dtype=torch.bfloat16) * 0.05)(640) * 16.0),
    }

def reference(A_x, A_w_r, A_w_i, B_x, B_Lambda_re, B_Lambda_im, B_B_re, B_B_im, B_C_re, B_C_im, B_log_dt, B_Dskip, gate_w):
    def _A_ref(x, w_r, w_i):
        B, S, D = x.shape
        X = (lambda x, axes=(-2,-1): torch.fft.rfft2(x.float(), dim=axes))(x, axes=(-2, -1))
        W = w_r + 1j * w_i
        X = X * W
        y = (lambda x, s, axes=(-2,-1): torch.fft.irfft2(x, s=s, dim=axes).to(torch.bfloat16))(X, (S, D), axes=(-2, -1))
        return y

    def _B_ref(x, Lambda_re, Lambda_im, B_re, B_im, C_re, C_im, log_dt, Dskip):
        Bsz, L, H = x.shape
        Lam = Lambda_re + 1j * Lambda_im
        Bmat = B_re + 1j * B_im
        Cmat = C_re + 1j * C_im
        dt = torch.exp(log_dt)
        Lam_bar = torch.exp(dt * Lam)
        B_bar = ((Lam_bar - 1.0) / Lam)[:, None] * Bmat
        Bu = torch.einsum("bld,pd->blp", x + 0j, B_bar)
        h = torch.zeros_like(Bu[:, 0, :])
        hs = []
        for t in range(L):
            h = Lam_bar[None, :] * h + Bu[:, t, :]
            hs.append(h)
        hseq = torch.stack(hs, 1)
        y = 2.0 * torch.einsum("blp,dp->bld", hseq, Cmat).real + x * Dskip[None, None, :]
        return y

    x = A_x
    g = _sigmoid((x * gate_w).sum(-1, keepdims=True))
    a = _A_ref(x, A_w_r, A_w_i)
    b = _B_ref(x, B_Lambda_re, B_Lambda_im, B_B_re, B_B_im, B_C_re, B_C_im, B_log_dt, B_Dskip)
    return g * a + (1 - g) * b
```

```cpp
#include <hip/hip_runtime.h>

typedef unsigned short u16;
typedef unsigned int u32;
typedef unsigned long long u64;
typedef __attribute__((ext_vector_type(2))) float f32x2;
typedef __attribute__((ext_vector_type(4))) float f32x4;
typedef __attribute__((ext_vector_type(16))) float f32x16;
typedef __attribute__((ext_vector_type(2))) u32 u32x2;
typedef __attribute__((ext_vector_type(4))) u32 u32x4;
typedef __attribute__((ext_vector_type(2))) __bf16 bf16x2;
typedef __attribute__((ext_vector_type(8))) __bf16 bf16x8;
typedef __attribute__((ext_vector_type(4))) short s16x4;
#define GAS __attribute__((address_space(1)))
#define LAS __attribute__((address_space(3)))
#define RLX __ATOMIC_RELAXED, __HIP_MEMORY_SCOPE_AGENT
#define DEV __device__ __forceinline__

#define STAMPS 0

constexpr u32 OFF_E = 0, OFF_ARR = 4096, OFF_X1F = 8192, OFF_ZFL = 12288, OFF_HFL = 16384, OFF_GATE = 32768,
              OFF_AGG = 65536, OFF_X1 = 262144, OFF_Z = 3u << 20, OFF_H = 6u << 20, OFF_STAMP = 12u << 20,
              WS_BYTES = 13u << 20;
constexpr u32 X1_PLANE = 328 * 16;
constexpr u32 L_WST = 81920, L_ZPL = 40960, L_RED = 81920, L_SM = 143360, L_TOTAL = 147456;
constexpr u32 ZROW = 4096 + 64;
constexpr u32 SPIN_CAP = 1u << 18;

struct Args {
    const u16 *x, *wr, *wi, *lre, *lim, *bre, *bim, *cre, *cim, *ldt, *dsk, *gw;
    float* out;
    char* ws;
};

DEV u32 pk(float a, float b) { f32x2 v = {a, b}; return __builtin_bit_cast(u32, __builtin_convertvector(v, bf16x2)); }
DEV float asf(u32 v) { return __builtin_bit_cast(float, v); }
DEV u32 asu(float v) { return __builtin_bit_cast(u32, v); }
DEV bf16x2 asb(u32 v) { return __builtin_bit_cast(bf16x2, v); }
DEV float bfl(u32 v) { return __builtin_bit_cast(float, v << 16); }
DEV float bfh(u32 v) { return __builtin_bit_cast(float, v & 0xffff0000u); }
DEV float rbf(float a) { return bfl(pk(a, a)); }
DEV f32x4 mfma16(u32x4 a, u32x4 b, f32x4 c) {
    return __builtin_amdgcn_mfma_f32_16x16x32_bf16(__builtin_bit_cast(bf16x8, a), __builtin_bit_cast(bf16x8, b), c, 0, 0, 0);
}
DEV f32x16 mfma32(u32x4 a, u32x4 b, f32x16 c) {
    return __builtin_amdgcn_mfma_f32_32x32x16_bf16(__builtin_bit_cast(bf16x8, a), __builtin_bit_cast(bf16x8, b), c, 0, 0, 0);
}
DEV float cosr(float r) { return __builtin_amdgcn_cosf(r); }
DEV float sinr(float r) { return __builtin_amdgcn_sinf(r); }
#define WAITVM(n) asm volatile("s_waitcnt vmcnt(" #n ")" ::: "memory")
DEV void bar() { asm volatile("s_waitcnt lgkmcnt(0)" ::: "memory"); __builtin_amdgcn_s_barrier(); asm volatile("" ::: "memory"); }
template <int AUX> DEV void glds(const void* g, LAS char* l) {
    __builtin_amdgcn_global_load_lds((const GAS void*)g, (LAS void*)l, 16, 0, AUX);
}
DEV u32x2 trd(LAS char* p) {
    return __builtin_bit_cast(u32x2, __builtin_amdgcn_ds_read_tr16_b64_v4i16((LAS s16x4*)p));
}
DEV u64 ldflag(char* p) { return __hip_atomic_load((GAS u64*)p, RLX); }
DEV void stflag(char* p, u64 v) { __hip_atomic_store((GAS u64*)p, v, RLX); }
DEV u32 sig(u32 t, u32 i) { return 8 * (i >> 2) + 4 * t + (i & 3); }

struct C2 { float re, im; };
DEV C2 cmul(C2 a, C2 b) { return {a.re * b.re - a.im * b.im, a.re * b.im + a.im * b.re}; }
DEV C2 cfma(C2 a, C2 b, C2 c) { return {a.re * b.re - a.im * b.im + c.re, a.re * b.im + a.im * b.re + c.im}; }

#define STAMP(k) do {} while (0)

DEV void wait2(char* p0, char* p1, u64 tag) {
    for (u32 s = 0; s < SPIN_CAP; ++s) {
 asm volatile("" ::: "memory");
        const u64 f0 = ldflag(p0), f1 = ldflag(p1);
        if (__all(f0 == tag && f1 == tag)) break;
    }
    asm volatile("" ::: "memory");
}
DEV void wait1(char* p0, u64 tag) {
    for (u32 s = 0; s < SPIN_CAP; ++s) {
 asm volatile("" ::: "memory");
        const u64 f0 = ldflag(p0);
        if (__all(f0 == tag)) break;
    }
    asm volatile("" ::: "memory");
}

__global__ __launch_bounds__(256, 1) void gmix_kernel(Args a) {
    __shared__ __attribute__((aligned(1024))) char smem[L_TOTAL];
    LAS char* lds = (LAS char*)smem;
    const u32 tid = threadIdx.x, lane = tid & 63;
    const u32 w = __builtin_amdgcn_readfirstlane(tid >> 6);
    const u32 b = blockIdx.x, xcd = b & 7, bi = b >> 3;
    const u32 li = lane & 15, lg = lane >> 4;
    const auto rs = __builtin_amdgcn_make_buffer_rsrc(a.ws, 0, WS_BYTES, 0x00020000);
    STAMP(0);
    const u64 e0 = ldflag(a.ws + OFF_E);
    u64 tag;
    u32 tlo, thi;

    const u32 mcc = 4 * xcd + (bi >> 3), d0 = 80 * (bi & 7);

    if (bi < 20) {
        const u32 mc = 4 * (xcd >> 1) + bi / 5, nt = 5 * (xcd & 1) + bi % 5;
        const u32 t0 = 128 * mc, p0 = 64 * nt;
        const u32 wm = w >> 1, wn = w & 1, n = lane & 31, h = lane >> 5;
        u32 aoff[4], boff[4];
#pragma unroll
        for (int q = 0; q < 4; ++q) {
            const u32 jj = (lane >> 3) & 3;
            const u32 ch = (lane & 7) ^ ((4 * q + (lane >> 4)) & 7);
            const u32 t = t0 + 64 * wm + 32 * h + 16 * (jj & 1) + 8 * wn + 2 * q + (jj >> 1);
            aoff[q] = t * 1280 + ch * 16;
            boff[q] = (p0 + 32 * wm + 8 * q + (lane >> 3)) * 1280 + ch * 16;
        }
        const char* xb = (const char*)a.x;
        const char* bb = (const char*)(wn ? a.bim : a.bre);
        auto issue = [&](u32 s) {
            LAS char* dst = lds + (s & 3) * 32768 + w * 4096;
#pragma unroll
            for (int q = 0; q < 4; ++q) glds<0>(xb + aoff[q] + s * 128, dst + q * 1024);
#pragma unroll
            for (int q = 0; q < 4; ++q) glds<0>(bb + boff[q] + s * 128, dst + 16384 + q * 1024);
        };
        const u32 p = p0 + 32 * wn + n;
        const u32 plr = a.lre[p], pli = a.lim[p], pld = a.ldt[p];
        issue(0); issue(1); issue(2);
        u32 fo[4];
#pragma unroll
        for (int kk = 0; kk < 4; ++kk) fo[kk] = n * 128 + (((2 * kk + h) ^ ((n >> 1) & 7)) << 4);
        f32x16 aR[2] = {}, aI[2] = {};
#pragma unroll 1
        for (u32 s = 0; s < 10; ++s) {
            if (s < 8) WAITVM(16); else if (s == 8) WAITVM(8); else WAITVM(0);
            bar();
            if (s < 7) issue(s + 3);
            LAS char* A = lds + (s & 3) * 32768 + wm * 8192;
            LAS char* B = lds + (s & 3) * 32768 + 16384 + wn * 8192;
#pragma unroll
            for (int kk = 0; kk < 4; ++kk) {
                const u32x4 a0 = *(LAS u32x4*)(A + fo[kk]), a1 = *(LAS u32x4*)(A + 4096 + fo[kk]);
                const u32x4 b0 = *(LAS u32x4*)(B + fo[kk]), b1 = *(LAS u32x4*)(B + 4096 + fo[kk]);
                aR[0] = mfma32(a0, b0, aR[0]); aI[0] = mfma32(a0, b1, aI[0]);
                aR[1] = mfma32(a1, b0, aR[1]); aI[1] = mfma32(a1, b1, aI[1]);
            }
        }
        STAMP(1);
        tag = e0 * 6364136223846793005ull + 1ull; tlo = (u32)tag; thi = (u32)(tag >> 32);
        if (tid == 0) stflag(a.ws + OFF_ARR + b * 8, tag);
        const float lr = bfl(plr), lim = bfl(pli), dt = rbf(expf(bfl(pld)));
        float sn, cs;
        sincosf(dt * lim, &sn, &cs);
        const float mag = expf(dt * lr);
        const C2 L = {mag * cs, mag * sn};
        const float inv = 1.0f / (lr * lr + lim * lim);
        const C2 coef = {((L.re - 1.0f) * lr + L.im * lim) * inv, (L.im * lr - (L.re - 1.0f) * lim) * inv};
        const C2 L2 = cmul(L, L), L4 = cmul(L2, L2), L8 = cmul(L4, L4), L16 = cmul(L8, L8), L32 = cmul(L16, L16),
                 L64 = cmul(L32, L32), L128 = cmul(L64, L64), L256 = cmul(L128, L128), L512 = cmul(L256, L256);
        f32x2 sr = {0.f, 0.f}, si = {0.f, 0.f};
#pragma unroll
        for (int pos = 0; pos < 16; ++pos) {
            const int mt = pos >> 3, r = 2 * (pos & 7);
            const f32x2 ur = {aR[mt][r], aR[mt][r + 1]}, ui = {aI[mt][r], aI[mt][r + 1]};
            const f32x2 nr = L.re * sr + (ur - L.im * si), ni = L.re * si + (ui + L.im * sr);
            sr = nr; si = ni;
            aR[mt][r] = sr.x; aR[mt][r + 1] = sr.y; aI[mt][r] = si.x; aI[mt][r + 1] = si.y;
        }
        const C2 a0 = {sr.x, si.x}, a1 = {sr.y, si.y};
        const C2 A32 = cfma(L16, a0, a1);
        const C2 O32 = {__shfl_xor(A32.re, 32), __shfl_xor(A32.im, 32)};
        const C2 lo = h ? O32 : A32, hi = h ? A32 : O32;
        const C2 W64 = cfma(L32, lo, hi);
        LAS f32x2* sW = (LAS f32x2*)(lds + L_SM);
        LAS f32x2* sP = (LAS f32x2*)(lds + L_SM + 1024);
        LAS float* sG = (LAS float*)(lds + L_SM + 2048);
        if (h == 0) sW[wm * 64 + 32 * wn + n] = f32x2{W64.re, W64.im};
        if (tid < 128) {
            u32x4 gr;
            for (u32 s = 0; s < SPIN_CAP; ++s) {
 asm volatile("" ::: "memory");
                gr = __builtin_amdgcn_raw_buffer_load_b128(rs, OFF_GATE + (t0 + tid) * 16, 0, 16);
                if (__all(gr.y == tlo && gr.w == thi)) break;
            }
            sG[tid] = 2.0f * asf(gr.z);
        }
        bar();
        const f32x2 w0v = sW[32 * wn + n];
        const C2 W0 = {w0v.x, w0v.y};
        if (wm == 1 && h == 0) {
            const C2 ag = cfma(L64, W0, W64);
            const u32x4 gr = {asu(ag.re), tlo, asu(ag.im), thi};
            __builtin_amdgcn_raw_buffer_store_b128(gr, rs, OFF_AGG + (mc * 640 + p) * 16, 0, 16);
        }
        STAMP(2);
        C2 S0 = {0.f, 0.f}, PW = {1.f, 0.f};
        if (wm) { S0 = W0; PW = L64; }
        if (h) { S0 = cfma(L32, S0, lo); PW = cmul(PW, L32); }
        const C2 S1 = cfma(L16, S0, a0), PW1 = cmul(PW, L16);
        f32x2 qr[16], qi[16];
        {
            C2 q = cmul(coef, L);
#pragma unroll
            for (int pos = 0; pos < 16; ++pos) {
                const int mt = pos >> 3, r = 2 * (pos & 7);
                const f32x2 s2 = {sG[64 * wm + 32 * h + pos], sG[64 * wm + 32 * h + 16 + pos]};
                const f32x2 ur = {aR[mt][r], aR[mt][r + 1]}, ui = {aI[mt][r], aI[mt][r + 1]};
                const f32x2 cr = s2 * coef.re, ci = s2 * coef.im;
                const f32x2 hr = cr * ur - ci * ui, hi_ = -(cr * ui + ci * ur);
                aR[mt][r] = hr.x; aR[mt][r + 1] = hr.y; aI[mt][r] = hi_.x; aI[mt][r + 1] = hi_.y;
                qr[pos] = s2 * q.re; qi[pos] = s2 * q.im;
                q = cmul(q, L);
            }
        }
        C2 part = {0.f, 0.f};
        {
            const int q = 2 * wm + h;
            u32x4 g4[4];
            for (u32 s = 0; s < SPIN_CAP; ++s) {
 asm volatile("" ::: "memory");
                bool ok = true;
#pragma unroll
                for (int m = 0; m < 4; ++m) {
                    const int c = (int)mc - 1 - q - 4 * m;
                    g4[m] = u32x4{0u, tlo, 0u, thi};
                    if (c >= 0) g4[m] = __builtin_amdgcn_raw_buffer_load_b128(rs, OFF_AGG + (c * 640 + p) * 16, 0, 16);
                    ok = ok && g4[m].y == tlo && g4[m].w == thi;
                }
                if (__all(ok)) break;
            }
#pragma unroll
            for (int m = 3; m >= 0; --m)
                part = cfma(L512, part, C2{asf(g4[m].x), asf(g4[m].z)});
            if (h) part = cmul(part, L128);
            if (wm) part = cmul(part, L256);
            part.re += __shfl_xor(part.re, 32); part.im += __shfl_xor(part.im, 32);
            if (h == 0) sP[wm * 64 + 32 * wn + n] = f32x2{part.re, part.im};
        }
        bar();
        STAMP(3);
        const f32x2 h0 = sP[32 * wn + n], h1 = sP[64 + 32 * wn + n];
        const C2 H = {h0.x + h1.x, h0.y + h1.y};
        const C2 K0 = cfma(PW, H, S0), K1 = cfma(PW1, H, S1);
        const f32x2 kr = {K0.re, K1.re}, ki = {K0.im, K1.im};
#pragma unroll
        for (int pos = 0; pos < 16; ++pos) {
            const int mt = pos >> 3, r = 2 * (pos & 7);
            const f32x2 ur = {aR[mt][r], aR[mt][r + 1]}, ui = {aI[mt][r], aI[mt][r + 1]};
            const f32x2 hr = ur + qr[pos] * kr - qi[pos] * ki, hi_ = ui - qr[pos] * ki - qi[pos] * kr;
            aR[mt][r] = hr.x; aR[mt][r + 1] = hr.y; aI[mt][r] = hi_.x; aI[mt][r + 1] = hi_.y;
        }
        LAS char* T = lds + w * 8192;
        const u32 fsw = (n & 3) | ((((n >> 2) ^ (n >> 3)) & 1) << 2);
#pragma unroll
        for (int c = 0; c < 2; ++c)
#pragma unroll
            for (int mt = 0; mt < 2; ++mt) {
                const u32 u = ((4 * h + 2 * c + mt) ^ fsw) << 4;
                *(LAS u32x4*)(T + n * 128 + u) = u32x4{pk(aR[mt][c], aR[mt][2 + c]), pk(aR[mt][4 + c], aR[mt][6 + c]),
                                                       pk(aR[mt][8 + c], aR[mt][10 + c]), pk(aR[mt][12 + c], aR[mt][14 + c])};
                *(LAS u32x4*)(T + 4096 + n * 128 + u) = u32x4{pk(aI[mt][c], aI[mt][2 + c]), pk(aI[mt][4 + c], aI[mt][6 + c]),
                                                              pk(aI[mt][8 + c], aI[mt][10 + c]), pk(aI[mt][12 + c], aI[mt][14 + c])};
            }
        asm volatile("s_waitcnt lgkmcnt(0)" ::: "memory");
        {
            const u32 r = li >> 2, cq = li & 3, cg = 2 * nt + wn;
#pragma unroll
            for (int tt = 0; tt < 4; ++tt)
#pragma unroll
                for (int ri = 0; ri < 2; ++ri) {
                    u32x2 v[2];
#pragma unroll
                    for (int hh = 0; hh < 2; ++hh) {
                        const u32 row = 8 * lg + 4 * hh + r;
                        const u32 f = (row & 3) | ((((row >> 2) ^ (row >> 3)) & 1) << 2);
                        v[hh] = trd(T + ri * 4096 + row * 128 + (((2 * tt + (cq >> 1)) ^ f) << 4) + (cq & 1) * 8);
                    }
                    __builtin_amdgcn_raw_buffer_store_b128(u32x4{v[0].x, v[0].y, v[1].x, v[1].y}, rs,
                        OFF_H + ((8 * mc + 4 * wm + tt) * 40 + 2 * cg + ri) * 1024 + lane * 16, 0, 16);
                }
            WAITVM(0);
            if (lane == 0) stflag(a.ws + OFF_HFL + ((2 * mc + wm) * 32 + cg) * 8, tag);
        }
        STAMP(4);
    } else {
        int job = -1;
        if (bi >= 22) job = 10 * xcd + bi - 22;
        if (b == 7 + 8 * 21) job = 80;
        u32x2 wv[16];
        const u32 G = xcd >> 1;
        u32 tgA = 0;
        const u32 beta = 20 * G + 10 * (xcd & 1) + (bi - 20);
        if (bi < 30) {
            tgA = (4 * beta) / 5;
            const u32 ntg = (4 * beta + 3) / 5 - tgA + 1;
            u32 so[5];
#pragma unroll
            for (int q = 0; q < 5; ++q) {
                const u32 Lx = 64 * q + lane, cq = Lx / 80;
                so[q] = (Lx ^ ((cq & 1) * 2 + (cq >> 1) * 8)) * 16;
            }
            for (u32 s = 0; s < ntg; ++s)
#pragma unroll
                for (int c2 = 0; c2 < 2; ++c2) {
                    const u32 jp = 2 * w + c2;
                    const char* src = (const char*)a.x + (512 * G + 64 * jp + 4 * ((tgA + s) & 15)) * 1280;
#pragma unroll
                    for (int q = 0; q < 5; ++q) glds<0>(src + so[q], lds + s * 40960 + jp * 5120 + q * 1024);
                }
        }
        if (job >= 0) {
            const u32 col0 = job == 80 ? 317 : 4 * job;
#pragma unroll
            for (int s = 0; s < 8; ++s) {
                const u32 nn = s * 256 + tid, k = ((nn >> 5) & 31) + 32 * (nn & 31) + 1024 * (nn >> 10);
                typedef __attribute__((aligned(2))) u32x2 u32x2u;
                wv[2 * s] = *(const GAS u32x2u*)(a.wr + k * 321 + col0);
                wv[2 * s + 1] = *(const GAS u32x2u*)(a.wi + k * 321 + col0);
            }
        }
        if (bi < 30) {
            const u32 u = 4 * beta + w, tg = u / 5, ct = u % 5, cp = 16 * ct + li;
            u32x4 tb[2][5];
            {
                const float st = (float)cp * (1.0f / 160.0f);
                const bool sp = cp == 0;
#pragma unroll
                for (int ks = 0; ks < 5; ++ks)
#pragma unroll
                    for (int j = 0; j < 4; ++j) {
                        const float m0 = (float)(32 * ks + 2 * j) + 8.0f * (float)lg;
                        const float x0 = m0 * st, x1 = x0 + st;
                        tb[0][ks][j] = pk(cosr(x0), cosr(x1));
                        tb[1][ks][j] = sp ? 0xBF803F80u : pk(-sinr(x0), -sinr(x1));
                    }
            }
            C2 tw[4];
#pragma unroll
            for (int e = 1; e < 4; ++e) { const float x = (float)(e * cp) * (1.0f / 640.0f); tw[e] = {cosr(x), -sinr(x)}; }
            if (job >= 0) WAITVM(16); else WAITVM(0);
            bar();
            STAMP(1);
            f32x4 yr[8] = {}, yi[8] = {};
            {
                const u32 r = li >> 2, cq = li & 3;
                const u32 sw = (cq & 1) * 32 + (cq >> 1) * 128;
                LAS char* X = lds + (tg - tgA) * 40960 + cq * 1280;
                const u32 o0 = (64 * lg + 8 * r) ^ sw, o1 = (64 * lg + 32 + 8 * r) ^ sw;
#pragma unroll
                for (int ks = 0; ks < 5; ++ks)
#pragma unroll
                    for (int j = 0; j < 8; ++j) {
                        const u32x2 v0 = trd(X + j * 5120 + ks * 256 + o0), v1 = trd(X + j * 5120 + ks * 256 + o1);
                        const u32x4 af = {v0.x, v0.y, v1.x, v1.y};
                        yr[j] = mfma16(af, tb[0][ks], yr[j]);
                        yi[j] = mfma16(af, tb[1][ks], yi[j]);
                    }
            }
            float o[8][8];
#pragma unroll
            for (int j = 0; j < 8; ++j) {
                const bool sp = cp == 0;
                C2 Y[4];
#pragma unroll
                for (int e = 0; e < 4; ++e) Y[e] = {yr[j][e], sp ? 0.f : yi[j][e]};
                const C2 U1 = cmul(tw[1], Y[1]), U2 = cmul(tw[2], Y[2]), U3 = cmul(tw[3], Y[3]);
                const C2 A = {Y[0].re + U2.re, Y[0].im + U2.im}, B = {Y[0].re - U2.re, Y[0].im - U2.im};
                const C2 C = {U1.re + U3.re, U1.im + U3.im}, D = {U1.re - U3.re, U1.im - U3.im};
                o[0][j] = A.re + C.re; o[1][j] = A.im + C.im;
                o[2][j] = A.re - C.re; o[3][j] = C.im - A.im;
                o[4][j] = B.re + D.im; o[5][j] = B.im - D.re;
                o[6][j] = B.re - D.im; o[7][j] = -(B.im + D.re);
                if (ct == 0) {
                    const float dm = 0.70710678f * (yi[j][1] - yi[j][3]), dp = 0.70710678f * (yi[j][1] + yi[j][3]);
                    if (sp) { o[1][j] = yi[j][0] + dm; o[3][j] = -yi[j][2] - dp; o[6][j] = yi[j][0] - dm; o[7][j] = yi[j][2] - dp; }
                }
            }
            {
                const bool sp = cp == 0;
                const u32 base = OFF_X1 + (G * 64 + 4 * (tg & 15) + lg) * 2 * X1_PLANE;
                const u32 of[8] = {cp * 16, sp ? 80u * 16 : X1_PLANE + cp * 16, (320 - cp) * 16,
                                   X1_PLANE + (sp ? 80u : 320 - cp) * 16, (160 + cp) * 16, X1_PLANE + (160 + cp) * 16,
                                   (sp ? 240u : 160 - cp) * 16, X1_PLANE + (sp ? 240u : 160 - cp) * 16};
#pragma unroll
                for (int q = 0; q < 8; ++q)
                    __builtin_amdgcn_raw_buffer_store_b128(
                        u32x4{pk(o[q][0], o[q][1]), pk(o[q][2], o[q][3]), pk(o[q][4], o[q][5]), pk(o[q][6], o[q][7])}, rs,
                        base + of[q], 0, 16);
            }
            tag = e0 * 6364136223846793005ull + 1ull; tlo = (u32)tag; thi = (u32)(tag >> 32);
            WAITVM(0);
            if (lane == 0) stflag(a.ws + OFF_X1F + (ct * 64 + tg) * 8, tag);
            STAMP(2);
        } else {
            const u32 r0 = 128 * (2 * xcd + bi - 30) + 32 * w + (lane >> 3);
            u32x4 gwv[10];
#pragma unroll
            for (int s = 0; s < 10; ++s) gwv[s] = *(const GAS u32x4*)(a.gw + (s * 8 + (lane & 7)) * 8);
            tag = e0 * 6364136223846793005ull + 1ull; tlo = (u32)tag; thi = (u32)(tag >> 32);
#pragma unroll 1
            for (int rg = 0; rg < 4; rg += 2) {
                u32x4 xv[2][10];
#pragma unroll
                for (int z = 0; z < 2; ++z)
#pragma unroll
                    for (int s = 0; s < 10; ++s) xv[z][s] = *(const GAS u32x4*)(a.x + (r0 + 8 * (rg + z)) * 640 + (s * 8 + (lane & 7)) * 8);
#pragma unroll
                for (int z = 0; z < 2; ++z) {
                    float acc = 0.f;
#pragma unroll
                    for (int s = 0; s < 10; ++s)
#pragma unroll
                        for (int j = 0; j < 4; ++j)
                            acc = __builtin_amdgcn_fdot2_f32_bf16(asb(xv[z][s][j]),
                                                                  asb(gwv[s][j]), acc, false);
                    acc += __shfl_xor(acc, 1); acc += __shfl_xor(acc, 2); acc += __shfl_xor(acc, 4);
                    const float g = rbf(1.0f / (1.0f + expf(-rbf(acc))));
                    const float og = rbf(1.0f - g);
                    if ((lane & 7) == 0)
                        __builtin_amdgcn_raw_buffer_store_b128(
                            u32x4{asu(g), tlo, asu(og), thi}, rs,
                            OFF_GATE + (r0 + 8 * (rg + z)) * 16, 0, 16);
                }
            }
            STAMP(2);
        }
        if (tid == 0) stflag(a.ws + OFF_ARR + b * 8, tag);
        if (job >= 0) {
            {
                LAS u32* WS = (LAS u32*)(lds + L_WST);
#pragma unroll
                for (int s = 0; s < 8; ++s) {
                    const u32 nn = s * 256 + tid, o = (nn >> 5) * 36 + (nn & 31);
                    const u32x2 r = wv[2 * s], i = wv[2 * s + 1];
                    WS[o] = (r.x & 0xffffu) | (i.x << 16);
                    WS[2304 + o] = (r.x >> 16) | (i.x & 0xffff0000u);
                    WS[2 * 2304 + o] = (r.y & 0xffffu) | (i.y << 16);
                    WS[3 * 2304 + o] = (r.y >> 16) | (i.y & 0xffff0000u);
                }
            }
            bar();
            u32x4 fc[2], fs[2], fn[2];
#pragma unroll
            for (int t = 0; t < 2; ++t)
#pragma unroll
                for (int j = 0; j < 4; ++j) {
                    const u32 sg = sig(t, li), k0 = 8 * lg + 2 * j;
                    const float x0 = (float)((sg * k0) & 31) * (1.0f / 32.0f), x1 = (float)((sg * (k0 + 1)) & 31) * (1.0f / 32.0f);
                    fc[t][j] = pk(cosr(x0), cosr(x1));
                    fs[t][j] = pk(sinr(x0), sinr(x1));
                    fn[t][j] = fs[t][j] ^ 0x80008000u;
                }
            f32x4 t1c[2][2], t1s[2][2];
            f32x4 Pr[2][2], Pi[2][2], Q1r[2][2], Q1i[2][2], Q2r[2][2], Q2i[2][2];
            {
                const u32 wc = job == 80 ? 3 : w;
                const float sc = ((job == 0 && w == 0) || job == 80 ? 1.0f : 2.0f) / (2048.0f * 640.0f);
                LAS u32* WS = (LAS u32*)(lds + L_WST) + wc * 2304;
#pragma unroll
                for (int rt = 0; rt < 2; ++rt)
#pragma unroll
                    for (int ctl = 0; ctl < 2; ++ctl) {
                        const u32 sg = sig(ctl, li);
                        const u32x4 wp = *(LAS u32x4*)(WS + sg * 36 + 8 * lg + 4 * rt);
                        const u32x4 wm = *(LAS u32x4*)(WS + 32 * 36 + sg * 36 + 8 * lg + 4 * rt);
#pragma unroll
                        for (int j = 0; j < 4; ++j) {
                            const u32 rr = 8 * lg + 4 * rt + j;
                            const float x = (float)(rr * sg) * (1.0f / 1024.0f);
                            t1c[rt][ctl][j] = cosr(x); t1s[rt][ctl][j] = sinr(x);
                            const float y = (float)(sg + 32 * rr) * (1.0f / 2048.0f);
                            const float c = cosr(y), s = sinr(y);
                            Pr[rt][ctl][j] = (bfl(wp[j]) + bfl(wm[j])) * sc; Pi[rt][ctl][j] = (bfh(wp[j]) + bfh(wm[j])) * sc;
                            const float mr = (bfl(wp[j]) - bfl(wm[j])) * sc, mi = (bfh(wp[j]) - bfh(wm[j])) * sc;
                            Q1r[rt][ctl][j] = mr * c + mi * s; Q1i[rt][ctl][j] = mi * c - mr * s;
                            Q2r[rt][ctl][j] = mr * c - mi * s; Q2i[rt][ctl][j] = mi * c + mr * s;
                        }
                    }
            }
            {
                auto ctof = [](u32 c) { const u32 r = c >= 320 ? c - 320 : (c >= 160 ? c - 160 : c); const u32 cp = r <= 80 ? r : 160 - r; return cp >= 80 ? 0u : cp >> 4; };
                const u32 ca = ctof(4 * job), cb = job == 80 ? 0u : ctof(4 * job + 3);
                wait2(a.ws + OFF_X1F + (ca * 64 + lane) * 8, a.ws + OFF_X1F + (cb * 64 + lane) * 8, tag);
            }
            STAMP(3);
#pragma unroll
            for (int q = 0; q < 8; ++q) {
                const u32 nq = w * 8 + q;
                const u32 i = lane >> 2, g = nq & 3, pl = (nq >> 2) & 1, rho = (nq >> 3) & 1, e = nq >> 4;
                const u32 v = 2 * sig(rho, i) + e;
                glds<16>(a.ws + OFF_X1 + ((g * 64 + v) * 2 + pl) * X1_PLANE + (4 * job + ((lane & 3) ^ (i >> 2))) * 16, lds + nq * 1024);
            }
            WAITVM(0);
            bar();
            STAMP(4);
            const bool zim = (job == 0 && w == 0) || job == 80;
            f32x4 dr[2][2][2], di[2][2][2];
            const f32x4 z4 = {0.f, 0.f, 0.f, 0.f};
#pragma unroll
            for (int e = 0; e < 2; ++e)
#pragma unroll
                for (int rho = 0; rho < 2; ++rho) {
                    LAS char* X = lds + (e * 2 + rho) * 8192 + lane * 64 + ((w ^ (li >> 2)) << 4);
                    const u32x4 xr = *(LAS u32x4*)X;
                    u32x4 xi = *(LAS u32x4*)(X + 4096);
                    if (zim) xi = u32x4{0u, 0u, 0u, 0u};
#pragma unroll
                    for (int t = 0; t < 2; ++t) {
                        dr[e][rho][t] = mfma16(xi, fs[t], mfma16(xr, fc[t], z4));
                        di[e][rho][t] = mfma16(xr, fn[t], mfma16(xi, fc[t], z4));
                    }
                }
            auto pack2 = [](const f32x4& x, const f32x4& y) { return u32x4{pk(x[0], x[1]), pk(x[2], x[3]), pk(y[0], y[1]), pk(y[2], y[3])}; };
#pragma unroll
            for (int e = 0; e < 2; ++e)
#pragma unroll
                for (int t = 0; t < 2; ++t) {
                    f32x4 ur[2], ui[2];
#pragma unroll
                    for (int rho = 0; rho < 2; ++rho) {
                        ur[rho] = dr[e][rho][t] * t1c[rho][t] + di[e][rho][t] * t1s[rho][t];
                        ui[rho] = di[e][rho][t] * t1c[rho][t] - dr[e][rho][t] * t1s[rho][t];
                    }
                    const u32x4 br = pack2(ur[0], ur[1]), bi_ = pack2(ui[0], ui[1]);
#pragma unroll
                    for (int t2 = 0; t2 < 2; ++t2) {
                        dr[e][t2][t] = mfma16(fs[t2], bi_, mfma16(fc[t2], br, z4));
                        di[e][t2][t] = mfma16(fn[t2], br, mfma16(fc[t2], bi_, z4));
                    }
                }
            f32x4 er[2][2][2], ei[2][2][2];
#pragma unroll
            for (int t = 0; t < 2; ++t) {
                f32x4 vr[2][2], vi[2][2];
#pragma unroll
                for (int t2 = 0; t2 < 2; ++t2) {
                    const f32x4 ar = dr[0][t2][t], ai = di[0][t2][t], br = dr[1][t2][t], bi_ = di[1][t2][t];
                    vr[0][t2] = Pr[t2][t] * ar - Pi[t2][t] * ai + Q1r[t2][t] * br - Q1i[t2][t] * bi_;
                    vi[0][t2] = Pr[t2][t] * ai + Pi[t2][t] * ar + Q1r[t2][t] * bi_ + Q1i[t2][t] * br;
                    vr[1][t2] = Pr[t2][t] * br - Pi[t2][t] * bi_ + Q2r[t2][t] * ar - Q2i[t2][t] * ai;
                    vi[1][t2] = Pr[t2][t] * bi_ + Pi[t2][t] * br + Q2r[t2][t] * ai + Q2i[t2][t] * ar;
                }
#pragma unroll
                for (int e = 0; e < 2; ++e) {
                    const u32x4 ar = pack2(vr[e][0], vr[e][1]), ai = pack2(vi[e][0], vi[e][1]);
#pragma unroll
                    for (int rho = 0; rho < 2; ++rho) {
                        er[e][t][rho] = mfma16(ai, fn[rho], mfma16(ar, fc[rho], z4));
                        ei[e][t][rho] = mfma16(ar, fs[rho], mfma16(ai, fc[rho], z4));
                    }
                }
            }
#pragma unroll
            for (int rho = 0; rho < 2; ++rho) {
#pragma unroll
                for (int e = 0; e < 2; ++e) {
                    f32x4 ur[2], ui[2];
#pragma unroll
                    for (int t = 0; t < 2; ++t) {
                        ur[t] = er[e][t][rho] * t1c[t][rho] - ei[e][t][rho] * t1s[t][rho];
                        ui[t] = ei[e][t][rho] * t1c[t][rho] + er[e][t][rho] * t1s[t][rho];
                    }
                    const u32x4 ar = pack2(ur[0], ur[1]), ai = pack2(ui[0], ui[1]);
#pragma unroll
                    for (int t = 0; t < 2; ++t) {
                        dr[e][rho][t] = mfma16(ai, fn[t], mfma16(ar, fc[t], z4));
                        di[e][rho][t] = mfma16(ar, fs[t], mfma16(ai, fc[t], z4));
                    }
                }
#pragma unroll
                for (int t = 0; t < 2; ++t) {
                    const u32 n1 = sig(t, li), f = (n1 & 3) | (((n1 >> 3) & 1) << 2);
                    LAS char* Z = lds + L_ZPL + 2 * w * ZROW + n1 * 128 + (((2 * lg + rho) ^ f) << 4);
                    *(LAS u32x4*)Z = u32x4{pk(dr[0][rho][t][0], dr[1][rho][t][0]), pk(dr[0][rho][t][1], dr[1][rho][t][1]),
                                           pk(dr[0][rho][t][2], dr[1][rho][t][2]), pk(dr[0][rho][t][3], dr[1][rho][t][3])};
                    *(LAS u32x4*)(Z + ZROW) = u32x4{pk(di[0][rho][t][0], di[1][rho][t][0]), pk(di[0][rho][t][1], di[1][rho][t][1]),
                                                    pk(di[0][rho][t][2], di[1][rho][t][2]), pk(di[0][rho][t][3], di[1][rho][t][3])};
                }
            }
            bar();
            STAMP(5);
            {
                const u32 r = li >> 2, cq = li & 3;
#pragma unroll
                for (int q = 0; q < 8; ++q) {
                    const u32 n1 = 8 * w + q, f = (n1 & 3) | (((n1 >> 3) & 1) << 2);
                    LAS char* Z = lds + L_ZPL + r * ZROW + n1 * 128 + (((2 * lg + (cq >> 1)) ^ f) << 4) + (cq & 1) * 8;
                    const u32x2 v0 = trd(Z), v1 = trd(Z + 4 * ZROW);
                    __builtin_amdgcn_raw_buffer_store_b128(u32x4{v0.x, v0.y, v1.x, v1.y}, rs,
                        OFF_Z + ((4 * n1 + lg) * 21 + (job >> 2)) * 1024 + (job & 3) * 256 + li * 16, 0, 16);
                }
                WAITVM(0);
                if (lane == 0) stflag(a.ws + OFF_ZFL + (w * 128 + job) * 8, tag);
            }
            STAMP(6);
        } else if (b == 8 * 20) {
            wait1(a.ws + OFF_ARR + tid * 8, tag);
            bar();
            if (tid == 0) stflag(a.ws + OFF_E, tag);
        }
    }

    {
        u32x4 cf[10][5];
#pragma unroll
        for (int k = 0; k < 5; ++k)
#pragma unroll
            for (int dt = 0; dt < 5; ++dt) {
                const u32 o = (d0 + 16 * dt + li) * 640 + 32 * (5 * w + k) + 8 * lg;
                cf[2 * k][dt] = *(const GAS u32x4*)(a.cre + o);
                cf[2 * k + 1][dt] = *(const GAS u32x4*)(a.cim + o);
            }
        u32x2 xs[5], dv[5];
        const u32 tow = 64 * mcc + 16 * w + li;
#pragma unroll
        for (int dt = 0; dt < 5; ++dt) {
            xs[dt] = *(const GAS u32x2*)(a.x + tow * 640 + d0 + 16 * dt + 4 * lg);
            dv[dt] = *(const GAS u32x2*)(a.dsk + d0 + 16 * dt + 4 * lg);
        }
        float gv[4], og = 0.f;
        {
            u32x4 gr[4];
            for (u32 s = 0; s < SPIN_CAP; ++s) {
 asm volatile("" ::: "memory");
                bool ok = true;
#pragma unroll
                for (int tt = 0; tt < 4; ++tt) {
                    gr[tt] = __builtin_amdgcn_raw_buffer_load_b128(rs, OFF_GATE + (64 * mcc + 16 * tt + li) * 16, 0, 16);
                    ok = ok && gr[tt].y == tlo && gr[tt].w == thi;
                }
                if (__all(ok)) break;
            }
#pragma unroll
            for (int tt = 0; tt < 4; ++tt) { gv[tt] = asf(gr[tt].x); if (tt == (int)w) og = asf(gr[tt].z); }
        }
        u32x4 gf[5][5];
#pragma unroll
        for (int dt = 0; dt < 5; ++dt) {
            const float st = (float)(d0 + 16 * dt + li) * (1.0f / 640.0f);
#pragma unroll
            for (int k = 0; k < 5; ++k)
#pragma unroll
                for (int m = 0; m < 4; ++m) {
                    const float x = __builtin_amdgcn_fractf((float)(16 * (5 * w + k) + 4 * lg + m) * st);
                    gf[k][dt][m] = pk(cosr(x), -sinr(x));
                }
        }
        STAMP(7);
        wait1(a.ws + OFF_ZFL + ((mcc >> 3) * 128 + (lane < 20 ? 20 * w + lane : 80)) * 8, tag);
        STAMP(8);
        f32x4 acc[4][5] = {};
        {
            u32x4 zf[4][5];
#pragma unroll
            for (int tt = 0; tt < 4; ++tt)
#pragma unroll
                for (int k = 0; k < 5; ++k)
                    zf[tt][k] = __builtin_amdgcn_raw_buffer_load_b128(rs, OFF_Z + ((4 * mcc + tt) * 21 + 5 * w + k) * 1024 + lane * 16, 0, 16);
#pragma unroll
            for (int tt = 0; tt < 4; ++tt) {
#pragma unroll
                for (int k = 0; k < 5; ++k)
#pragma unroll
                    for (int dt = 0; dt < 5; ++dt) acc[tt][dt] = mfma16(gf[k][dt], zf[tt][k], acc[tt][dt]);
#pragma unroll
                for (int dt = 0; dt < 5; ++dt) acc[tt][dt] *= gv[tt];
            }
        }
        const u32 z320 = __builtin_amdgcn_raw_buffer_load_b32(rs, OFF_Z + ((4 * mcc + w) * 21 + 20) * 1024 + li * 16, 0, 16);
        STAMP(9);
        wait1(a.ws + OFF_HFL + (mcc * 32 + 5 * w + (lane < 5 ? lane : 0)) * 8, tag);
        STAMP(10);
        LAS f32x4* R = (LAS f32x4*)(lds + L_RED);
        {
            u32x4 hf[4][10];
#pragma unroll
            for (int tt = 0; tt < 4; ++tt)
#pragma unroll
                for (int k = 0; k < 10; ++k)
                    hf[tt][k] = __builtin_amdgcn_raw_buffer_load_b128(rs, OFF_H + ((4 * mcc + tt) * 40 + 10 * w + k) * 1024 + lane * 16, 0, 16);
#pragma unroll
            for (int tt = 0; tt < 4; ++tt) {
#pragma unroll
                for (int k = 0; k < 10; ++k)
#pragma unroll
                    for (int dt = 0; dt < 5; ++dt) acc[tt][dt] = mfma16(cf[k][dt], hf[tt][k], acc[tt][dt]);
                if (tt != (int)w) {
                    const u32 slot = (w - tt - 1) & 3;
#pragma unroll
                    for (int dt = 0; dt < 5; ++dt) R[((tt * 3 + slot) * 5 + dt) * 64 + lane] = acc[tt][dt];
                }
            }
        }
        bar();
        STAMP(11);
        f32x4 mine[5];
#pragma unroll
        for (int dt = 0; dt < 5; ++dt) mine[dt] = w == 0 ? acc[0][dt] : (w == 1 ? acc[1][dt] : (w == 2 ? acc[2][dt] : acc[3][dt]));
        const float gm = w == 0 ? gv[0] : (w == 1 ? gv[1] : (w == 2 ? gv[2] : gv[3]));
        const float zz = gm * bfl(z320);
#pragma unroll
        for (int dt = 0; dt < 5; ++dt) {
            f32x4 v = mine[dt] + R[((w * 3 + 0) * 5 + dt) * 64 + lane] + R[((w * 3 + 1) * 5 + dt) * 64 + lane] + R[((w * 3 + 2) * 5 + dt) * 64 + lane];
            v[0] += og * rbf(bfl(xs[dt].x) * bfl(dv[dt].x)) + zz;
            v[1] += og * rbf(bfh(xs[dt].x) * bfh(dv[dt].x)) - zz;
            v[2] += og * rbf(bfl(xs[dt].y) * bfl(dv[dt].y)) + zz;
            v[3] += og * rbf(bfh(xs[dt].y) * bfh(dv[dt].y)) - zz;
            *(GAS f32x4*)(a.out + tow * 640 + d0 + 16 * dt + 4 * lg) = v;
        }
        STAMP(12);
    }
}

extern "C" void kernel_launch(void* const* d_in, const int* in_sizes, int n_in, void* d_out, int out_size, void* d_ws,
                              size_t ws_size, hipStream_t stream) {
    Args a;
    a.x = (const u16*)d_in[0]; a.wr = (const u16*)d_in[1]; a.wi = (const u16*)d_in[2];
    a.lre = (const u16*)d_in[4]; a.lim = (const u16*)d_in[5]; a.bre = (const u16*)d_in[6]; a.bim = (const u16*)d_in[7];
    a.cre = (const u16*)d_in[8]; a.cim = (const u16*)d_in[9]; a.ldt = (const u16*)d_in[10]; a.dsk = (const u16*)d_in[11];
    a.gw = (const u16*)d_in[12];
    a.out = (float*)d_out; a.ws = (char*)d_ws;
    gmix_kernel<<<256, 256, 0, stream>>>(a);
}
```

```cpp
#include <hip/hip_runtime.h>

struct Desc { const void* p; unsigned n; unsigned f; };
struct Args { Desc d[14]; };

__global__ __launch_bounds__(256, 1) void gmix_kernel(Args a) {
    __shared__ __attribute__((aligned(1024))) char smem[162304];
    auto ka = __builtin_amdgcn_kernarg_segment_ptr();
    unsigned bid = blockIdx.x, tid = threadIdx.x;
    unsigned lb = (unsigned)(size_t)(__attribute__((address_space(3))) char*)smem;
    asm volatile(
        "s_load_dwordx16 s[8:23], s[0:1], 0\n"
        "s_load_dwordx16 s[24:39], s[0:1], 64\n"
        "s_load_dwordx16 s[40:55], s[0:1], 128\n"
        "s_load_dwordx8 s[56:63], s[0:1], 192\n"
        "v_lshrrev_b32 v1, 6, v0\n"
        "s_nop 0\n"
        "v_readfirstlane_b32 s3, v1\n"
        "v_and_b32 v0, 63, v0\n"
        "v_and_b32 v1, 15, v0\n"
        "v_lshrrev_b32 v2, 4, v0\n"
        "v_lshlrev_b32 v3, 4, v0\n"
        "v_mov_b32 v12, v3\n"
        "v_add_u32 v13, 0x1000, v3\n"
        "v_add_u32 v14, 0x2000, v3\n"
        "s_and_b32 s4, s2, 7\n"
        "s_lshr_b32 s5, s2, 3\n"
        "s_getpc_b64 s[78:79]\n"
        "PC_%=:\n"
        "s_lshl_b32 s70, s5, 2\n"
        "s_add_u32 s70, s70, s3\n"
        "s_lshl_b32 s70, s70, 10\n"
        "v_add_u32 v4, s70, v3\n"
        "v_min_u32 v4, END_%=-PC_%=-16, v4\n"
        "global_load_dwordx4 a[192:195], v4, s[78:79]\n"
        "s_lshr_b32 s70, s5, 3\n"
        "s_lshl_b32 s66, s4, 2\n"
        "s_add_u32 s66, s66, s70\n"
        "s_and_b32 s70, s5, 7\n"
        "s_mul_i32 s69, s70, 80\n"
        "s_lshl_b32 s70, s70, 2\n"
        "s_add_u32 s70, s70, s3\n"
        "s_mul_i32 s67, s70, 51200\n"
        "s_add_u32 s67, s67, 0xb80000\n"
        "s_cmp_ge_u32 s5, 30\n"
        "s_cbranch_scc1 PACK_%=\n"
        "s_cmp_ge_u32 s5, 20\n"
        "s_cbranch_scc1 DFT_%=\n"
        "s_mul_i32 s70, s5, 205\n"
        "s_lshr_b32 s70, s70, 10\n"
        "s_lshr_b32 s86, s4, 1\n"
        "s_lshl_b32 s86, s86, 2\n"
        "s_add_u32 s86, s86, s70\n"
        "s_mul_i32 s90, s70, 3\n"
        "s_mul_i32 s70, s70, 5\n"
        "s_sub_u32 s87, s5, s70\n"
        "s_lshl_b32 s70, s87, 1\n"
        "s_add_u32 s90, s90, s70\n"
        "s_cmp_ge_u32 s90, 10\n"
        "s_cselect_b32 s70, 10, 0\n"
        "s_sub_u32 s90, s90, s70\n"
        "s_lshl_b32 s90, s90, 7\n"
        "s_and_b32 s70, s4, 1\n"
        "s_mul_i32 s70, s70, 5\n"
        "s_add_u32 s87, s87, s70\n"
        "s_lshr_b32 s88, s3, 1\n"
        "s_and_b32 s89, s3, 1\n"
        "v_and_b32 v252, 31, v0\n"
        "v_lshrrev_b32 v253, 5, v0\n"
        "s_lshl_b32 s70, s86, 7\n"
        "s_lshl_b32 s71, s88, 6\n"
        "s_add_u32 s70, s70, s71\n"
        "s_lshl_b32 s71, s89, 3\n"
        "s_add_u32 s70, s70, s71\n"
        "v_bfe_u32 v250, v0, 3, 1\n"
        "v_lshlrev_b32 v250, 4, v250\n"
        "v_bfe_u32 v251, v0, 4, 1\n"
        "v_add3_u32 v250, v250, v251, s70\n"
        "v_lshl_add_u32 v250, v253, 5, v250\n"
        "v_and_b32 v251, 7, v0\n"
        "v_xor_b32 v251, v251, v2\n"
        "v_lshlrev_b32 v251, 4, v251\n"
        "v_xor_b32 v254, 64, v251\n"
        "v_mul_u32_u24 v250, 1280, v250\n"
        "v_add_u32 v4, v250, v251\n"
        "v_add_u32 v5, v250, v254\n"
        "v_add_u32 v5, 1536, v5\n"
        "v_add_u32 v6, v250, v251\n"
        "v_add_u32 v6, 3072, v6\n"
        "v_add_u32 v7, v250, v254\n"
        "v_add_u32 v7, 4608, v7\n"
        "s_lshl_b32 s70, s87, 6\n"
        "s_lshl_b32 s71, s88, 5\n"
        "s_add_u32 s71, s70, s71\n"
        "v_lshrrev_b32 v250, 3, v0\n"
        "v_add_u32 v250, s71, v250\n"
        "v_mul_u32_u24 v250, 1280, v250\n"
        "v_add_u32 v8, v250, v251\n"
        "v_add_u32 v9, v250, v254\n"
        "v_add_u32 v9, 9216, v9\n"
        "v_add_u32 v10, v250, v251\n"
        "v_add_u32 v10, 18432, v10\n"
        "v_add_u32 v11, v250, v254\n"
        "v_add_u32 v11, 27648, v11\n"
        "s_lshl_b32 s71, s89, 5\n"
        "s_add_u32 s70, s70, s71\n"
        "v_add_u32 v255, s70, v252\n"
        "v_lshlrev_b32 v250, 1, v255\n"
        "s_lshl_b32 s91, s3, 12\n"
        "s_cmp_eq_u32 s89, 0\n"
        "s_waitcnt lgkmcnt(0)\n"
        "s_cselect_b64 s[80:81], s[20:21], s[24:25]\n"
        "s_cselect_b64 s[82:83], s[22:23], s[26:27]\n"
        "s_load_dwordx2 s[64:65], s[40:41], 0\n"
        "s_add_u32 m0, s91, 0\n"
        "s_nop 0\n"
        "buffer_load_dwordx4 v4, s[8:11], s90 offen offset:0 lds\n"
        "buffer_load_dwordx4 v5, s[8:11], s90 offen offset:1024 lds\n"
        "buffer_load_dwordx4 v6, s[8:11], s90 offen offset:2048 lds\n"
        "buffer_load_dwordx4 v7, s[8:11], s90 offen offset:3072 lds\n"
        "s_add_u32 m0, s91, 16384\n"
        "s_nop 0\n"
        "buffer_load_dwordx4 v8, s[80:83], s90 offen offset:0 lds\n"
        "buffer_load_dwordx4 v9, s[80:83], s90 offen offset:1024 lds\n"
        "buffer_load_dwordx4 v10, s[80:83], s90 offen offset:2048 lds\n"
        "buffer_load_dwordx4 v11, s[80:83], s90 offen offset:3072 lds\n"
        "buffer_load_ushort v160, v250, s[44:47], 0 offen\n"
        "buffer_load_ushort v161, v250, s[48:51], 0 offen\n"
        "buffer_load_ushort v162, v250, s[52:55], 0 offen\n"
        "s_add_u32 s90, s90, 128\n"
        "s_cmp_eq_u32 s90, 1280\n"
        "s_cselect_b32 s90, 0, s90\n"
        "s_add_u32 m0, s91, 32768\n"
        "s_nop 0\n"
        "buffer_load_dwordx4 v4, s[8:11], s90 offen offset:0 lds\n"
        "buffer_load_dwordx4 v5, s[8:11], s90 offen offset:1024 lds\n"
        "buffer_load_dwordx4 v6, s[8:11], s90 offen offset:2048 lds\n"
        "buffer_load_dwordx4 v7, s[8:11], s90 offen offset:3072 lds\n"
        "s_add_u32 m0, s91, 49152\n"
        "s_nop 0\n"
        "buffer_load_dwordx4 v8, s[80:83], s90 offen offset:0 lds\n"
        "buffer_load_dwordx4 v9, s[80:83], s90 offen offset:1024 lds\n"
        "buffer_load_dwordx4 v10, s[80:83], s90 offen offset:2048 lds\n"
        "buffer_load_dwordx4 v11, s[80:83], s90 offen offset:3072 lds\n"
        "v_bfe_u32 v250, v252, 1, 3\n"
        "v_xor_b32 v250, v250, v253\n"
        "v_lshlrev_b32 v250, 4, v250\n"
        "v_lshl_add_u32 v250, v252, 7, v250\n"
        "s_lshl_b32 s70, s88, 13\n"
        "s_lshl_b32 s71, s89, 13\n"
        "s_add_u32 s71, s71, 16384\n"
        "v_xor_b32 v251, 0, v250\n"
        "v_add_u32 v144, s70, v251\n"
        "v_add_u32 v152, s71, v251\n"
        "v_add_u32 v148, 0x10000, v144\n"
        "v_add_u32 v156, 0x10000, v152\n"
        "v_xor_b32 v251, 32, v250\n"
        "v_add_u32 v145, s70, v251\n"
        "v_add_u32 v153, s71, v251\n"
        "v_add_u32 v149, 0x10000, v145\n"
        "v_add_u32 v157, 0x10000, v153\n"
        "v_xor_b32 v251, 64, v250\n"
        "v_add_u32 v146, s70, v251\n"
        "v_add_u32 v154, s71, v251\n"
        "v_add_u32 v150, 0x10000, v146\n"
        "v_add_u32 v158, 0x10000, v154\n"
        "v_xor_b32 v251, 96, v250\n"
        "v_add_u32 v147, s70, v251\n"
        "v_add_u32 v155, s71, v251\n"
        "v_add_u32 v151, 0x10000, v147\n"
        "v_add_u32 v159, 0x10000, v155\n"
        "s_mov_b32 s74, 0\n"
        "s_mov_b32 s75, -1\n"
        "s_sub_u32 s76, 0, s88\n"
        "s_mov_b32 s77, s76\n"
        "s_add_u32 s90, s90, 128\n"
        "s_cmp_eq_u32 s90, 1280\n"
        "s_cselect_b32 s90, 0, s90\n"
        "s_add_u32 m0, s91, 65536\n"
        "s_nop 0\n"
        "buffer_load_dwordx4 v4, s[8:11], s90 offen offset:0 lds\n"
        "buffer_load_dwordx4 v5, s[8:11], s90 offen offset:1024 lds\n"
        "buffer_load_dwordx4 v6, s[8:11], s90 offen offset:2048 lds\n"
        "buffer_load_dwordx4 v7, s[8:11], s90 offen offset:3072 lds\n"
        "s_add_u32 m0, s91, 81920\n"
        "s_nop 0\n"
        "buffer_load_dwordx4 v8, s[80:83], s90 offen offset:0 lds\n"
        "buffer_load_dwordx4 v9, s[80:83], s90 offen offset:1024 lds\n"
        "buffer_load_dwordx4 v10, s[80:83], s90 offen offset:2048 lds\n"
        "buffer_load_dwordx4 v11, s[80:83], s90 offen offset:3072 lds\n"
        "s_waitcnt lgkmcnt(0)\n"
        "s_mul_hi_u32 s70, s64, 0x4c957f2d\n"
        "s_mul_i32 s71, s64, 0x5851f42d\n"
        "s_mul_i32 s72, s65, 0x4c957f2d\n"
        "s_mul_i32 s64, s64, 0x4c957f2d\n"
        "s_add_u32 s70, s70, s71\n"
        "s_add_u32 s70, s70, s72\n"
        "s_add_u32 s64, s64, 1\n"
        "s_addc_u32 s65, s70, 0\n"
        "s_waitcnt vmcnt(19)\n"
        "s_barrier\n"
        "ds_read_b128 v[80:83], v144 offset:0\n"
        "ds_read_b128 v[88:91], v152 offset:0\n"
        "ds_read_b128 v[92:95], v152 offset:4096\n"
        "ds_read_b128 v[84:87], v144 offset:4096\n"
        "ds_read_b128 v[96:99], v145 offset:0\n"
        "ds_read_b128 v[104:107], v153 offset:0\n"
        "ds_read_b128 v[108:111], v153 offset:4096\n"
        "ds_read_b128 v[100:103], v145 offset:4096\n"
        "ds_read_b128 v[112:115], v146 offset:0\n"
        "ds_read_b128 v[120:123], v154 offset:0\n"
        "ds_read_b128 v[124:127], v154 offset:4096\n"
        "ds_read_b128 v[116:119], v146 offset:4096\n"
        "ds_read_b128 v[128:131], v147 offset:0\n"
        "ds_read_b128 v[136:139], v155 offset:0\n"
        "ds_read_b128 v[140:143], v155 offset:4096\n"
        "ds_read_b128 v[132:135], v147 offset:4096\n"
        "s_add_u32 s90, s90, 128\n"
        "s_cmp_eq_u32 s90, 1280\n"
        "s_cselect_b32 s90, 0, s90\n"
        "s_add_u32 m0, s91, 98304\n"
        "s_nop 0\n"
        "buffer_load_dwordx4 v4, s[8:11], s90 offen offset:0 lds\n"
        "buffer_load_dwordx4 v5, s[8:11], s90 offen offset:1024 lds\n"
        "buffer_load_dwordx4 v6, s[8:11], s90 offen offset:2048 lds\n"
        "buffer_load_dwordx4 v7, s[8:11], s90 offen offset:3072 lds\n"
        "s_add_u32 m0, s91, 114688\n"
        "s_nop 0\n"
        "buffer_load_dwordx4 v8, s[80:83], s90 offen offset:0 lds\n"
        "buffer_load_dwordx4 v9, s[80:83], s90 offen offset:1024 lds\n"
        "buffer_load_dwordx4 v10, s[80:83], s90 offen offset:2048 lds\n"
        "buffer_load_dwordx4 v11, s[80:83], s90 offen offset:3072 lds\n"
        "s_lshl_b32 s78, s3, 24\n"
        "v_lshlrev_b32 v184, 18, v0\n"
        "buffer_load_dword a196, v184, s[40:43], s78 offen\n"
        "buffer_load_dword a197, v184, s[36:39], s78 offen\n"
        "s_lshl_b32 s70, s2, 7\n"
        "s_add_u32 s70, s70, 11534336\n"
        "s_cmp_eq_u32 s3, 0\n"
        "s_cselect_b32 s70, s70, 0x40000000\n"
        "v_mov_b32 v184, s64\n"
        "v_mov_b32 v185, s65\n"
        "v_cmp_eq_u32 vcc, 0, v0\n"
        "v_mov_b32 v186, 0x40000000\n"
        "s_nop 0\n"
        "v_cndmask_b32 v186, v186, v0, vcc\n"
        "buffer_store_dwordx2 v[184:185], v186, s[40:43], s70 offen sc1\n"
        "s_waitcnt vmcnt(19)\n"
        "s_waitcnt lgkmcnt(0)\n"
        "s_barrier\n"
        "v_mfma_f32_32x32x16_bf16 v[16:31], v[80:83], v[88:91], 0\n"
        "s_add_u32 s90, s90, 128\n"
        "s_cmp_eq_u32 s90, 1280\n"
        "s_cselect_b32 s90, 0, s90\n"
        "s_add_u32 m0, s91, 0\n"
        "s_nop 0\n"
        "buffer_load_dwordx4 v4, s[8:11], s90 offen offset:0 lds\n"
        "v_mfma_f32_32x32x16_bf16 v[32:47], v[80:83], v[92:95], 0\n"
        "ds_read_b128 a[64:67], v144 offset:32768\n"
        "ds_read_b128 a[72:75], v152 offset:32768\n"
        "ds_read_b128 a[76:79], v152 offset:36864\n"
        "ds_read_b128 a[68:71], v144 offset:36864\n"
        "ds_read_b128 a[80:83], v145 offset:32768\n"
        "ds_read_b128 a[88:91], v153 offset:32768\n"
        "v_mfma_f32_32x32x16_bf16 v[48:63], v[84:87], v[88:91], 0\n"
        "buffer_load_dwordx4 v5, s[8:11], s90 offen offset:1024 lds\n"
        "ds_read_b128 a[92:95], v153 offset:36864\n"
        "ds_read_b128 a[84:87], v145 offset:36864\n"
        "v_mfma_f32_32x32x16_bf16 v[64:79], v[84:87], v[92:95], 0\n"
        "ds_read_b128 a[96:99], v146 offset:32768\n"
        "ds_read_b128 a[104:107], v154 offset:32768\n"
        "ds_read_b128 a[108:111], v154 offset:36864\n"
        "ds_read_b128 a[100:103], v146 offset:36864\n"
        "buffer_load_dwordx4 v6, s[8:11], s90 offen offset:2048 lds\n"
        "v_mfma_f32_32x32x16_bf16 v[16:31], v[96:99], v[104:107], v[16:31]\n"
        "ds_read_b128 a[112:115], v147 offset:32768\n"
        "ds_read_b128 a[120:123], v155 offset:32768\n"
        "ds_read_b128 a[124:127], v155 offset:36864\n"
        "ds_read_b128 a[116:119], v147 offset:36864\n"
        "v_mfma_f32_32x32x16_bf16 v[32:47], v[96:99], v[108:111], v[32:47]\n"
        "buffer_load_dwordx4 v7, s[8:11], s90 offen offset:3072 lds\n"
        "s_add_u32 m0, s91, 16384\n"
        "v_mfma_f32_32x32x16_bf16 v[48:63], v[100:103], v[104:107], v[48:63]\n"
        "buffer_load_dwordx4 v8, s[80:83], s90 offen offset:0 lds\n"
        "v_mfma_f32_32x32x16_bf16 v[64:79], v[100:103], v[108:111], v[64:79]\n"
        "v_mfma_f32_32x32x16_bf16 v[16:31], v[112:115], v[120:123], v[16:31]\n"
        "buffer_load_dwordx4 v9, s[80:83], s90 offen offset:1024 lds\n"
        "v_mfma_f32_32x32x16_bf16 v[32:47], v[112:115], v[124:127], v[32:47]\n"
        "buffer_load_dwordx4 v10, s[80:83], s90 offen offset:2048 lds\n"
        "v_mfma_f32_32x32x16_bf16 v[48:63], v[116:119], v[120:123], v[48:63]\n"
        "v_mfma_f32_32x32x16_bf16 v[64:79], v[116:119], v[124:127], v[64:79]\n"
        "buffer_load_dwordx4 v11, s[80:83], s90 offen offset:3072 lds\n"
        "s_waitcnt vmcnt(19)\n"
        "s_waitcnt lgkmcnt(0)\n"
        "s_barrier\n"
        "v_mfma_f32_32x32x16_bf16 v[16:31], v[128:131], v[136:139], v[16:31]\n"
        "s_add_u32 s90, s90, 128\n"
        "s_cmp_eq_u32 s90, 1280\n"
        "s_cselect_b32 s90, 0, s90\n"
        "s_add_u32 m0, s91, 32768\n"
        "s_nop 0\n"
        "buffer_load_dwordx4 v4, s[8:11], s90 offen offset:0 lds\n"
        "v_mfma_f32_32x32x16_bf16 v[32:47], v[128:131], v[140:143], v[32:47]\n"
        "v_lshlrev_b32 v162, 16, v162\n"
        "v_lshlrev_b32 v161, 16, v161\n"
        "v_mul_f32 v162, 0x3fb8aa3b, v162\n"
        "v_lshlrev_b32 v160, 16, v160\n"
        "v_exp_f32 v162, v162\n"
        "v_mfma_f32_32x32x16_bf16 v[48:63], v[132:135], v[136:139], v[48:63]\n"
        "buffer_load_dwordx4 v5, s[8:11], s90 offen offset:1024 lds\n"
        "v_cvt_pk_bf16_f32 v162, v162, v162\n"
        "ds_read_b128 v[80:83], v148 offset:0\n"
        "v_mfma_f32_32x32x16_bf16 v[64:79], v[132:135], v[140:143], v[64:79]\n"
        "v_lshlrev_b32 v162, 16, v162\n"
        "ds_read_b128 v[88:91], v156 offset:0\n"
        "v_mul_f32 v251, v162, v161\n"
        "v_mul_f32 v250, v162, v160\n"
        "buffer_load_dwordx4 v6, s[8:11], s90 offen offset:2048 lds\n"
        "v_mfma_f32_32x32x16_bf16 v[16:31], a[64:67], a[72:75], v[16:31]\n"
        "v_mul_f32 v251, 0x3e22f983, v251\n"
        "v_mul_f32 v250, 0x3fb8aa3b, v250\n"
        "v_rndne_f32 v254, v251\n"
        "v_exp_f32 v250, v250\n"
        "v_sub_f32 v251, v251, v254\n"
        "v_mfma_f32_32x32x16_bf16 v[32:47], a[64:67], a[76:79], v[32:47]\n"
        "buffer_load_dwordx4 v7, s[8:11], s90 offen offset:3072 lds\n"
        "s_add_u32 m0, s91, 49152\n"
        "v_cos_f32 v164, v251\n"
        "v_mfma_f32_32x32x16_bf16 v[48:63], a[68:71], a[72:75], v[48:63]\n"
        "v_sin_f32 v165, v251\n"
        "v_mul_f32 v164, v250, v164\n"
        "buffer_load_dwordx4 v8, s[80:83], s90 offen offset:0 lds\n"
        "v_mfma_f32_32x32x16_bf16 v[64:79], a[68:71], a[76:79], v[64:79]\n"
        "v_mul_f32 v165, v250, v165\n"
        "v_mul_f32 v250, v160, v160\n"
        "v_add_f32 v251, -1.0, v164\n"
        "v_fmac_f32 v250, v161, v161\n"
        "ds_read_b128 v[92:95], v156 offset:4096\n"
        "v_rcp_f32 v250, v250\n"
        "v_mfma_f32_32x32x16_bf16 v[16:31], a[80:83], a[88:91], v[16:31]\n"
        "buffer_load_dwordx4 v9, s[80:83], s90 offen offset:1024 lds\n"
        "ds_read_b128 v[84:87], v148 offset:4096\n"
        "ds_read_b128 v[96:99], v149 offset:0\n"
        "v_mfma_f32_32x32x16_bf16 v[32:47], a[80:83], a[92:95], v[32:47]\n"
        "ds_read_b128 v[104:107], v157 offset:0\n"
        "v_mul_f32 v166, v251, v160\n"
        "v_mul_f32 v167, v165, v160\n"
        "ds_read_b128 v[108:111], v157 offset:4096\n"
        "buffer_load_dwordx4 v10, s[80:83], s90 offen offset:2048 lds\n"
        "v_mfma_f32_32x32x16_bf16 v[48:63], a[84:87], a[88:91], v[48:63]\n"
        "ds_read_b128 v[100:103], v149 offset:4096\n"
        "ds_read_b128 v[112:115], v150 offset:0\n"
        "ds_read_b128 v[120:123], v158 offset:0\n"
        "ds_read_b128 v[124:127], v158 offset:4096\n"
        "v_add_f32 v250, v250, v250\n"
        "v_fmac_f32 v166, v165, v161\n"
        "v_mfma_f32_32x32x16_bf16 v[64:79], a[84:87], a[92:95], v[64:79]\n"
        "buffer_load_dwordx4 v11, s[80:83], s90 offen offset:3072 lds\n"
        "v_fma_f32 v167, -v251, v161, v167\n"
        "ds_read_b128 v[116:119], v150 offset:4096\n"
        "v_mfma_f32_32x32x16_bf16 v[16:31], a[96:99], a[104:107], v[16:31]\n"
        "ds_read_b128 v[128:131], v151 offset:0\n"
        "ds_read_b128 v[136:139], v159 offset:0\n"
        "ds_read_b128 v[140:143], v159 offset:4096\n"
        "ds_read_b128 v[132:135], v151 offset:4096\n"
        "v_mul_f32 v166, v250, v166\n"
        "v_mul_f32 v167, v250, v167\n"
        "v_mfma_f32_32x32x16_bf16 v[32:47], a[96:99], a[108:111], v[32:47]\n"
        "v_mfma_f32_32x32x16_bf16 v[48:63], a[100:103], a[104:107], v[48:63]\n"
        "v_mfma_f32_32x32x16_bf16 v[64:79], a[100:103], a[108:111], v[64:79]\n"
        "s_waitcnt vmcnt(19)\n"
        "s_waitcnt lgkmcnt(0)\n"
        "s_barrier\n"
        "v_mfma_f32_32x32x16_bf16 v[16:31], a[112:115], a[120:123], v[16:31]\n"
        "s_add_u32 s90, s90, 128\n"
        "s_cmp_eq_u32 s90, 1280\n"
        "s_cselect_b32 s90, 0, s90\n"
        "s_add_u32 m0, s91, 65536\n"
        "s_nop 0\n"
        "buffer_load_dwordx4 v4, s[8:11], s90 offen offset:0 lds\n"
        "v_mfma_f32_32x32x16_bf16 v[32:47], a[112:115], a[124:127], v[32:47]\n"
        "v_mul_f32 v180, v165, v165\n"
        "v_add_f32 v181, v164, v164\n"
        "v_fma_f32 v180, v164, v164, -v180\n"
        "v_mul_f32 v181, v181, v165\n"
        "v_add_f32 v183, v180, v180\n"
        "v_mul_f32 v182, v181, v181\n"
        "v_mfma_f32_32x32x16_bf16 v[48:63], a[116:119], a[120:123], v[48:63]\n"
        "buffer_load_dwordx4 v5, s[8:11], s90 offen offset:1024 lds\n"
        "v_fma_f32 v182, v180, v180, -v182\n"
        "v_mul_f32 v183, v183, v181\n"
        "v_mfma_f32_32x32x16_bf16 v[64:79], a[116:119], a[124:127], v[64:79]\n"
        "v_mul_f32 v180, v183, v183\n"
        "v_add_f32 v181, v182, v182\n"
        "v_fma_f32 v180, v182, v182, -v180\n"
        "v_mul_f32 v181, v181, v183\n"
        "buffer_load_dwordx4 v6, s[8:11], s90 offen offset:2048 lds\n"
        "v_mfma_f32_32x32x16_bf16 v[16:31], v[80:83], v[88:91], v[16:31]\n"
        "v_mul_f32 v168, v181, v181\n"
        "v_add_f32 v169, v180, v180\n"
        "v_fma_f32 v168, v180, v180, -v168\n"
        "v_mul_f32 v169, v169, v181\n"
        "v_add_f32 v171, v168, v168\n"
        "v_mul_f32 v170, v169, v169\n"
        "v_mfma_f32_32x32x16_bf16 v[32:47], v[80:83], v[92:95], v[32:47]\n"
        "buffer_load_dwordx4 v7, s[8:11], s90 offen offset:3072 lds\n"
        "s_add_u32 m0, s91, 81920\n"
        "v_fma_f32 v170, v168, v168, -v170\n"
        "v_mfma_f32_32x32x16_bf16 v[48:63], v[84:87], v[88:91], v[48:63]\n"
        "v_mul_f32 v171, v171, v169\n"
        "v_add_f32 v173, v170, v170\n"
        "v_mul_f32 v172, v171, v171\n"
        "v_mul_f32 v173, v173, v171\n"
        "buffer_load_dwordx4 v8, s[80:83], s90 offen offset:0 lds\n"
        "v_mfma_f32_32x32x16_bf16 v[64:79], v[84:87], v[92:95], v[64:79]\n"
        "v_fma_f32 v172, v170, v170, -v172\n"
        "v_mul_f32 v174, v173, v173\n"
        "v_add_f32 v175, v172, v172\n"
        "v_fma_f32 v174, v172, v172, -v174\n"
        "v_mul_f32 v175, v175, v173\n"
        "ds_read_b128 a[64:67], v148 offset:32768\n"
        "v_mfma_f32_32x32x16_bf16 v[16:31], v[96:99], v[104:107], v[16:31]\n"
        "buffer_load_dwordx4 v9, s[80:83], s90 offen offset:1024 lds\n"
        "v_mul_f32 v176, v175, v175\n"
        "v_add_f32 v177, v174, v174\n"
        "v_mfma_f32_32x32x16_bf16 v[32:47], v[96:99], v[108:111], v[32:47]\n"
        "ds_read_b128 a[72:75], v156 offset:32768\n"
        "ds_read_b128 a[76:79], v156 offset:36864\n"
        "ds_read_b128 a[68:71], v148 offset:36864\n"
        "ds_read_b128 a[80:83], v149 offset:32768\n"
        "buffer_load_dwordx4 v10, s[80:83], s90 offen offset:2048 lds\n"
        "v_mfma_f32_32x32x16_bf16 v[48:63], v[100:103], v[104:107], v[48:63]\n"
        "ds_read_b128 a[88:91], v157 offset:32768\n"
        "v_fma_f32 v176, v174, v174, -v176\n"
        "v_mul_f32 v177, v177, v175\n"
        "ds_read_b128 a[92:95], v157 offset:36864\n"
        "ds_read_b128 a[84:87], v149 offset:36864\n"
        "ds_read_b128 a[96:99], v150 offset:32768\n"
        "v_mfma_f32_32x32x16_bf16 v[64:79], v[100:103], v[108:111], v[64:79]\n"
        "buffer_load_dwordx4 v11, s[80:83], s90 offen offset:3072 lds\n"
        "ds_read_b128 a[104:107], v158 offset:32768\n"
        "ds_read_b128 a[108:111], v158 offset:36864\n"
        "v_mfma_f32_32x32x16_bf16 v[16:31], v[112:115], v[120:123], v[16:31]\n"
        "v_mul_f32 v178, v177, v177\n"
        "v_add_f32 v179, v176, v176\n"
        "ds_read_b128 a[100:103], v150 offset:36864\n"
        "ds_read_b128 a[112:115], v151 offset:32768\n"
        "ds_read_b128 a[120:123], v159 offset:32768\n"
        "ds_read_b128 a[124:127], v159 offset:36864\n"
        "v_mfma_f32_32x32x16_bf16 v[32:47], v[112:115], v[124:127], v[32:47]\n"
        "ds_read_b128 a[116:119], v151 offset:36864\n"
        "v_fma_f32 v178, v176, v176, -v178\n"
        "v_mul_f32 v179, v179, v177\n"
        "v_mfma_f32_32x32x16_bf16 v[48:63], v[116:119], v[120:123], v[48:63]\n"
        "v_mfma_f32_32x32x16_bf16 v[64:79], v[116:119], v[124:127], v[64:79]\n"
        "s_waitcnt vmcnt(16)\n"
        "s_waitcnt lgkmcnt(0)\n"
        "s_barrier\n"
        "v_mfma_f32_32x32x16_bf16 v[16:31], v[128:131], v[136:139], v[16:31]\n"
        "s_add_u32 s90, s90, 128\n"
        "s_cmp_eq_u32 s90, 1280\n"
        "s_cselect_b32 s90, 0, s90\n"
        "s_add_u32 m0, s91, 98304\n"
        "s_nop 0\n"
        "buffer_load_dwordx4 v4, s[8:11], s90 offen offset:0 lds\n"
        "v_mfma_f32_32x32x16_bf16 v[32:47], v[128:131], v[140:143], v[32:47]\n"
        "v_mul_f32 v192, v167, v165\n"
        "v_mul_f32 v193, v166, v165\n"
        "v_fma_f32 v192, v166, v164, -v192\n"
        "v_fmac_f32 v193, v167, v164\n"
        "v_mul_f32 v195, v192, v165\n"
        "v_mul_f32 v194, v193, v165\n"
        "v_mfma_f32_32x32x16_bf16 v[48:63], v[132:135], v[136:139], v[48:63]\n"
        "buffer_load_dwordx4 v5, s[8:11], s90 offen offset:1024 lds\n"
        "v_fma_f32 v194, v192, v164, -v194\n"
        "v_fmac_f32 v195, v193, v164\n"
        "v_mfma_f32_32x32x16_bf16 v[64:79], v[132:135], v[140:143], v[64:79]\n"
        "v_mul_f32 v196, v195, v165\n"
        "v_mul_f32 v197, v194, v165\n"
        "v_fma_f32 v196, v194, v164, -v196\n"
        "v_fmac_f32 v197, v195, v164\n"
        "buffer_load_dwordx4 v6, s[8:11], s90 offen offset:2048 lds\n"
        "v_mfma_f32_32x32x16_bf16 v[16:31], a[64:67], a[72:75], v[16:31]\n"
        "v_mul_f32 v198, v197, v165\n"
        "v_mul_f32 v199, v196, v165\n"
        "v_fma_f32 v198, v196, v164, -v198\n"
        "v_fmac_f32 v199, v197, v164\n"
        "v_mul_f32 v201, v198, v165\n"
        "v_mul_f32 v200, v199, v165\n"
        "v_mfma_f32_32x32x16_bf16 v[32:47], a[64:67], a[76:79], v[32:47]\n"
        "buffer_load_dwordx4 v7, s[8:11], s90 offen offset:3072 lds\n"
        "s_add_u32 m0, s91, 114688\n"
        "v_fma_f32 v200, v198, v164, -v200\n"
        "v_mfma_f32_32x32x16_bf16 v[48:63], a[68:71], a[72:75], v[48:63]\n"
        "v_fmac_f32 v201, v199, v164\n"
        "v_mul_f32 v203, v200, v165\n"
        "v_mul_f32 v202, v201, v165\n"
        "v_fmac_f32 v203, v201, v164\n"
        "buffer_load_dwordx4 v8, s[80:83], s90 offen offset:0 lds\n"
        "v_mfma_f32_32x32x16_bf16 v[64:79], a[68:71], a[76:79], v[64:79]\n"
        "v_fma_f32 v202, v200, v164, -v202\n"
        "ds_read_b128 v[80:83], v144 offset:0\n"
        "v_mul_f32 v204, v203, v165\n"
        "v_mul_f32 v205, v202, v165\n"
        "ds_read_b128 v[88:91], v152 offset:0\n"
        "ds_read_b128 v[92:95], v152 offset:4096\n"
        "v_mfma_f32_32x32x16_bf16 v[16:31], a[80:83], a[88:91], v[16:31]\n"
        "buffer_load_dwordx4 v9, s[80:83], s90 offen offset:1024 lds\n"
        "ds_read_b128 v[84:87], v144 offset:4096\n"
        "ds_read_b128 v[96:99], v145 offset:0\n"
        "v_mfma_f32_32x32x16_bf16 v[32:47], a[80:83], a[92:95], v[32:47]\n"
        "ds_read_b128 v[104:107], v153 offset:0\n"
        "v_fma_f32 v204, v202, v164, -v204\n"
        "v_fmac_f32 v205, v203, v164\n"
        "ds_read_b128 v[108:111], v153 offset:4096\n"
        "buffer_load_dwordx4 v10, s[80:83], s90 offen offset:2048 lds\n"
        "v_mfma_f32_32x32x16_bf16 v[48:63], a[84:87], a[88:91], v[48:63]\n"
        "ds_read_b128 v[100:103], v145 offset:4096\n"
        "ds_read_b128 v[112:115], v146 offset:0\n"
        "ds_read_b128 v[120:123], v154 offset:0\n"
        "ds_read_b128 v[124:127], v154 offset:4096\n"
        "v_mul_f32 v206, v205, v165\n"
        "v_mul_f32 v207, v204, v165\n"
        "v_mfma_f32_32x32x16_bf16 v[64:79], a[84:87], a[92:95], v[64:79]\n"
        "buffer_load_dwordx4 v11, s[80:83], s90 offen offset:3072 lds\n"
        "ds_read_b128 v[116:119], v146 offset:4096\n"
        "ds_read_b128 v[128:131], v147 offset:0\n"
        "v_mfma_f32_32x32x16_bf16 v[16:31], a[96:99], a[104:107], v[16:31]\n"
        "ds_read_b128 v[136:139], v155 offset:0\n"
        "ds_read_b128 v[140:143], v155 offset:4096\n"
        "ds_read_b128 v[132:135], v147 offset:4096\n"
        "v_fma_f32 v206, v204, v164, -v206\n"
        "v_fmac_f32 v207, v205, v164\n"
        "v_mfma_f32_32x32x16_bf16 v[32:47], a[96:99], a[108:111], v[32:47]\n"
        "v_mfma_f32_32x32x16_bf16 v[48:63], a[100:103], a[104:107], v[48:63]\n"
        "v_mfma_f32_32x32x16_bf16 v[64:79], a[100:103], a[108:111], v[64:79]\n"
        "s_waitcnt vmcnt(16)\n"
        "s_waitcnt lgkmcnt(0)\n"
        "s_barrier\n"
        "v_mfma_f32_32x32x16_bf16 v[16:31], a[112:115], a[120:123], v[16:31]\n"
        "s_add_u32 s90, s90, 128\n"
        "s_cmp_eq_u32 s90, 1280\n"
        "s_cselect_b32 s90, 0, s90\n"
        "s_add_u32 m0, s91, 0\n"
        "s_nop 0\n"
        "buffer_load_dwordx4 v4, s[8:11], s90 offen offset:0 lds\n"
        "v_mfma_f32_32x32x16_bf16 v[32:47], a[112:115], a[124:127], v[32:47]\n"
        "v_mul_f32 v208, v207, v165\n"
        "v_mul_f32 v209, v206, v165\n"
        "v_fma_f32 v208, v206, v164, -v208\n"
        "v_fmac_f32 v209, v207, v164\n"
        "v_mul_f32 v211, v208, v165\n"
        "v_mul_f32 v210, v209, v165\n"
        "v_mfma_f32_32x32x16_bf16 v[48:63], a[116:119], a[120:123], v[48:63]\n"
        "buffer_load_dwordx4 v5, s[8:11], s90 offen offset:1024 lds\n"
        "v_fma_f32 v210, v208, v164, -v210\n"
        "v_fmac_f32 v211, v209, v164\n"
        "v_mfma_f32_32x32x16_bf16 v[64:79], a[116:119], a[124:127], v[64:79]\n"
        "v_mul_f32 v212, v211, v165\n"
        "v_mul_f32 v213, v210, v165\n"
        "v_fma_f32 v212, v210, v164, -v212\n"
        "v_fmac_f32 v213, v211, v164\n"
        "buffer_load_dwordx4 v6, s[8:11], s90 offen offset:2048 lds\n"
        "v_mfma_f32_32x32x16_bf16 v[16:31], v[80:83], v[88:91], v[16:31]\n"
        "v_mul_f32 v214, v213, v165\n"
        "v_mul_f32 v215, v212, v165\n"
        "v_fma_f32 v214, v212, v164, -v214\n"
        "v_fmac_f32 v215, v213, v164\n"
        "v_mul_f32 v217, v214, v165\n"
        "v_mul_f32 v216, v215, v165\n"
        "v_mfma_f32_32x32x16_bf16 v[32:47], v[80:83], v[92:95], v[32:47]\n"
        "buffer_load_dwordx4 v7, s[8:11], s90 offen offset:3072 lds\n"
        "s_add_u32 m0, s91, 16384\n"
        "v_fma_f32 v216, v214, v164, -v216\n"
        "v_mfma_f32_32x32x16_bf16 v[48:63], v[84:87], v[88:91], v[48:63]\n"
        "v_fmac_f32 v217, v215, v164\n"
        "v_mul_f32 v219, v216, v165\n"
        "v_mul_f32 v218, v217, v165\n"
        "v_fmac_f32 v219, v217, v164\n"
        "buffer_load_dwordx4 v8, s[80:83], s90 offen offset:0 lds\n"
        "v_mfma_f32_32x32x16_bf16 v[64:79], v[84:87], v[92:95], v[64:79]\n"
        "v_fma_f32 v218, v216, v164, -v218\n"
        "ds_read_b128 a[64:67], v144 offset:32768\n"
        "v_mul_f32 v220, v219, v165\n"
        "v_mul_f32 v221, v218, v165\n"
        "ds_read_b128 a[72:75], v152 offset:32768\n"
        "ds_read_b128 a[76:79], v152 offset:36864\n"
        "v_mfma_f32_32x32x16_bf16 v[16:31], v[96:99], v[104:107], v[16:31]\n"
        "buffer_load_dwordx4 v9, s[80:83], s90 offen offset:1024 lds\n"
        "ds_read_b128 a[68:71], v144 offset:36864\n"
        "ds_read_b128 a[80:83], v145 offset:32768\n"
        "v_mfma_f32_32x32x16_bf16 v[32:47], v[96:99], v[108:111], v[32:47]\n"
        "ds_read_b128 a[88:91], v153 offset:32768\n"
        "v_fma_f32 v220, v218, v164, -v220\n"
        "v_fmac_f32 v221, v219, v164\n"
        "ds_read_b128 a[92:95], v153 offset:36864\n"
        "buffer_load_dwordx4 v10, s[80:83], s90 offen offset:2048 lds\n"
        "v_mfma_f32_32x32x16_bf16 v[48:63], v[100:103], v[104:107], v[48:63]\n"
        "ds_read_b128 a[84:87], v145 offset:36864\n"
        "ds_read_b128 a[96:99], v146 offset:32768\n"
        "ds_read_b128 a[104:107], v154 offset:32768\n"
        "ds_read_b128 a[108:111], v154 offset:36864\n"
        "v_mul_f32 v222, v221, v165\n"
        "v_mul_f32 v223, v220, v165\n"
        "v_mfma_f32_32x32x16_bf16 v[64:79], v[100:103], v[108:111], v[64:79]\n"
        "buffer_load_dwordx4 v11, s[80:83], s90 offen offset:3072 lds\n"
        "ds_read_b128 a[100:103], v146 offset:36864\n"
        "ds_read_b128 a[112:115], v147 offset:32768\n"
        "v_mfma_f32_32x32x16_bf16 v[16:31], v[112:115], v[120:123], v[16:31]\n"
        "ds_read_b128 a[120:123], v155 offset:32768\n"
        "ds_read_b128 a[124:127], v155 offset:36864\n"
        "ds_read_b128 a[116:119], v147 offset:36864\n"
        "v_fma_f32 v222, v220, v164, -v222\n"
        "v_fmac_f32 v223, v221, v164\n"
        "v_mfma_f32_32x32x16_bf16 v[32:47], v[112:115], v[124:127], v[32:47]\n"
        "v_mfma_f32_32x32x16_bf16 v[48:63], v[116:119], v[120:123], v[48:63]\n"
        "v_mfma_f32_32x32x16_bf16 v[64:79], v[116:119], v[124:127], v[64:79]\n"
        "s_waitcnt vmcnt(16)\n"
        "s_waitcnt lgkmcnt(0)\n"
        "s_barrier\n"
        "v_mfma_f32_32x32x16_bf16 v[16:31], v[128:131], v[136:139], v[16:31]\n"
        "s_add_u32 s90, s90, 128\n"
        "s_cmp_eq_u32 s90, 1280\n"
        "s_cselect_b32 s90, 0, s90\n"
        "s_add_u32 m0, s91, 32768\n"
        "s_nop 0\n"
        "buffer_load_dwordx4 v4, s[8:11], s90 offen offset:0 lds\n"
        "v_mfma_f32_32x32x16_bf16 v[32:47], v[128:131], v[140:143], v[32:47]\n"
        "v_cndmask_b32_e64 v248, 1.0, v170, s[74:75]\n"
        "v_cndmask_b32_e64 v249, 0, v171, s[74:75]\n"
        "v_cndmask_b32_e64 v251, 0, v173, s[76:77]\n"
        "v_cndmask_b32_e64 v250, 1.0, v172, s[76:77]\n"
        "v_mul_f32 v180, v249, v251\n"
        "v_mul_f32 v181, v248, v251\n"
        "v_mfma_f32_32x32x16_bf16 v[48:63], v[132:135], v[136:139], v[48:63]\n"
        "buffer_load_dwordx4 v5, s[8:11], s90 offen offset:1024 lds\n"
        "v_fma_f32 v180, v248, v250, -v180\n"
        "v_fmac_f32 v181, v249, v250\n"
        "v_mfma_f32_32x32x16_bf16 v[64:79], v[132:135], v[140:143], v[64:79]\n"
        "v_cndmask_b32_e64 v251, 0, v175, s[74:75]\n"
        "v_cndmask_b32_e64 v250, 1.0, v174, s[74:75]\n"
        "v_cndmask_b32_e64 v175, 0, v177, s[76:77]\n"
        "v_cndmask_b32_e64 v174, 1.0, v176, s[76:77]\n"
        "buffer_load_dwordx4 v6, s[8:11], s90 offen offset:2048 lds\n"
        "v_mfma_f32_32x32x16_bf16 v[16:31], a[64:67], a[72:75], v[16:31]\n"
        "v_mul_f32 v184, v251, v175\n"
        "v_mul_f32 v185, v250, v175\n"
        "v_fma_f32 v184, v250, v174, -v184\n"
        "v_fmac_f32 v185, v251, v174\n"
        "v_mul_f32 v187, v184, v179\n"
        "v_mul_f32 v186, v185, v179\n"
        "v_mfma_f32_32x32x16_bf16 v[32:47], a[64:67], a[76:79], v[32:47]\n"
        "buffer_load_dwordx4 v7, s[8:11], s90 offen offset:3072 lds\n"
        "s_add_u32 m0, s91, 49152\n"
        "v_fma_f32 v186, v184, v178, -v186\n"
        "v_mfma_f32_32x32x16_bf16 v[48:63], a[68:71], a[72:75], v[48:63]\n"
        "v_fmac_f32 v187, v185, v178\n"
        "ds_read_b128 v[80:83], v148 offset:0\n"
        "v_mul_f32 v188, v187, v179\n"
        "v_mul_f32 v189, v186, v179\n"
        "buffer_load_dwordx4 v8, s[80:83], s90 offen offset:0 lds\n"
        "v_mfma_f32_32x32x16_bf16 v[64:79], a[68:71], a[76:79], v[64:79]\n"
        "ds_read_b128 v[88:91], v156 offset:0\n"
        "ds_read_b128 v[92:95], v156 offset:4096\n"
        "ds_read_b128 v[84:87], v148 offset:4096\n"
        "ds_read_b128 v[96:99], v149 offset:0\n"
        "ds_read_b128 v[104:107], v157 offset:0\n"
        "v_fma_f32 v188, v186, v178, -v188\n"
        "v_mfma_f32_32x32x16_bf16 v[16:31], a[80:83], a[88:91], v[16:31]\n"
        "buffer_load_dwordx4 v9, s[80:83], s90 offen offset:1024 lds\n"
        "v_fmac_f32 v189, v187, v178\n"
        "ds_read_b128 v[108:111], v157 offset:4096\n"
        "v_mfma_f32_32x32x16_bf16 v[32:47], a[80:83], a[92:95], v[32:47]\n"
        "ds_read_b128 v[100:103], v149 offset:4096\n"
        "ds_read_b128 v[112:115], v150 offset:0\n"
        "ds_read_b128 v[120:123], v158 offset:0\n"
        "ds_read_b128 v[124:127], v158 offset:4096\n"
        "buffer_load_dwordx4 v10, s[80:83], s90 offen offset:2048 lds\n"
        "v_mfma_f32_32x32x16_bf16 v[48:63], a[84:87], a[88:91], v[48:63]\n"
        "v_mul_f32 v182, v181, v169\n"
        "v_mul_f32 v183, v180, v169\n"
        "v_mul_f32 v190, v189, v179\n"
        "v_mul_f32 v191, v188, v179\n"
        "ds_read_b128 v[116:119], v150 offset:4096\n"
        "ds_read_b128 v[128:131], v151 offset:0\n"
        "v_mfma_f32_32x32x16_bf16 v[64:79], a[84:87], a[92:95], v[64:79]\n"
        "buffer_load_dwordx4 v11, s[80:83], s90 offen offset:3072 lds\n"
        "ds_read_b128 v[136:139], v159 offset:0\n"
        "ds_read_b128 v[140:143], v159 offset:4096\n"
        "v_mfma_f32_32x32x16_bf16 v[16:31], a[96:99], a[104:107], v[16:31]\n"
        "ds_read_b128 v[132:135], v151 offset:4096\n"
        "v_fma_f32 v182, v180, v168, -v182\n"
        "v_fmac_f32 v183, v181, v168\n"
        "v_fma_f32 v190, v188, v178, -v190\n"
        "v_fmac_f32 v191, v189, v178\n"
        "v_mfma_f32_32x32x16_bf16 v[32:47], a[96:99], a[108:111], v[32:47]\n"
        "v_mfma_f32_32x32x16_bf16 v[48:63], a[100:103], a[104:107], v[48:63]\n"
        "v_mfma_f32_32x32x16_bf16 v[64:79], a[100:103], a[108:111], v[64:79]\n"
        "s_waitcnt vmcnt(16)\n"
        "s_waitcnt lgkmcnt(0)\n"
        "s_barrier\n"
        "v_mfma_f32_32x32x16_bf16 v[16:31], a[112:115], a[120:123], v[16:31]\n"
        "s_lshl_b32 s70, s88, 1\n"
        "s_sub_u32 s70, s86, s70\n"
        "s_sub_u32 s70, s70, 1\n"
        "v_sub_u32 v250, s70, v253\n"
        "v_lshlrev_b32 v251, 4, v255\n"
        "v_ashrrev_i32 v228, 31, v250\n"
        "v_mfma_f32_32x32x16_bf16 v[32:47], a[112:115], a[124:127], v[32:47]\n"
        "v_mul_i32_i24 v224, 10240, v250\n"
        "v_add_u32 v250, -4, v250\n"
        "s_mul_i32 s70, s86, 10240\n"
        "v_ashrrev_i32 v229, 31, v250\n"
        "v_mul_i32_i24 v225, 10240, v250\n"
        "v_add_u32 v250, -4, v250\n"
        "v_mfma_f32_32x32x16_bf16 v[48:63], a[116:119], a[120:123], v[48:63]\n"
        "v_ashrrev_i32 v230, 31, v250\n"
        "v_mul_i32_i24 v226, 10240, v250\n"
        "v_add_u32 v250, -4, v250\n"
        "s_cmp_eq_u32 s88, 1\n"
        "v_add_u32 v251, 0x10000, v251\n"
        "v_mul_i32_i24 v227, 10240, v250\n"
        "v_mfma_f32_32x32x16_bf16 v[64:79], a[116:119], a[124:127], v[64:79]\n"
        "s_cselect_b32 s70, s70, 0x40000000\n"
        "v_add_u32 v224, v224, v251\n"
        "v_add_u32 v225, v225, v251\n"
        "v_add_u32 v226, v226, v251\n"
        "v_ashrrev_i32 v231, 31, v250\n"
        "v_add_u32 v227, v227, v251\n"
        "v_mfma_f32_32x32x16_bf16 v[16:31], v[80:83], v[88:91], v[16:31]\n"
        "v_add_u32 v232, s70, v251\n"
        "v_lshrrev_b32 v250, 2, v252\n"
        "v_lshrrev_b32 v251, 3, v252\n"
        "v_and_b32 v254, 0x40000000, v228\n"
        "v_xor_b32 v250, v250, v251\n"
        "v_max_u32 v224, v254, v224\n"
        "v_mfma_f32_32x32x16_bf16 v[32:47], v[80:83], v[92:95], v[32:47]\n"
        "v_xor_b32 v250, v250, v253\n"
        "v_and_b32 v254, 0x40000000, v229\n"
        "v_and_b32 v250, 1, v250\n"
        "v_max_u32 v225, v254, v225\n"
        "v_lshlrev_b32 v250, 2, v250\n"
        "s_lshl_b32 s70, s89, 8\n"
        "v_mfma_f32_32x32x16_bf16 v[48:63], v[84:87], v[88:91], v[48:63]\n"
        "v_and_b32 v254, 0x40000000, v230\n"
        "v_and_or_b32 v250, v252, 3, v250\n"
        "s_add_u32 s70, s70, 160256\n"
        "v_max_u32 v226, v254, v226\n"
        "v_and_b32 v254, 0x40000000, v231\n"
        "v_lshl_add_u32 v233, v252, 3, s70\n"
        "v_mfma_f32_32x32x16_bf16 v[64:79], v[84:87], v[92:95], v[64:79]\n"
        "v_lshlrev_b32 v250, 4, v250\n"
        "s_lshl_b32 s70, s3, 13\n"
        "v_max_u32 v227, v254, v227\n"
        "v_lshl_add_u32 v251, v252, 7, s70\n"
        "v_xor_b32 v254, 0, v250\n"
        "ds_read_b128 a[64:67], v148 offset:32768\n"
        "v_mfma_f32_32x32x16_bf16 v[16:31], v[96:99], v[104:107], v[16:31]\n"
        "v_add_u32 v234, v251, v254\n"
        "v_xor_b32 v254, 16, v250\n"
        "ds_read_b128 a[72:75], v156 offset:32768\n"
        "v_add_u32 v235, v251, v254\n"
        "v_xor_b32 v254, 32, v250\n"
        "ds_read_b128 a[76:79], v156 offset:36864\n"
        "v_mfma_f32_32x32x16_bf16 v[32:47], v[96:99], v[108:111], v[32:47]\n"
        "v_add_u32 v236, v251, v254\n"
        "v_xor_b32 v254, 48, v250\n"
        "v_lshrrev_b32 v250, 2, v1\n"
        "v_add_u32 v237, v251, v254\n"
        "v_lshl_add_u32 v251, v2, 3, v250\n"
        "v_and_b32 v254, 1, v1\n"
        "v_mfma_f32_32x32x16_bf16 v[48:63], v[100:103], v[104:107], v[48:63]\n"
        "v_lshlrev_b32 v251, 7, v251\n"
        "ds_read_b128 a[68:71], v148 offset:36864\n"
        "v_lshl_add_u32 v251, v254, 3, v251\n"
        "v_and_b32 v254, 1, v2\n"
        "v_add_u32 v251, s70, v251\n"
        "v_lshl_or_b32 v250, v254, 2, v250\n"
        "v_mfma_f32_32x32x16_bf16 v[64:79], v[100:103], v[108:111], v[64:79]\n"
        "v_bfe_u32 v254, v1, 1, 1\n"
        "ds_read_b128 a[80:83], v149 offset:32768\n"
        "v_xor_b32 v250, v250, v254\n"
        "ds_read_b128 a[88:91], v157 offset:32768\n"
        "v_lshlrev_b32 v250, 4, v250\n"
        "ds_read_b128 a[92:95], v157 offset:36864\n"
        "v_mfma_f32_32x32x16_bf16 v[16:31], v[112:115], v[120:123], v[16:31]\n"
        "v_xor_b32 v254, 0, v250\n"
        "ds_read_b128 a[84:87], v149 offset:36864\n"
        "v_add_u32 v238, v251, v254\n"
        "v_xor_b32 v254, 64, v250\n"
        "ds_read_b128 a[96:99], v150 offset:32768\n"
        "v_add_u32 v239, v251, v254\n"
        "v_mfma_f32_32x32x16_bf16 v[32:47], v[112:115], v[124:127], v[32:47]\n"
        "v_xor_b32 v254, 32, v250\n"
        "ds_read_b128 a[104:107], v158 offset:32768\n"
        "v_add_u32 v240, v251, v254\n"
        "v_xor_b32 v254, 96, v250\n"
        "ds_read_b128 a[108:111], v158 offset:36864\n"
        "v_add_u32 v241, v251, v254\n"
        "v_mfma_f32_32x32x16_bf16 v[48:63], v[116:119], v[120:123], v[48:63]\n"
        "v_xor_b32 v254, 64, v250\n"
        "ds_read_b128 a[100:103], v150 offset:36864\n"
        "v_add_u32 v242, v251, v254\n"
        "v_xor_b32 v254, 0, v250\n"
        "ds_read_b128 a[112:115], v151 offset:32768\n"
        "v_add_u32 v243, v251, v254\n"
        "v_mfma_f32_32x32x16_bf16 v[64:79], v[116:119], v[124:127], v[64:79]\n"
        "v_xor_b32 v254, 96, v250\n"
        "ds_read_b128 a[120:123], v159 offset:32768\n"
        "v_add_u32 v244, v251, v254\n"
        "v_xor_b32 v254, 32, v250\n"
        "ds_read_b128 a[124:127], v159 offset:36864\n"
        "ds_read_b128 a[116:119], v151 offset:36864\n"
        "v_not_b32 v228, v228\n"
        "v_not_b32 v229, v229\n"
        "v_not_b32 v230, v230\n"
        "v_not_b32 v231, v231\n"
        "v_lshl_or_b32 v232, v253, 30, v232\n"
        "v_add_u32 v245, v251, v254\n"
        "s_waitcnt vmcnt(8)\n"
        "s_waitcnt lgkmcnt(0)\n"
        "s_barrier\n"
        "v_mfma_f32_32x32x16_bf16 v[16:31], v[128:131], v[136:139], v[16:31]\n"
        "s_and_b32 s70, s5, 7\n"
        "s_lshl_b32 s70, s70, 2\n"
        "s_add_u32 s70, s70, s3\n"
        "s_lshl_b32 s70, s70, 8\n"
        "v_and_b32 v250, 1, v0\n"
        "s_add_u32 s70, s70, 11632640\n"
        "v_mfma_f32_32x32x16_bf16 v[32:47], v[128:131], v[140:143], v[32:47]\n"
        "v_lshl_add_u32 v250, v250, 7, s70\n"
        "ds_read_b128 v[80:83], v144 offset:0\n"
        "ds_read_b128 v[88:91], v152 offset:0\n"
        "ds_read_b128 v[92:95], v152 offset:4096\n"
        "ds_read_b128 v[84:87], v144 offset:4096\n"
        "buffer_load_dwordx2 v[246:247], v250, s[40:43], 0 offen sc1\n"
        "v_mfma_f32_32x32x16_bf16 v[48:63], v[132:135], v[136:139], v[48:63]\n"
        "ds_read_b128 v[96:99], v145 offset:0\n"
        "ds_read_b128 v[104:107], v153 offset:0\n"
        "ds_read_b128 v[108:111], v153 offset:4096\n"
        "ds_read_b128 v[100:103], v145 offset:4096\n"
        "ds_read_b128 v[112:115], v146 offset:0\n"
        "ds_read_b128 v[120:123], v154 offset:0\n"
        "v_mfma_f32_32x32x16_bf16 v[64:79], v[132:135], v[140:143], v[64:79]\n"
        "ds_read_b128 v[124:127], v154 offset:4096\n"
        "ds_read_b128 v[116:119], v146 offset:4096\n"
        "ds_read_b128 v[128:131], v147 offset:0\n"
        "ds_read_b128 v[136:139], v155 offset:0\n"
        "ds_read_b128 v[140:143], v155 offset:4096\n"
        "ds_read_b128 v[132:135], v147 offset:4096\n"
        "v_mfma_f32_32x32x16_bf16 v[16:31], a[64:67], a[72:75], v[16:31]\n"
        "v_mfma_f32_32x32x16_bf16 v[32:47], a[64:67], a[76:79], v[32:47]\n"
        "v_mfma_f32_32x32x16_bf16 v[48:63], a[68:71], a[72:75], v[48:63]\n"
        "v_mfma_f32_32x32x16_bf16 v[64:79], a[68:71], a[76:79], v[64:79]\n"
        "v_mfma_f32_32x32x16_bf16 v[16:31], a[80:83], a[88:91], v[16:31]\n"
        "v_mfma_f32_32x32x16_bf16 v[32:47], a[80:83], a[92:95], v[32:47]\n"
        "v_mfma_f32_32x32x16_bf16 v[48:63], a[84:87], a[88:91], v[48:63]\n"
        "v_mfma_f32_32x32x16_bf16 v[64:79], a[84:87], a[92:95], v[64:79]\n"
        "v_mfma_f32_32x32x16_bf16 v[16:31], a[96:99], a[104:107], v[16:31]\n"
        "v_mfma_f32_32x32x16_bf16 v[32:47], a[96:99], a[108:111], v[32:47]\n"
        "v_mfma_f32_32x32x16_bf16 v[48:63], a[100:103], a[104:107], v[48:63]\n"
        "v_mfma_f32_32x32x16_bf16 v[64:79], a[100:103], a[108:111], v[64:79]\n"
        "s_waitcnt vmcnt(1)\n"
        "s_waitcnt lgkmcnt(0)\n"
        "s_barrier\n"
        "s_waitcnt vmcnt(0)\n"
        "v_cmp_ne_u64 vcc, s[64:65], v[246:247]\n"
        "s_cmp_eq_u64 vcc, 0\n"
        "s_cselect_b32 s7, 0, 0x40000000\n"
        "v_mfma_f32_32x32x16_bf16 v[16:31], a[112:115], a[120:123], v[16:31]\n"
        "v_add_u32 v160, s7, v12\n"
        "s_add_u32 s84, s67, 0\n"
        "buffer_load_dwordx4 a[0:3], v160, s[40:43], s84 offen offset:0 sc1\n"
        "v_mfma_f32_32x32x16_bf16 v[32:47], a[112:115], a[124:127], v[32:47]\n"
        "v_add_u32 v161, s7, v13\n"
        "ds_read_b128 a[64:67], v144 offset:32768\n"
        "ds_read_b128 a[72:75], v152 offset:32768\n"
        "ds_read_b128 a[76:79], v152 offset:36864\n"
        "ds_read_b128 a[68:71], v144 offset:36864\n"
        "ds_read_b128 a[80:83], v145 offset:32768\n"
        "v_mfma_f32_32x32x16_bf16 v[48:63], a[116:119], a[120:123], v[48:63]\n"
        "buffer_load_dwordx4 a[4:7], v160, s[40:43], s84 offen offset:1024 sc1\n"
        "ds_read_b128 a[88:91], v153 offset:32768\n"
        "ds_read_b128 a[92:95], v153 offset:36864\n"
        "v_mfma_f32_32x32x16_bf16 v[64:79], a[116:119], a[124:127], v[64:79]\n"
        "ds_read_b128 a[84:87], v145 offset:36864\n"
        "ds_read_b128 a[96:99], v146 offset:32768\n"
        "ds_read_b128 a[104:107], v154 offset:32768\n"
        "ds_read_b128 a[108:111], v154 offset:36864\n"
        "buffer_load_dwordx4 a[8:11], v160, s[40:43], s84 offen offset:2048 sc1\n"
        "v_mfma_f32_32x32x16_bf16 v[16:31], v[80:83], v[88:91], v[16:31]\n"
        "ds_read_b128 a[100:103], v146 offset:36864\n"
        "ds_read_b128 a[112:115], v147 offset:32768\n"
        "ds_read_b128 a[120:123], v155 offset:32768\n"
        "ds_read_b128 a[124:127], v155 offset:36864\n"
        "v_add_u32 v162, s7, v14\n"
        "ds_read_b128 a[116:119], v147 offset:36864\n"
        "v_mfma_f32_32x32x16_bf16 v[32:47], v[80:83], v[92:95], v[32:47]\n"
        "buffer_load_dwordx4 a[12:15], v160, s[40:43], s84 offen offset:3072 sc1\n"
        "v_mfma_f32_32x32x16_bf16 v[48:63], v[84:87], v[88:91], v[48:63]\n"
        "buffer_load_dwordx4 a[16:19], v161, s[40:43], s84 offen offset:0 sc1\n"
        "v_mfma_f32_32x32x16_bf16 v[64:79], v[84:87], v[92:95], v[64:79]\n"
        "v_mfma_f32_32x32x16_bf16 v[16:31], v[96:99], v[104:107], v[16:31]\n"
        "v_mfma_f32_32x32x16_bf16 v[32:47], v[96:99], v[108:111], v[32:47]\n"
        "v_mfma_f32_32x32x16_bf16 v[48:63], v[100:103], v[104:107], v[48:63]\n"
        "v_mfma_f32_32x32x16_bf16 v[64:79], v[100:103], v[108:111], v[64:79]\n"
        "v_mfma_f32_32x32x16_bf16 v[16:31], v[112:115], v[120:123], v[16:31]\n"
        "v_mfma_f32_32x32x16_bf16 v[32:47], v[112:115], v[124:127], v[32:47]\n"
        "v_mfma_f32_32x32x16_bf16 v[48:63], v[116:119], v[120:123], v[48:63]\n"
        "v_mfma_f32_32x32x16_bf16 v[64:79], v[116:119], v[124:127], v[64:79]\n"
        "v_mfma_f32_32x32x16_bf16 v[16:31], v[128:131], v[136:139], v[16:31]\n"
        "s_add_u32 s84, s67, 0\n"
        "buffer_load_dwordx4 a[20:23], v161, s[40:43], s84 offen offset:1024 sc1\n"
        "v_mfma_f32_32x32x16_bf16 v[32:47], v[128:131], v[140:143], v[32:47]\n"
        "buffer_load_dwordx4 a[24:27], v161, s[40:43], s84 offen offset:2048 sc1\n"
        "v_mfma_f32_32x32x16_bf16 v[48:63], v[132:135], v[136:139], v[48:63]\n"
        "v_mfma_f32_32x32x16_bf16 v[64:79], v[132:135], v[140:143], v[64:79]\n"
        "buffer_load_dwordx4 a[28:31], v161, s[40:43], s84 offen offset:3072 sc1\n"
        "s_waitcnt lgkmcnt(14)\n"
        "v_mfma_f32_32x32x16_bf16 v[16:31], a[64:67], a[72:75], v[16:31]\n"
        "buffer_load_dwordx4 a[32:35], v162, s[40:43], s84 offen offset:0 sc1\n"
        "s_waitcnt lgkmcnt(13)\n"
        "v_mfma_f32_32x32x16_bf16 v[32:47], a[64:67], a[76:79], v[32:47]\n"
        "s_waitcnt lgkmcnt(12)\n"
        "v_mfma_f32_32x32x16_bf16 v[48:63], a[68:71], a[72:75], v[48:63]\n"
        "buffer_load_dwordx4 a[36:39], v162, s[40:43], s84 offen offset:1024 sc1\n"
        "v_mfma_f32_32x32x16_bf16 v[64:79], a[68:71], a[76:79], v[64:79]\n"
        "s_waitcnt lgkmcnt(10)\n"
        "v_mfma_f32_32x32x16_bf16 v[16:31], a[80:83], a[88:91], v[16:31]\n"
        "s_waitcnt lgkmcnt(9)\n"
        "v_mfma_f32_32x32x16_bf16 v[32:47], a[80:83], a[92:95], v[32:47]\n"
        "s_waitcnt lgkmcnt(8)\n"
        "v_mfma_f32_32x32x16_bf16 v[48:63], a[84:87], a[88:91], v[48:63]\n"
        "v_mfma_f32_32x32x16_bf16 v[64:79], a[84:87], a[92:95], v[64:79]\n"
        "s_waitcnt lgkmcnt(6)\n"
        "v_mfma_f32_32x32x16_bf16 v[16:31], a[96:99], a[104:107], v[16:31]\n"
        "s_waitcnt lgkmcnt(5)\n"
        "v_mfma_f32_32x32x16_bf16 v[32:47], a[96:99], a[108:111], v[32:47]\n"
        "s_waitcnt lgkmcnt(4)\n"
        "v_mfma_f32_32x32x16_bf16 v[48:63], a[100:103], a[104:107], v[48:63]\n"
        "v_mfma_f32_32x32x16_bf16 v[64:79], a[100:103], a[108:111], v[64:79]\n"
        "s_waitcnt lgkmcnt(2)\n"
        "v_mfma_f32_32x32x16_bf16 v[16:31], a[112:115], a[120:123], v[16:31]\n"
        "s_waitcnt lgkmcnt(1)\n"
        "v_mfma_f32_32x32x16_bf16 v[32:47], a[112:115], a[124:127], v[32:47]\n"
        "s_waitcnt lgkmcnt(0)\n"
        "v_mfma_f32_32x32x16_bf16 v[48:63], a[116:119], a[120:123], v[48:63]\n"
        "v_mfma_f32_32x32x16_bf16 v[64:79], a[116:119], a[124:127], v[64:79]\n"
        "s_nop 8\n"
        "v_pk_fma_f32 v[18:19], v[32:33], v[164:165], v[18:19] op_sel:[0,1,0] neg_lo:[1,0,0] neg_hi:[1,0,0]\n"
        "v_pk_fma_f32 v[34:35], v[16:17], v[164:165], v[34:35] op_sel:[0,1,0]\n"
        "v_pk_fma_f32 v[18:19], v[16:17], v[164:165], v[18:19] op_sel_hi:[1,0,1]\n"
        "v_pk_fma_f32 v[34:35], v[32:33], v[164:165], v[34:35] op_sel_hi:[1,0,1]\n"
        "v_pk_fma_f32 v[20:21], v[34:35], v[164:165], v[20:21] op_sel:[0,1,0] neg_lo:[1,0,0] neg_hi:[1,0,0]\n"
        "v_pk_fma_f32 v[36:37], v[18:19], v[164:165], v[36:37] op_sel:[0,1,0]\n"
        "v_pk_fma_f32 v[20:21], v[18:19], v[164:165], v[20:21] op_sel_hi:[1,0,1]\n"
        "v_pk_fma_f32 v[36:37], v[34:35], v[164:165], v[36:37] op_sel_hi:[1,0,1]\n"
        "v_pk_fma_f32 v[22:23], v[36:37], v[164:165], v[22:23] op_sel:[0,1,0] neg_lo:[1,0,0] neg_hi:[1,0,0]\n"
        "v_pk_fma_f32 v[38:39], v[20:21], v[164:165], v[38:39] op_sel:[0,1,0]\n"
        "v_pk_fma_f32 v[22:23], v[20:21], v[164:165], v[22:23] op_sel_hi:[1,0,1]\n"
        "v_pk_fma_f32 v[38:39], v[36:37], v[164:165], v[38:39] op_sel_hi:[1,0,1]\n"
        "v_pk_fma_f32 v[24:25], v[38:39], v[164:165], v[24:25] op_sel:[0,1,0] neg_lo:[1,0,0] neg_hi:[1,0,0]\n"
        "v_pk_fma_f32 v[40:41], v[22:23], v[164:165], v[40:41] op_sel:[0,1,0]\n"
        "v_pk_fma_f32 v[24:25], v[22:23], v[164:165], v[24:25] op_sel_hi:[1,0,1]\n"
        "v_pk_fma_f32 v[40:41], v[38:39], v[164:165], v[40:41] op_sel_hi:[1,0,1]\n"
        "v_pk_fma_f32 v[26:27], v[40:41], v[164:165], v[26:27] op_sel:[0,1,0] neg_lo:[1,0,0] neg_hi:[1,0,0]\n"
        "v_pk_fma_f32 v[42:43], v[24:25], v[164:165], v[42:43] op_sel:[0,1,0]\n"
        "v_pk_fma_f32 v[26:27], v[24:25], v[164:165], v[26:27] op_sel_hi:[1,0,1]\n"
        "v_pk_fma_f32 v[42:43], v[40:41], v[164:165], v[42:43] op_sel_hi:[1,0,1]\n"
        "v_pk_fma_f32 v[28:29], v[42:43], v[164:165], v[28:29] op_sel:[0,1,0] neg_lo:[1,0,0] neg_hi:[1,0,0]\n"
        "v_pk_fma_f32 v[44:45], v[26:27], v[164:165], v[44:45] op_sel:[0,1,0]\n"
        "v_pk_fma_f32 v[28:29], v[26:27], v[164:165], v[28:29] op_sel_hi:[1,0,1]\n"
        "v_pk_fma_f32 v[44:45], v[42:43], v[164:165], v[44:45] op_sel_hi:[1,0,1]\n"
        "v_pk_fma_f32 v[30:31], v[44:45], v[164:165], v[30:31] op_sel:[0,1,0] neg_lo:[1,0,0] neg_hi:[1,0,0]\n"
        "v_pk_fma_f32 v[46:47], v[28:29], v[164:165], v[46:47] op_sel:[0,1,0]\n"
        "v_pk_fma_f32 v[30:31], v[28:29], v[164:165], v[30:31] op_sel_hi:[1,0,1]\n"
        "v_pk_fma_f32 v[46:47], v[44:45], v[164:165], v[46:47] op_sel_hi:[1,0,1]\n"
        "v_pk_fma_f32 v[48:49], v[46:47], v[164:165], v[48:49] op_sel:[0,1,0] neg_lo:[1,0,0] neg_hi:[1,0,0]\n"
        "v_pk_fma_f32 v[64:65], v[30:31], v[164:165], v[64:65] op_sel:[0,1,0]\n"
        "v_pk_fma_f32 v[48:49], v[30:31], v[164:165], v[48:49] op_sel_hi:[1,0,1]\n"
        "v_pk_fma_f32 v[64:65], v[46:47], v[164:165], v[64:65] op_sel_hi:[1,0,1]\n"
        "v_pk_fma_f32 v[50:51], v[64:65], v[164:165], v[50:51] op_sel:[0,1,0] neg_lo:[1,0,0] neg_hi:[1,0,0]\n"
        "v_pk_fma_f32 v[66:67], v[48:49], v[164:165], v[66:67] op_sel:[0,1,0]\n"
        "v_pk_fma_f32 v[50:51], v[48:49], v[164:165], v[50:51] op_sel_hi:[1,0,1]\n"
        "v_pk_fma_f32 v[66:67], v[64:65], v[164:165], v[66:67] op_sel_hi:[1,0,1]\n"
        "v_pk_fma_f32 v[52:53], v[66:67], v[164:165], v[52:53] op_sel:[0,1,0] neg_lo:[1,0,0] neg_hi:[1,0,0]\n"
        "v_pk_fma_f32 v[68:69], v[50:51], v[164:165], v[68:69] op_sel:[0,1,0]\n"
        "v_pk_fma_f32 v[52:53], v[50:51], v[164:165], v[52:53] op_sel_hi:[1,0,1]\n"
        "v_pk_fma_f32 v[68:69], v[66:67], v[164:165], v[68:69] op_sel_hi:[1,0,1]\n"
        "v_pk_fma_f32 v[54:55], v[68:69], v[164:165], v[54:55] op_sel:[0,1,0] neg_lo:[1,0,0] neg_hi:[1,0,0]\n"
        "v_pk_fma_f32 v[70:71], v[52:53], v[164:165], v[70:71] op_sel:[0,1,0]\n"
        "v_pk_fma_f32 v[54:55], v[52:53], v[164:165], v[54:55] op_sel_hi:[1,0,1]\n"
        "v_pk_fma_f32 v[70:71], v[68:69], v[164:165], v[70:71] op_sel_hi:[1,0,1]\n"
        "v_pk_fma_f32 v[56:57], v[70:71], v[164:165], v[56:57] op_sel:[0,1,0] neg_lo:[1,0,0] neg_hi:[1,0,0]\n"
        "v_pk_fma_f32 v[72:73], v[54:55], v[164:165], v[72:73] op_sel:[0,1,0]\n"
        "v_pk_fma_f32 v[56:57], v[54:55], v[164:165], v[56:57] op_sel_hi:[1,0,1]\n"
        "v_pk_fma_f32 v[72:73], v[70:71], v[164:165], v[72:73] op_sel_hi:[1,0,1]\n"
        "v_pk_fma_f32 v[58:59], v[72:73], v[164:165], v[58:59] op_sel:[0,1,0] neg_lo:[1,0,0] neg_hi:[1,0,0]\n"
        "v_pk_fma_f32 v[74:75], v[56:57], v[164:165], v[74:75] op_sel:[0,1,0]\n"
        "v_pk_fma_f32 v[58:59], v[56:57], v[164:165], v[58:59] op_sel_hi:[1,0,1]\n"
        "v_pk_fma_f32 v[74:75], v[72:73], v[164:165], v[74:75] op_sel_hi:[1,0,1]\n"
        "v_pk_fma_f32 v[60:61], v[74:75], v[164:165], v[60:61] op_sel:[0,1,0] neg_lo:[1,0,0] neg_hi:[1,0,0]\n"
        "v_pk_fma_f32 v[76:77], v[58:59], v[164:165], v[76:77] op_sel:[0,1,0]\n"
        "v_pk_fma_f32 v[60:61], v[58:59], v[164:165], v[60:61] op_sel_hi:[1,0,1]\n"
        "v_pk_fma_f32 v[76:77], v[74:75], v[164:165], v[76:77] op_sel_hi:[1,0,1]\n"
        "v_pk_fma_f32 v[62:63], v[76:77], v[164:165], v[62:63] op_sel:[0,1,0] neg_lo:[1,0,0] neg_hi:[1,0,0]\n"
        "v_pk_fma_f32 v[78:79], v[60:61], v[164:165], v[78:79] op_sel:[0,1,0]\n"
        "v_pk_fma_f32 v[62:63], v[60:61], v[164:165], v[62:63] op_sel_hi:[1,0,1]\n"
        "v_pk_fma_f32 v[78:79], v[76:77], v[164:165], v[78:79] op_sel_hi:[1,0,1]\n"
        "v_fma_f32 v80, v168, v62, v63\n"
        "v_fma_f32 v81, v168, v78, v79\n"
        "v_fma_f32 v80, -v169, v78, v80\n"
        "v_fmac_f32 v81, v169, v62\n"
        "v_mov_b32 v82, v80\n"
        "v_mov_b32 v83, v81\n"
        "s_nop 0\n"
        "v_permlane32_swap_b32 v80, v82\n"
        "v_permlane32_swap_b32 v81, v83\n"
        "v_fma_f32 v84, v170, v80, v82\n"
        "v_fma_f32 v85, v170, v81, v83\n"
        "v_fma_f32 v84, -v171, v81, v84\n"
        "v_fmac_f32 v85, v171, v80\n"
        "s_lshl_b32 s70, s88, 9\n"
        "v_add_u32 v250, s70, v233\n"
        "ds_write_b64 v250, v[84:85]\n"
        "v_mov_b32 v93, s64\n"
        "v_mov_b32 v95, s65\n"
        "s_waitcnt lgkmcnt(0)\n"
        "s_barrier\n"
        "ds_read_b64 v[86:87], v233\n"
        "s_waitcnt lgkmcnt(0)\n"
        "v_fma_f32 v92, v172, v86, v84\n"
        "v_fma_f32 v94, v172, v87, v85\n"
        "v_fma_f32 v92, -v173, v87, v92\n"
        "v_fmac_f32 v94, v173, v86\n"
        "buffer_store_dwordx4 v[92:95], v232, s[40:43], 0 offen sc1\n"
        "s_cmp_eq_u32 s7, 0\n"
        "s_cbranch_scc1 L1_%=\n"
        "s_and_b32 s70, s5, 7\n"
        "s_lshl_b32 s70, s70, 2\n"
        "s_add_u32 s70, s70, s3\n"
        "s_lshl_b32 s70, s70, 8\n"
        "s_add_u32 s70, s70, 11632640\n"
        "v_and_b32 v251, 1, v0\n"
        "v_lshl_add_u32 v251, v251, 7, s70\n"
        "buffer_load_dwordx2 v[246:247], v251, s[40:43], 0 offen sc1\n"
        "s_waitcnt vmcnt(0)\n"
        "v_cmp_ne_u64 vcc, s[64:65], v[246:247]\n"
        "s_cbranch_vccz L3_%=\n"
        "s_nop 7\n"
        "s_nop 7\n"
        "s_nop 7\n"
        "s_waitcnt vmcnt(0) lgkmcnt(0)\n"
        "s_mov_b32 s6, 65536\n"
        "L2_%=:\n"
        "s_sleep 4\n"
        "buffer_load_dwordx2 v[246:247], v251, s[40:43], 0 offen sc1\n"
        "s_waitcnt vmcnt(0)\n"
        "v_cmp_ne_u64 vcc, s[64:65], v[246:247]\n"
        "s_cbranch_vccz L3_%=\n"
        "s_sub_u32 s6, s6, 1\n"
        "s_cmp_lg_u32 s6, 0\n"
        "s_cbranch_scc1 L2_%=\n"
        "L3_%=:\n"
        "s_add_u32 s84, s67, 0\n"
        "buffer_load_dwordx4 a[0:3], v12, s[40:43], s84 offen offset:0 sc1\n"
        "buffer_load_dwordx4 a[4:7], v12, s[40:43], s84 offen offset:1024 sc1\n"
        "buffer_load_dwordx4 a[8:11], v12, s[40:43], s84 offen offset:2048 sc1\n"
        "buffer_load_dwordx4 a[12:15], v12, s[40:43], s84 offen offset:3072 sc1\n"
        "buffer_load_dwordx4 a[16:19], v13, s[40:43], s84 offen offset:0 sc1\n"
        "buffer_load_dwordx4 a[20:23], v13, s[40:43], s84 offen offset:1024 sc1\n"
        "buffer_load_dwordx4 a[24:27], v13, s[40:43], s84 offen offset:2048 sc1\n"
        "buffer_load_dwordx4 a[28:31], v13, s[40:43], s84 offen offset:3072 sc1\n"
        "buffer_load_dwordx4 a[32:35], v14, s[40:43], s84 offen offset:0 sc1\n"
        "buffer_load_dwordx4 a[36:39], v14, s[40:43], s84 offen offset:1024 sc1\n"
        "L1_%=:\n"
        "s_add_u32 s85, s67, 10240\n"
        "buffer_load_dwordx4 a[40:43], v12, s[40:43], s85 offen offset:0 sc1\n"
        "v_pk_mul_f32 v[96:97], v[32:33], v[166:167] op_sel:[0,1]\n"
        "v_pk_mul_f32 v[98:99], v[16:17], v[166:167] op_sel:[0,1]\n"
        "buffer_load_dwordx4 a[44:47], v12, s[40:43], s85 offen offset:1024 sc1\n"
        "v_pk_mul_f32 v[100:101], v[34:35], v[166:167] op_sel:[0,1]\n"
        "v_pk_mul_f32 v[102:103], v[18:19], v[166:167] op_sel:[0,1]\n"
        "buffer_load_dwordx4 a[48:51], v12, s[40:43], s85 offen offset:2048 sc1\n"
        "v_pk_mul_f32 v[104:105], v[36:37], v[166:167] op_sel:[0,1]\n"
        "v_pk_mul_f32 v[106:107], v[20:21], v[166:167] op_sel:[0,1]\n"
        "buffer_load_dwordx4 a[52:55], v12, s[40:43], s85 offen offset:3072 sc1\n"
        "v_pk_mul_f32 v[108:109], v[38:39], v[166:167] op_sel:[0,1]\n"
        "v_pk_mul_f32 v[110:111], v[22:23], v[166:167] op_sel:[0,1]\n"
        "buffer_load_dwordx4 a[56:59], v13, s[40:43], s85 offen offset:0 sc1\n"
        "v_cndmask_b32_e64 v86, 0, v86, s[76:77]\n"
        "v_cndmask_b32_e64 v80, 0, v80, s[74:75]\n"
        "buffer_load_dwordx4 a[60:63], v13, s[40:43], s85 offen offset:1024 sc1\n"
        "v_cndmask_b32_e64 v87, 0, v87, s[76:77]\n"
        "v_cndmask_b32_e64 v81, 0, v81, s[74:75]\n"
        "buffer_load_dwordx4 a[64:67], v13, s[40:43], s85 offen offset:2048 sc1\n"
        "v_pk_fma_f32 v[16:17], v[16:17], v[166:167], v[96:97] op_sel_hi:[1,0,1] neg_lo:[0,0,1] neg_hi:[0,0,1]\n"
        "v_pk_fma_f32 v[32:33], v[32:33], v[166:167], v[98:99] op_sel_hi:[1,0,1] neg_lo:[1,0,1] neg_hi:[1,0,1]\n"
        "buffer_load_dwordx4 a[68:71], v13, s[40:43], s85 offen offset:3072 sc1\n"
        "v_pk_fma_f32 v[18:19], v[18:19], v[166:167], v[100:101] op_sel_hi:[1,0,1] neg_lo:[0,0,1] neg_hi:[0,0,1]\n"
        "v_pk_fma_f32 v[34:35], v[34:35], v[166:167], v[102:103] op_sel_hi:[1,0,1] neg_lo:[1,0,1] neg_hi:[1,0,1]\n"
        "buffer_load_dwordx4 a[72:75], v14, s[40:43], s85 offen offset:0 sc1\n"
        "v_pk_fma_f32 v[20:21], v[20:21], v[166:167], v[104:105] op_sel_hi:[1,0,1] neg_lo:[0,0,1] neg_hi:[0,0,1]\n"
        "v_pk_fma_f32 v[36:37], v[36:37], v[166:167], v[106:107] op_sel_hi:[1,0,1] neg_lo:[1,0,1] neg_hi:[1,0,1]\n"
        "v_pk_fma_f32 v[22:23], v[22:23], v[166:167], v[108:109] op_sel_hi:[1,0,1] neg_lo:[0,0,1] neg_hi:[0,0,1]\n"
        "v_pk_fma_f32 v[38:39], v[38:39], v[166:167], v[110:111] op_sel_hi:[1,0,1] neg_lo:[1,0,1] neg_hi:[1,0,1]\n"
        "v_pk_mul_f32 v[96:97], v[40:41], v[166:167] op_sel:[0,1]\n"
        "v_pk_mul_f32 v[98:99], v[24:25], v[166:167] op_sel:[0,1]\n"
        "v_pk_mul_f32 v[100:101], v[42:43], v[166:167] op_sel:[0,1]\n"
        "v_pk_mul_f32 v[102:103], v[26:27], v[166:167] op_sel:[0,1]\n"
        "v_pk_mul_f32 v[104:105], v[44:45], v[166:167] op_sel:[0,1]\n"
        "v_pk_mul_f32 v[106:107], v[28:29], v[166:167] op_sel:[0,1]\n"
        "v_pk_mul_f32 v[108:109], v[46:47], v[166:167] op_sel:[0,1]\n"
        "v_pk_mul_f32 v[110:111], v[30:31], v[166:167] op_sel:[0,1]\n"
        "v_fma_f32 v88, v248, v86, v80\n"
        "v_fma_f32 v89, v248, v87, v81\n"
        "buffer_load_dwordx4 a[76:79], v14, s[40:43], s85 offen offset:1024 sc1\n"
        "v_pk_fma_f32 v[24:25], v[24:25], v[166:167], v[96:97] op_sel_hi:[1,0,1] neg_lo:[0,0,1] neg_hi:[0,0,1]\n"
        "v_pk_fma_f32 v[40:41], v[40:41], v[166:167], v[98:99] op_sel_hi:[1,0,1] neg_lo:[1,0,1] neg_hi:[1,0,1]\n"
        "v_pk_fma_f32 v[26:27], v[26:27], v[166:167], v[100:101] op_sel_hi:[1,0,1] neg_lo:[0,0,1] neg_hi:[0,0,1]\n"
        "v_pk_fma_f32 v[42:43], v[42:43], v[166:167], v[102:103] op_sel_hi:[1,0,1] neg_lo:[1,0,1] neg_hi:[1,0,1]\n"
        "v_pk_fma_f32 v[28:29], v[28:29], v[166:167], v[104:105] op_sel_hi:[1,0,1] neg_lo:[0,0,1] neg_hi:[0,0,1]\n"
        "v_pk_fma_f32 v[44:45], v[44:45], v[166:167], v[106:107] op_sel_hi:[1,0,1] neg_lo:[1,0,1] neg_hi:[1,0,1]\n"
        "v_pk_fma_f32 v[30:31], v[30:31], v[166:167], v[108:109] op_sel_hi:[1,0,1] neg_lo:[0,0,1] neg_hi:[0,0,1]\n"
        "v_pk_fma_f32 v[46:47], v[46:47], v[166:167], v[110:111] op_sel_hi:[1,0,1] neg_lo:[1,0,1] neg_hi:[1,0,1]\n"
        "v_pk_mul_f32 v[96:97], v[64:65], v[166:167] op_sel:[0,1]\n"
        "v_pk_mul_f32 v[98:99], v[48:49], v[166:167] op_sel:[0,1]\n"
        "v_pk_mul_f32 v[100:101], v[66:67], v[166:167] op_sel:[0,1]\n"
        "v_pk_mul_f32 v[102:103], v[50:51], v[166:167] op_sel:[0,1]\n"
        "v_pk_mul_f32 v[104:105], v[68:69], v[166:167] op_sel:[0,1]\n"
        "v_pk_mul_f32 v[106:107], v[52:53], v[166:167] op_sel:[0,1]\n"
        "v_pk_mul_f32 v[108:109], v[70:71], v[166:167] op_sel:[0,1]\n"
        "v_pk_mul_f32 v[110:111], v[54:55], v[166:167] op_sel:[0,1]\n"
        "v_fma_f32 v88, -v249, v87, v88\n"
        "v_fmac_f32 v89, v249, v86\n"
        "v_pk_fma_f32 v[48:49], v[48:49], v[166:167], v[96:97] op_sel_hi:[1,0,1] neg_lo:[0,0,1] neg_hi:[0,0,1]\n"
        "v_pk_fma_f32 v[64:65], v[64:65], v[166:167], v[98:99] op_sel_hi:[1,0,1] neg_lo:[1,0,1] neg_hi:[1,0,1]\n"
        "v_pk_fma_f32 v[50:51], v[50:51], v[166:167], v[100:101] op_sel_hi:[1,0,1] neg_lo:[0,0,1] neg_hi:[0,0,1]\n"
        "v_pk_fma_f32 v[66:67], v[66:67], v[166:167], v[102:103] op_sel_hi:[1,0,1] neg_lo:[1,0,1] neg_hi:[1,0,1]\n"
        "v_pk_fma_f32 v[52:53], v[52:53], v[166:167], v[104:105] op_sel_hi:[1,0,1] neg_lo:[0,0,1] neg_hi:[0,0,1]\n"
        "v_pk_fma_f32 v[68:69], v[68:69], v[166:167], v[106:107] op_sel_hi:[1,0,1] neg_lo:[1,0,1] neg_hi:[1,0,1]\n"
        "v_pk_fma_f32 v[54:55], v[54:55], v[166:167], v[108:109] op_sel_hi:[1,0,1] neg_lo:[0,0,1] neg_hi:[0,0,1]\n"
        "v_pk_fma_f32 v[70:71], v[70:71], v[166:167], v[110:111] op_sel_hi:[1,0,1] neg_lo:[1,0,1] neg_hi:[1,0,1]\n"
        "v_fma_f32 v90, v168, v88, v62\n"
        "v_fma_f32 v91, v168, v89, v78\n"
        "v_pk_mul_f32 v[96:97], v[72:73], v[166:167] op_sel:[0,1]\n"
        "v_pk_mul_f32 v[98:99], v[56:57], v[166:167] op_sel:[0,1]\n"
        "v_pk_mul_f32 v[100:101], v[74:75], v[166:167] op_sel:[0,1]\n"
        "v_pk_mul_f32 v[102:103], v[58:59], v[166:167] op_sel:[0,1]\n"
        "v_pk_mul_f32 v[104:105], v[76:77], v[166:167] op_sel:[0,1]\n"
        "v_pk_mul_f32 v[106:107], v[60:61], v[166:167] op_sel:[0,1]\n"
        "v_pk_mul_f32 v[108:109], v[78:79], v[166:167] op_sel:[0,1]\n"
        "v_pk_mul_f32 v[110:111], v[62:63], v[166:167] op_sel:[0,1]\n"
        "v_fma_f32 v90, -v169, v89, v90\n"
        "v_fmac_f32 v91, v169, v88\n"
        "v_pk_fma_f32 v[56:57], v[56:57], v[166:167], v[96:97] op_sel_hi:[1,0,1] neg_lo:[0,0,1] neg_hi:[0,0,1]\n"
        "v_pk_fma_f32 v[72:73], v[72:73], v[166:167], v[98:99] op_sel_hi:[1,0,1] neg_lo:[1,0,1] neg_hi:[1,0,1]\n"
        "v_pk_fma_f32 v[58:59], v[58:59], v[166:167], v[100:101] op_sel_hi:[1,0,1] neg_lo:[0,0,1] neg_hi:[0,0,1]\n"
        "v_pk_fma_f32 v[74:75], v[74:75], v[166:167], v[102:103] op_sel_hi:[1,0,1] neg_lo:[1,0,1] neg_hi:[1,0,1]\n"
        "v_pk_fma_f32 v[60:61], v[60:61], v[166:167], v[104:105] op_sel_hi:[1,0,1] neg_lo:[0,0,1] neg_hi:[0,0,1]\n"
        "v_pk_fma_f32 v[76:77], v[76:77], v[166:167], v[106:107] op_sel_hi:[1,0,1] neg_lo:[1,0,1] neg_hi:[1,0,1]\n"
        "v_pk_fma_f32 v[62:63], v[62:63], v[166:167], v[108:109] op_sel_hi:[1,0,1] neg_lo:[0,0,1] neg_hi:[0,0,1]\n"
        "v_pk_fma_f32 v[78:79], v[78:79], v[166:167], v[110:111] op_sel_hi:[1,0,1] neg_lo:[1,0,1] neg_hi:[1,0,1]\n"
        "buffer_load_dwordx4 v[112:115], v224, s[40:43], 0 offen sc1\n"
        "buffer_load_dwordx4 v[116:119], v225, s[40:43], 0 offen sc1\n"
        "buffer_load_dwordx4 v[120:123], v226, s[40:43], 0 offen sc1\n"
        "buffer_load_dwordx4 v[124:127], v227, s[40:43], 0 offen sc1\n"
        "s_waitcnt vmcnt(3)\n"
        "v_xor_b32 v251, s64, v113\n"
        "v_xor_b32 v254, s65, v115\n"
        "v_or_b32 v251, v251, v254\n"
        "v_and_b32 v255, v251, v228\n"
        "s_waitcnt vmcnt(2)\n"
        "v_xor_b32 v251, s64, v117\n"
        "v_xor_b32 v254, s65, v119\n"
        "v_or_b32 v251, v251, v254\n"
        "v_and_or_b32 v255, v251, v229, v255\n"
        "s_waitcnt vmcnt(1)\n"
        "v_xor_b32 v251, s64, v121\n"
        "v_xor_b32 v254, s65, v123\n"
        "v_or_b32 v251, v251, v254\n"
        "v_and_or_b32 v255, v251, v230, v255\n"
        "s_waitcnt vmcnt(0)\n"
        "v_xor_b32 v251, s64, v125\n"
        "v_xor_b32 v254, s65, v127\n"
        "v_or_b32 v251, v251, v254\n"
        "v_and_or_b32 v255, v251, v231, v255\n"
        "v_cmp_ne_u32 vcc, 0, v255\n"
        "s_cbranch_vccz L5_%=\n"
        "s_nop 7\n"
        "s_nop 7\n"
        "s_nop 7\n"
        "s_waitcnt vmcnt(0) lgkmcnt(0)\n"
        "s_mov_b32 s6, 65536\n"
        "L4_%=:\n"
        "s_sleep 4\n"
        "buffer_load_dwordx4 v[112:115], v224, s[40:43], 0 offen sc1\n"
        "buffer_load_dwordx4 v[116:119], v225, s[40:43], 0 offen sc1\n"
        "buffer_load_dwordx4 v[120:123], v226, s[40:43], 0 offen sc1\n"
        "buffer_load_dwordx4 v[124:127], v227, s[40:43], 0 offen sc1\n"
        "s_waitcnt vmcnt(3)\n"
        "v_xor_b32 v251, s64, v113\n"
        "v_xor_b32 v254, s65, v115\n"
        "v_or_b32 v251, v251, v254\n"
        "v_and_b32 v255, v251, v228\n"
        "s_waitcnt vmcnt(2)\n"
        "v_xor_b32 v251, s64, v117\n"
        "v_xor_b32 v254, s65, v119\n"
        "v_or_b32 v251, v251, v254\n"
        "v_and_or_b32 v255, v251, v229, v255\n"
        "s_waitcnt vmcnt(1)\n"
        "v_xor_b32 v251, s64, v121\n"
        "v_xor_b32 v254, s65, v123\n"
        "v_or_b32 v251, v251, v254\n"
        "v_and_or_b32 v255, v251, v230, v255\n"
        "s_waitcnt vmcnt(0)\n"
        "v_xor_b32 v251, s64, v125\n"
        "v_xor_b32 v254, s65, v127\n"
        "v_or_b32 v251, v251, v254\n"
        "v_and_or_b32 v255, v251, v231, v255\n"
        "v_cmp_ne_u32 vcc, 0, v255\n"
        "s_cbranch_vccz L5_%=\n"
        "s_sub_u32 s6, s6, 1\n"
        "s_cmp_lg_u32 s6, 0\n"
        "s_cbranch_scc1 L4_%=\n"
        "L5_%=:\n"
        "v_mul_f32 v96, v184, v112\n"
        "v_mul_f32 v97, v184, v114\n"
        "v_mul_f32 v98, v186, v116\n"
        "v_mul_f32 v99, v186, v118\n"
        "v_fma_f32 v96, -v185, v114, v96\n"
        "v_fmac_f32 v97, v185, v112\n"
        "v_fma_f32 v98, -v187, v118, v98\n"
        "v_fmac_f32 v99, v187, v116\n"
        "v_fmac_f32 v96, v188, v120\n"
        "v_fmac_f32 v97, v188, v122\n"
        "v_fma_f32 v96, -v189, v122, v96\n"
        "v_fmac_f32 v97, v189, v120\n"
        "v_fmac_f32 v98, v190, v124\n"
        "v_fmac_f32 v99, v190, v126\n"
        "v_fma_f32 v98, -v191, v126, v98\n"
        "v_fmac_f32 v99, v191, v124\n"
        "v_pk_add_f32 v[96:97], v[96:97], v[98:99]\n"
        "v_mov_b32 v98, v96\n"
        "v_mov_b32 v99, v97\n"
        "s_nop 0\n"
        "v_permlane32_swap_b32 v96, v98\n"
        "v_permlane32_swap_b32 v97, v99\n"
        "v_pk_add_f32 v[96:97], v[96:97], v[98:99]\n"
        "ds_write_b64 v250, v[96:97] offset:1024\n"
        "s_waitcnt lgkmcnt(0)\n"
        "s_barrier\n"
        "ds_read2_b64 v[100:103], v233 offset0:128 offset1:192\n"
        "s_waitcnt lgkmcnt(0)\n"
        "v_pk_add_f32 v[100:101], v[100:101], v[102:103]\n"
        "s_add_u32 s84, s67, 20480\n"
        "v_fma_f32 v104, v180, v100, v88\n"
        "v_fma_f32 v105, v182, v100, v90\n"
        "v_fma_f32 v106, v180, v101, v89\n"
        "v_fma_f32 v107, v182, v101, v91\n"
        "v_fma_f32 v104, -v181, v101, v104\n"
        "v_fma_f32 v105, -v183, v101, v105\n"
        "v_fmac_f32 v106, v181, v100\n"
        "v_fmac_f32 v107, v183, v100\n"
        "v_pk_fma_f32 v[16:17], v[104:105], v[192:193], v[16:17] op_sel_hi:[1,0,1]\n"
        "v_pk_fma_f32 v[18:19], v[104:105], v[194:195], v[18:19] op_sel_hi:[1,0,1]\n"
        "v_pk_fma_f32 v[20:21], v[104:105], v[196:197], v[20:21] op_sel_hi:[1,0,1]\n"
        "v_pk_fma_f32 v[22:23], v[104:105], v[198:199], v[22:23] op_sel_hi:[1,0,1]\n"
        "v_pk_fma_f32 v[24:25], v[104:105], v[200:201], v[24:25] op_sel_hi:[1,0,1]\n"
        "v_pk_fma_f32 v[26:27], v[104:105], v[202:203], v[26:27] op_sel_hi:[1,0,1]\n"
        "v_pk_fma_f32 v[28:29], v[104:105], v[204:205], v[28:29] op_sel_hi:[1,0,1]\n"
        "v_pk_fma_f32 v[30:31], v[104:105], v[206:207], v[30:31] op_sel_hi:[1,0,1]\n"
        "v_pk_fma_f32 v[32:33], v[106:107], v[192:193], v[32:33] op_sel_hi:[1,0,1] neg_lo:[1,0,0] neg_hi:[1,0,0]\n"
        "v_pk_fma_f32 v[34:35], v[106:107], v[194:195], v[34:35] op_sel_hi:[1,0,1] neg_lo:[1,0,0] neg_hi:[1,0,0]\n"
        "v_pk_fma_f32 v[36:37], v[106:107], v[196:197], v[36:37] op_sel_hi:[1,0,1] neg_lo:[1,0,0] neg_hi:[1,0,0]\n"
        "v_pk_fma_f32 v[38:39], v[106:107], v[198:199], v[38:39] op_sel_hi:[1,0,1] neg_lo:[1,0,0] neg_hi:[1,0,0]\n"
        "v_pk_fma_f32 v[40:41], v[106:107], v[200:201], v[40:41] op_sel_hi:[1,0,1] neg_lo:[1,0,0] neg_hi:[1,0,0]\n"
        "v_pk_fma_f32 v[42:43], v[106:107], v[202:203], v[42:43] op_sel_hi:[1,0,1] neg_lo:[1,0,0] neg_hi:[1,0,0]\n"
        "v_pk_fma_f32 v[44:45], v[106:107], v[204:205], v[44:45] op_sel_hi:[1,0,1] neg_lo:[1,0,0] neg_hi:[1,0,0]\n"
        "v_pk_fma_f32 v[46:47], v[106:107], v[206:207], v[46:47] op_sel_hi:[1,0,1] neg_lo:[1,0,0] neg_hi:[1,0,0]\n"
        "v_pk_fma_f32 v[48:49], v[104:105], v[208:209], v[48:49] op_sel_hi:[1,0,1]\n"
        "v_pk_fma_f32 v[50:51], v[104:105], v[210:211], v[50:51] op_sel_hi:[1,0,1]\n"
        "v_pk_fma_f32 v[52:53], v[104:105], v[212:213], v[52:53] op_sel_hi:[1,0,1]\n"
        "v_pk_fma_f32 v[54:55], v[104:105], v[214:215], v[54:55] op_sel_hi:[1,0,1]\n"
        "v_pk_fma_f32 v[56:57], v[104:105], v[216:217], v[56:57] op_sel_hi:[1,0,1]\n"
        "v_pk_fma_f32 v[58:59], v[104:105], v[218:219], v[58:59] op_sel_hi:[1,0,1]\n"
        "v_pk_fma_f32 v[60:61], v[104:105], v[220:221], v[60:61] op_sel_hi:[1,0,1]\n"
        "v_pk_fma_f32 v[62:63], v[104:105], v[222:223], v[62:63] op_sel_hi:[1,0,1]\n"
        "v_pk_fma_f32 v[16:17], v[106:107], v[192:193], v[16:17] op_sel:[0,1,0] neg_lo:[1,0,0] neg_hi:[1,0,0]\n"
        "v_pk_fma_f32 v[18:19], v[106:107], v[194:195], v[18:19] op_sel:[0,1,0] neg_lo:[1,0,0] neg_hi:[1,0,0]\n"
        "v_pk_fma_f32 v[20:21], v[106:107], v[196:197], v[20:21] op_sel:[0,1,0] neg_lo:[1,0,0] neg_hi:[1,0,0]\n"
        "v_pk_fma_f32 v[22:23], v[106:107], v[198:199], v[22:23] op_sel:[0,1,0] neg_lo:[1,0,0] neg_hi:[1,0,0]\n"
        "v_pk_fma_f32 v[24:25], v[106:107], v[200:201], v[24:25] op_sel:[0,1,0] neg_lo:[1,0,0] neg_hi:[1,0,0]\n"
        "v_pk_fma_f32 v[26:27], v[106:107], v[202:203], v[26:27] op_sel:[0,1,0] neg_lo:[1,0,0] neg_hi:[1,0,0]\n"
        "v_pk_fma_f32 v[28:29], v[106:107], v[204:205], v[28:29] op_sel:[0,1,0] neg_lo:[1,0,0] neg_hi:[1,0,0]\n"
        "v_pk_fma_f32 v[30:31], v[106:107], v[206:207], v[30:31] op_sel:[0,1,0] neg_lo:[1,0,0] neg_hi:[1,0,0]\n"
        "v_pk_fma_f32 v[64:65], v[106:107], v[208:209], v[64:65] op_sel_hi:[1,0,1] neg_lo:[1,0,0] neg_hi:[1,0,0]\n"
        "v_pk_fma_f32 v[66:67], v[106:107], v[210:211], v[66:67] op_sel_hi:[1,0,1] neg_lo:[1,0,0] neg_hi:[1,0,0]\n"
        "v_pk_fma_f32 v[68:69], v[106:107], v[212:213], v[68:69] op_sel_hi:[1,0,1] neg_lo:[1,0,0] neg_hi:[1,0,0]\n"
        "v_pk_fma_f32 v[70:71], v[106:107], v[214:215], v[70:71] op_sel_hi:[1,0,1] neg_lo:[1,0,0] neg_hi:[1,0,0]\n"
        "v_pk_fma_f32 v[72:73], v[106:107], v[216:217], v[72:73] op_sel_hi:[1,0,1] neg_lo:[1,0,0] neg_hi:[1,0,0]\n"
        "v_pk_fma_f32 v[74:75], v[106:107], v[218:219], v[74:75] op_sel_hi:[1,0,1] neg_lo:[1,0,0] neg_hi:[1,0,0]\n"
        "v_pk_fma_f32 v[76:77], v[106:107], v[220:221], v[76:77] op_sel_hi:[1,0,1] neg_lo:[1,0,0] neg_hi:[1,0,0]\n"
        "v_pk_fma_f32 v[78:79], v[106:107], v[222:223], v[78:79] op_sel_hi:[1,0,1] neg_lo:[1,0,0] neg_hi:[1,0,0]\n"
        "v_pk_fma_f32 v[32:33], v[104:105], v[192:193], v[32:33] op_sel:[0,1,0] neg_lo:[1,0,0] neg_hi:[1,0,0]\n"
        "v_pk_fma_f32 v[34:35], v[104:105], v[194:195], v[34:35] op_sel:[0,1,0] neg_lo:[1,0,0] neg_hi:[1,0,0]\n"
        "v_pk_fma_f32 v[36:37], v[104:105], v[196:197], v[36:37] op_sel:[0,1,0] neg_lo:[1,0,0] neg_hi:[1,0,0]\n"
        "v_pk_fma_f32 v[38:39], v[104:105], v[198:199], v[38:39] op_sel:[0,1,0] neg_lo:[1,0,0] neg_hi:[1,0,0]\n"
        "v_pk_fma_f32 v[40:41], v[104:105], v[200:201], v[40:41] op_sel:[0,1,0] neg_lo:[1,0,0] neg_hi:[1,0,0]\n"
        "v_pk_fma_f32 v[42:43], v[104:105], v[202:203], v[42:43] op_sel:[0,1,0] neg_lo:[1,0,0] neg_hi:[1,0,0]\n"
        "v_pk_fma_f32 v[44:45], v[104:105], v[204:205], v[44:45] op_sel:[0,1,0] neg_lo:[1,0,0] neg_hi:[1,0,0]\n"
        "v_pk_fma_f32 v[46:47], v[104:105], v[206:207], v[46:47] op_sel:[0,1,0] neg_lo:[1,0,0] neg_hi:[1,0,0]\n"
        "v_pk_fma_f32 v[48:49], v[106:107], v[208:209], v[48:49] op_sel:[0,1,0] neg_lo:[1,0,0] neg_hi:[1,0,0]\n"
        "v_pk_fma_f32 v[50:51], v[106:107], v[210:211], v[50:51] op_sel:[0,1,0] neg_lo:[1,0,0] neg_hi:[1,0,0]\n"
        "v_pk_fma_f32 v[52:53], v[106:107], v[212:213], v[52:53] op_sel:[0,1,0] neg_lo:[1,0,0] neg_hi:[1,0,0]\n"
        "v_pk_fma_f32 v[54:55], v[106:107], v[214:215], v[54:55] op_sel:[0,1,0] neg_lo:[1,0,0] neg_hi:[1,0,0]\n"
        "v_pk_fma_f32 v[56:57], v[106:107], v[216:217], v[56:57] op_sel:[0,1,0] neg_lo:[1,0,0] neg_hi:[1,0,0]\n"
        "v_pk_fma_f32 v[58:59], v[106:107], v[218:219], v[58:59] op_sel:[0,1,0] neg_lo:[1,0,0] neg_hi:[1,0,0]\n"
        "v_pk_fma_f32 v[60:61], v[106:107], v[220:221], v[60:61] op_sel:[0,1,0] neg_lo:[1,0,0] neg_hi:[1,0,0]\n"
        "v_pk_fma_f32 v[62:63], v[106:107], v[222:223], v[62:63] op_sel:[0,1,0] neg_lo:[1,0,0] neg_hi:[1,0,0]\n"
        "v_pk_fma_f32 v[64:65], v[104:105], v[208:209], v[64:65] op_sel:[0,1,0] neg_lo:[1,0,0] neg_hi:[1,0,0]\n"
        "v_pk_fma_f32 v[66:67], v[104:105], v[210:211], v[66:67] op_sel:[0,1,0] neg_lo:[1,0,0] neg_hi:[1,0,0]\n"
        "v_pk_fma_f32 v[68:69], v[104:105], v[212:213], v[68:69] op_sel:[0,1,0] neg_lo:[1,0,0] neg_hi:[1,0,0]\n"
        "v_pk_fma_f32 v[70:71], v[104:105], v[214:215], v[70:71] op_sel:[0,1,0] neg_lo:[1,0,0] neg_hi:[1,0,0]\n"
        "v_pk_fma_f32 v[72:73], v[104:105], v[216:217], v[72:73] op_sel:[0,1,0] neg_lo:[1,0,0] neg_hi:[1,0,0]\n"
        "v_pk_fma_f32 v[74:75], v[104:105], v[218:219], v[74:75] op_sel:[0,1,0] neg_lo:[1,0,0] neg_hi:[1,0,0]\n"
        "v_pk_fma_f32 v[76:77], v[104:105], v[220:221], v[76:77] op_sel:[0,1,0] neg_lo:[1,0,0] neg_hi:[1,0,0]\n"
        "v_pk_fma_f32 v[78:79], v[104:105], v[222:223], v[78:79] op_sel:[0,1,0] neg_lo:[1,0,0] neg_hi:[1,0,0]\n"
        "v_cvt_pk_bf16_f32 v112, v16, v18\n"
        "v_cvt_pk_bf16_f32 v113, v20, v22\n"
        "v_cvt_pk_bf16_f32 v114, v24, v26\n"
        "v_cvt_pk_bf16_f32 v115, v28, v30\n"
        "v_cvt_pk_bf16_f32 v116, v32, v34\n"
        "v_cvt_pk_bf16_f32 v117, v36, v38\n"
        "v_cvt_pk_bf16_f32 v118, v40, v42\n"
        "v_cvt_pk_bf16_f32 v119, v44, v46\n"
        "v_cvt_pk_bf16_f32 v120, v17, v19\n"
        "v_cvt_pk_bf16_f32 v121, v21, v23\n"
        "v_cvt_pk_bf16_f32 v122, v25, v27\n"
        "v_cvt_pk_bf16_f32 v123, v29, v31\n"
        "v_cvt_pk_bf16_f32 v124, v33, v35\n"
        "v_cvt_pk_bf16_f32 v125, v37, v39\n"
        "v_cvt_pk_bf16_f32 v126, v41, v43\n"
        "v_cvt_pk_bf16_f32 v127, v45, v47\n"
        "v_cvt_pk_bf16_f32 v128, v48, v50\n"
        "v_cvt_pk_bf16_f32 v129, v52, v54\n"
        "v_cvt_pk_bf16_f32 v130, v56, v58\n"
        "v_cvt_pk_bf16_f32 v131, v60, v62\n"
        "ds_write_b128 v234, v[112:115] offset:0\n"
        "v_cvt_pk_bf16_f32 v132, v64, v66\n"
        "v_cvt_pk_bf16_f32 v133, v68, v70\n"
        "v_cvt_pk_bf16_f32 v134, v72, v74\n"
        "v_cvt_pk_bf16_f32 v135, v76, v78\n"
        "ds_write_b128 v234, v[116:119] offset:4096\n"
        "v_cvt_pk_bf16_f32 v136, v49, v51\n"
        "v_cvt_pk_bf16_f32 v137, v53, v55\n"
        "v_cvt_pk_bf16_f32 v138, v57, v59\n"
        "v_cvt_pk_bf16_f32 v139, v61, v63\n"
        "ds_write_b128 v236, v[120:123] offset:0\n"
        "v_cvt_pk_bf16_f32 v140, v65, v67\n"
        "v_cvt_pk_bf16_f32 v141, v69, v71\n"
        "v_cvt_pk_bf16_f32 v142, v73, v75\n"
        "v_cvt_pk_bf16_f32 v143, v77, v79\n"
        "ds_write_b128 v236, v[124:127] offset:4096\n"
        "ds_write_b128 v235, v[128:131] offset:0\n"
        "ds_write_b128 v235, v[132:135] offset:4096\n"
        "ds_write_b128 v237, v[136:139] offset:0\n"
        "ds_write_b128 v237, v[140:143] offset:4096\n"
        "ds_read_b64_tr_b16 v[144:145], v238 offset:0\n"
        "ds_read_b64_tr_b16 v[146:147], v239 offset:512\n"
        "ds_read_b64_tr_b16 v[148:149], v238 offset:4096\n"
        "ds_read_b64_tr_b16 v[150:151], v239 offset:4608\n"
        "ds_read_b64_tr_b16 v[152:153], v240 offset:0\n"
        "buffer_load_dwordx4 a[80:83], v12, s[40:43], s84 offen offset:0 sc1\n"
        "ds_read_b64_tr_b16 v[154:155], v241 offset:512\n"
        "ds_read_b64_tr_b16 v[156:157], v240 offset:4096\n"
        "buffer_load_dwordx4 a[84:87], v12, s[40:43], s84 offen offset:1024 sc1\n"
        "ds_read_b64_tr_b16 v[158:159], v241 offset:4608\n"
        "s_lshl_b32 s70, s86, 1\n"
        "ds_read_b64_tr_b16 v[160:161], v242 offset:0\n"
        "buffer_load_dwordx4 a[88:91], v12, s[40:43], s84 offen offset:2048 sc1\n"
        "ds_read_b64_tr_b16 v[162:163], v243 offset:512\n"
        "s_add_u32 s70, s70, s88\n"
        "s_lshl_b32 s71, s87, 1\n"
        "s_add_u32 s71, s71, s89\n"
        "s_lshl_b32 s68, s70, 5\n"
        "s_add_u32 s68, s68, s71\n"
        "s_lshl_b32 s68, s68, 7\n"
        "ds_read_b64_tr_b16 v[164:165], v242 offset:4096\n"
        "buffer_load_dwordx4 a[92:95], v12, s[40:43], s84 offen offset:3072 sc1\n"
        "ds_read_b64_tr_b16 v[166:167], v243 offset:4608\n"
        "s_add_u32 s68, s68, 11796480\n"
        "s_mul_i32 s70, s70, 163840\n"
        "s_lshl_b32 s71, s71, 11\n"
        "s_add_u32 s70, s70, s71\n"
        "s_add_u32 s86, s70, 0x600000\n"
        "ds_read_b64_tr_b16 v[168:169], v244 offset:0\n"
        "ds_read_b64_tr_b16 v[170:171], v245 offset:512\n"
        "s_waitcnt lgkmcnt(12)\n"
        "buffer_store_dwordx4 v[144:147], v3, s[40:43], s86 offen offset:0 sc1\n"
        "ds_read_b64_tr_b16 v[172:173], v244 offset:4096\n"
        "ds_read_b64_tr_b16 v[174:175], v245 offset:4608\n"
        "s_waitcnt lgkmcnt(12)\n"
        "buffer_store_dwordx4 v[148:151], v3, s[40:43], s86 offen offset:1024 sc1\n"
        "s_add_u32 s87, s86, 40960\n"
        "s_waitcnt lgkmcnt(10)\n"
        "buffer_store_dwordx4 v[152:155], v3, s[40:43], s87 offen offset:0 sc1\n"
        "s_waitcnt lgkmcnt(8)\n"
        "buffer_store_dwordx4 v[156:159], v3, s[40:43], s87 offen offset:1024 sc1\n"
        "s_add_u32 s88, s86, 81920\n"
        "s_waitcnt lgkmcnt(6)\n"
        "buffer_store_dwordx4 v[160:163], v3, s[40:43], s88 offen offset:0 sc1\n"
        "s_waitcnt lgkmcnt(4)\n"
        "buffer_store_dwordx4 v[164:167], v3, s[40:43], s88 offen offset:1024 sc1\n"
        "s_add_u32 s89, s86, 122880\n"
        "s_waitcnt lgkmcnt(2)\n"
        "buffer_store_dwordx4 v[168:171], v3, s[40:43], s89 offen offset:0 sc1\n"
        "s_waitcnt lgkmcnt(0)\n"
        "buffer_store_dwordx4 v[172:175], v3, s[40:43], s89 offen offset:1024 sc1\n"
        "s_add_u32 s12, s3, 1\n"
        "s_and_b32 s12, s12, 3\n"
        "s_add_u32 s13, s3, 2\n"
        "s_and_b32 s13, s13, 3\n"
        "s_add_u32 s14, s3, 3\n"
        "s_and_b32 s14, s14, 3\n"
        "s_add_u32 s15, s3, 4\n"
        "s_and_b32 s15, s15, 3\n"
        "s_mul_i32 s70, s66, 163840\n"
        "s_mul_i32 s71, s3, 10240\n"
        "s_add_u32 s70, s70, s71\n"
        "s_add_u32 s70, s70, 0x600000\n"
        "s_mul_i32 s16, s12, 40960\n"
        "s_add_u32 s16, s16, s70\n"
        "s_mul_i32 s17, s13, 40960\n"
        "s_add_u32 s17, s17, s70\n"
        "s_mul_i32 s18, s14, 40960\n"
        "s_add_u32 s18, s18, s70\n"
        "s_mul_i32 s19, s15, 40960\n"
        "s_add_u32 s19, s19, s70\n"
        "s_mul_i32 s71, s3, 640\n"
        "s_lshl_b32 s48, s66, 12\n"
        "s_add_u32 s48, s48, s71\n"
        "s_add_u32 s48, s48, 11796480\n"
        "s_mov_b32 s7, 0x40000000\n"
        "s_branch G2_%=\n"
        "PACK_%=:\n"
        "s_sub_u32 s86, s5, 30\n"
        "s_lshl_b32 s87, s4, 1\n"
        "s_add_u32 s87, s87, s86\n"
        "s_movk_i32 s70, 0x78\n"
        "v_add_u32 v250, 0, v0\n"
        "v_mul_u32_u24 v251, 205, v250\n"
        "v_lshrrev_b32 v251, 12, v251\n"
        "v_mul_u32_u24 v252, 20, v251\n"
        "v_sub_u32 v250, v250, v252\n"
        "v_lshrrev_b32 v252, 2, v251\n"
        "v_lshlrev_b32 v252, 1, v252\n"
        "v_bfe_u32 v252, s70, v252, 2\n"
        "v_xor_b32 v250, v250, v252\n"
        "v_mul_u32_u24 v251, 1280, v251\n"
        "v_lshl_add_u32 v250, v250, 4, v251\n"
        "v_subrev_u32 v4, 0, v250\n"
        "v_add_u32 v250, 64, v0\n"
        "v_mul_u32_u24 v251, 205, v250\n"
        "v_lshrrev_b32 v251, 12, v251\n"
        "v_mul_u32_u24 v252, 20, v251\n"
        "v_sub_u32 v250, v250, v252\n"
        "v_lshrrev_b32 v252, 2, v251\n"
        "v_lshlrev_b32 v252, 1, v252\n"
        "v_bfe_u32 v252, s70, v252, 2\n"
        "v_xor_b32 v250, v250, v252\n"
        "v_mul_u32_u24 v251, 1280, v251\n"
        "v_lshl_add_u32 v250, v250, 4, v251\n"
        "v_subrev_u32 v5, 1024, v250\n"
        "v_add_u32 v250, 128, v0\n"
        "v_mul_u32_u24 v251, 205, v250\n"
        "v_lshrrev_b32 v251, 12, v251\n"
        "v_mul_u32_u24 v252, 20, v251\n"
        "v_sub_u32 v250, v250, v252\n"
        "v_lshrrev_b32 v252, 2, v251\n"
        "v_lshlrev_b32 v252, 1, v252\n"
        "v_bfe_u32 v252, s70, v252, 2\n"
        "v_xor_b32 v250, v250, v252\n"
        "v_mul_u32_u24 v251, 1280, v251\n"
        "v_lshl_add_u32 v250, v250, 4, v251\n"
        "v_subrev_u32 v6, 2048, v250\n"
        "v_add_u32 v250, 192, v0\n"
        "v_mul_u32_u24 v251, 205, v250\n"
        "v_lshrrev_b32 v251, 12, v251\n"
        "v_mul_u32_u24 v252, 20, v251\n"
        "v_sub_u32 v250, v250, v252\n"
        "v_lshrrev_b32 v252, 2, v251\n"
        "v_lshlrev_b32 v252, 1, v252\n"
        "v_bfe_u32 v252, s70, v252, 2\n"
        "v_xor_b32 v250, v250, v252\n"
        "v_mul_u32_u24 v251, 1280, v251\n"
        "v_lshl_add_u32 v250, v250, 4, v251\n"
        "v_subrev_u32 v7, 3072, v250\n"
        "v_add_u32 v250, 256, v0\n"
        "v_mul_u32_u24 v251, 205, v250\n"
        "v_lshrrev_b32 v251, 12, v251\n"
        "v_mul_u32_u24 v252, 20, v251\n"
        "v_sub_u32 v250, v250, v252\n"
        "v_lshrrev_b32 v252, 2, v251\n"
        "v_lshlrev_b32 v252, 1, v252\n"
        "v_bfe_u32 v252, s70, v252, 2\n"
        "v_xor_b32 v250, v250, v252\n"
        "v_mul_u32_u24 v251, 1280, v251\n"
        "v_lshl_add_u32 v250, v250, 4, v251\n"
        "v_subrev_u32 v8, 0, v250\n"
        "v_lshrrev_b32 v252, 2, v1\n"
        "v_lshlrev_b32 v252, 1, v252\n"
        "v_bfe_u32 v252, s70, v252, 2\n"
        "v_xor_b32 v252, v252, v2\n"
        "v_mul_u32_u24 v251, 320, v1\n"
        "v_lshl_add_u32 v251, v252, 4, v251\n"
        "s_mul_i32 s71, s3, 25600\n"
        "v_add_u32 v9, s71, v251\n"
        "s_mul_i32 s72, s4, 102400\n"
        "s_mul_i32 s73, s3, 320\n"
        "s_add_u32 s72, s72, s73\n"
        "s_cmp_eq_u32 s86, 0\n"
        "s_waitcnt lgkmcnt(0)\n"
        "s_cselect_b64 s[80:81], s[28:29], s[32:33]\n"
        "s_cselect_b64 s[82:83], s[30:31], s[34:35]\n"
        "s_load_dwordx2 s[64:65], s[40:41], 0\n"
        "s_add_u32 m0, s71, 0\n"
        "s_nop 0\n"
        "buffer_load_dwordx4 v4, s[80:83], s72 offen offset:0 lds\n"
        "buffer_load_dwordx4 v5, s[80:83], s72 offen offset:1024 lds\n"
        "buffer_load_dwordx4 v6, s[80:83], s72 offen offset:2048 lds\n"
        "buffer_load_dwordx4 v7, s[80:83], s72 offen offset:3072 lds\n"
        "s_add_u32 m0, m0, 4096\n"
        "s_nop 0\n"
        "buffer_load_dwordx4 v8, s[80:83], s72 offen offset:0 lds\n"
        "s_add_u32 m0, s71, 5120\n"
        "s_add_u32 s72, s72, 20480\n"
        "buffer_load_dwordx4 v4, s[80:83], s72 offen offset:0 lds\n"
        "buffer_load_dwordx4 v5, s[80:83], s72 offen offset:1024 lds\n"
        "buffer_load_dwordx4 v6, s[80:83], s72 offen offset:2048 lds\n"
        "buffer_load_dwordx4 v7, s[80:83], s72 offen offset:3072 lds\n"
        "s_add_u32 m0, m0, 4096\n"
        "s_nop 0\n"
        "buffer_load_dwordx4 v8, s[80:83], s72 offen offset:0 lds\n"
        "s_add_u32 m0, s71, 10240\n"
        "s_add_u32 s72, s72, 20480\n"
        "buffer_load_dwordx4 v4, s[80:83], s72 offen offset:0 lds\n"
        "buffer_load_dwordx4 v5, s[80:83], s72 offen offset:1024 lds\n"
        "buffer_load_dwordx4 v6, s[80:83], s72 offen offset:2048 lds\n"
        "buffer_load_dwordx4 v7, s[80:83], s72 offen offset:3072 lds\n"
        "s_add_u32 m0, m0, 4096\n"
        "s_nop 0\n"
        "buffer_load_dwordx4 v8, s[80:83], s72 offen offset:0 lds\n"
        "s_add_u32 m0, s71, 15360\n"
        "s_add_u32 s72, s72, 20480\n"
        "buffer_load_dwordx4 v4, s[80:83], s72 offen offset:0 lds\n"
        "buffer_load_dwordx4 v5, s[80:83], s72 offen offset:1024 lds\n"
        "buffer_load_dwordx4 v6, s[80:83], s72 offen offset:2048 lds\n"
        "buffer_load_dwordx4 v7, s[80:83], s72 offen offset:3072 lds\n"
        "s_add_u32 m0, m0, 4096\n"
        "s_nop 0\n"
        "buffer_load_dwordx4 v8, s[80:83], s72 offen offset:0 lds\n"
        "s_add_u32 m0, s71, 20480\n"
        "s_add_u32 s72, s72, 20480\n"
        "buffer_load_dwordx4 v4, s[80:83], s72 offen offset:0 lds\n"
        "buffer_load_dwordx4 v5, s[80:83], s72 offen offset:1024 lds\n"
        "buffer_load_dwordx4 v6, s[80:83], s72 offen offset:2048 lds\n"
        "buffer_load_dwordx4 v7, s[80:83], s72 offen offset:3072 lds\n"
        "s_add_u32 m0, m0, 4096\n"
        "s_nop 0\n"
        "buffer_load_dwordx4 v8, s[80:83], s72 offen offset:0 lds\n"
        "s_waitcnt lgkmcnt(0)\n"
        "s_mul_hi_u32 s70, s64, 0x4c957f2d\n"
        "s_mul_i32 s71, s64, 0x5851f42d\n"
        "s_mul_i32 s72, s65, 0x4c957f2d\n"
        "s_mul_i32 s64, s64, 0x4c957f2d\n"
        "s_add_u32 s70, s70, s71\n"
        "s_add_u32 s70, s70, s72\n"
        "s_add_u32 s64, s64, 1\n"
        "s_addc_u32 s65, s70, 0\n"
        "s_lshl_b32 s70, s4, 2\n"
        "s_add_u32 s70, s70, s3\n"
        "s_lshl_b32 s68, s70, 1\n"
        "s_add_u32 s68, s68, s86\n"
        "s_lshl_b32 s68, s68, 7\n"
        "s_add_u32 s68, s68, 11632640\n"
        "s_mul_i32 s70, s70, 51200\n"
        "s_mul_i32 s71, s86, 5120\n"
        "s_add_u32 s70, s70, s71\n"
        "s_add_u32 s70, s70, 0xb80000\n"
        "s_add_u32 s71, s70, 10240\n"
        "s_add_u32 s72, s70, 20480\n"
        "s_add_u32 s73, s70, 30720\n"
        "s_add_u32 s89, s70, 40960\n"
        "s_waitcnt vmcnt(20)\n"
        "ds_read_b128 a[0:3], v9 offset:0\n"
        "ds_read_b128 a[4:7], v9 offset:64\n"
        "ds_read_b128 a[8:11], v9 offset:128\n"
        "ds_read_b128 a[12:15], v9 offset:192\n"
        "ds_read_b128 a[16:19], v9 offset:256\n"
        "s_waitcnt lgkmcnt(4)\n"
        "buffer_store_dwordx4 a[0:3], v12, s[40:43], s70 offen offset:0 sc1\n"
        "s_waitcnt lgkmcnt(3)\n"
        "buffer_store_dwordx4 a[4:7], v12, s[40:43], s71 offen offset:0 sc1\n"
        "s_waitcnt lgkmcnt(2)\n"
        "buffer_store_dwordx4 a[8:11], v12, s[40:43], s72 offen offset:0 sc1\n"
        "s_waitcnt lgkmcnt(1)\n"
        "buffer_store_dwordx4 a[12:15], v12, s[40:43], s73 offen offset:0 sc1\n"
        "s_waitcnt lgkmcnt(0)\n"
        "buffer_store_dwordx4 a[16:19], v12, s[40:43], s89 offen offset:0 sc1\n"
        "s_waitcnt vmcnt(20)\n"
        "ds_read_b128 a[20:23], v9 offset:5120\n"
        "ds_read_b128 a[24:27], v9 offset:5184\n"
        "ds_read_b128 a[28:31], v9 offset:5248\n"
        "ds_read_b128 a[32:35], v9 offset:5312\n"
        "ds_read_b128 a[36:39], v9 offset:5376\n"
        "s_waitcnt lgkmcnt(4)\n"
        "buffer_store_dwordx4 a[20:23], v12, s[40:43], s70 offen offset:1024 sc1\n"
        "s_waitcnt lgkmcnt(3)\n"
        "buffer_store_dwordx4 a[24:27], v12, s[40:43], s71 offen offset:1024 sc1\n"
        "s_waitcnt lgkmcnt(2)\n"
        "buffer_store_dwordx4 a[28:31], v12, s[40:43], s72 offen offset:1024 sc1\n"
        "s_waitcnt lgkmcnt(1)\n"
        "buffer_store_dwordx4 a[32:35], v12, s[40:43], s73 offen offset:1024 sc1\n"
        "s_waitcnt lgkmcnt(0)\n"
        "buffer_store_dwordx4 a[36:39], v12, s[40:43], s89 offen offset:1024 sc1\n"
        "s_waitcnt vmcnt(20)\n"
        "ds_read_b128 a[40:43], v9 offset:10240\n"
        "ds_read_b128 a[44:47], v9 offset:10304\n"
        "ds_read_b128 a[48:51], v9 offset:10368\n"
        "ds_read_b128 a[52:55], v9 offset:10432\n"
        "ds_read_b128 a[56:59], v9 offset:10496\n"
        "s_waitcnt lgkmcnt(4)\n"
        "buffer_store_dwordx4 a[40:43], v12, s[40:43], s70 offen offset:2048 sc1\n"
        "s_waitcnt lgkmcnt(3)\n"
        "buffer_store_dwordx4 a[44:47], v12, s[40:43], s71 offen offset:2048 sc1\n"
        "s_waitcnt lgkmcnt(2)\n"
        "buffer_store_dwordx4 a[48:51], v12, s[40:43], s72 offen offset:2048 sc1\n"
        "s_waitcnt lgkmcnt(1)\n"
        "buffer_store_dwordx4 a[52:55], v12, s[40:43], s73 offen offset:2048 sc1\n"
        "s_waitcnt lgkmcnt(0)\n"
        "buffer_store_dwordx4 a[56:59], v12, s[40:43], s89 offen offset:2048 sc1\n"
        "s_waitcnt vmcnt(20)\n"
        "ds_read_b128 a[60:63], v9 offset:15360\n"
        "ds_read_b128 a[64:67], v9 offset:15424\n"
        "ds_read_b128 a[68:71], v9 offset:15488\n"
        "ds_read_b128 a[72:75], v9 offset:15552\n"
        "ds_read_b128 a[76:79], v9 offset:15616\n"
        "s_waitcnt lgkmcnt(4)\n"
        "buffer_store_dwordx4 a[60:63], v12, s[40:43], s70 offen offset:3072 sc1\n"
        "s_waitcnt lgkmcnt(3)\n"
        "buffer_store_dwordx4 a[64:67], v12, s[40:43], s71 offen offset:3072 sc1\n"
        "s_waitcnt lgkmcnt(2)\n"
        "buffer_store_dwordx4 a[68:71], v12, s[40:43], s72 offen offset:3072 sc1\n"
        "s_waitcnt lgkmcnt(1)\n"
        "buffer_store_dwordx4 a[72:75], v12, s[40:43], s73 offen offset:3072 sc1\n"
        "s_waitcnt lgkmcnt(0)\n"
        "buffer_store_dwordx4 a[76:79], v12, s[40:43], s89 offen offset:3072 sc1\n"
        "s_waitcnt vmcnt(20)\n"
        "ds_read_b128 a[80:83], v9 offset:20480\n"
        "ds_read_b128 a[84:87], v9 offset:20544\n"
        "ds_read_b128 a[88:91], v9 offset:20608\n"
        "ds_read_b128 a[92:95], v9 offset:20672\n"
        "ds_read_b128 a[96:99], v9 offset:20736\n"
        "s_waitcnt lgkmcnt(4)\n"
        "buffer_store_dwordx4 a[80:83], v13, s[40:43], s70 offen offset:0 sc1\n"
        "s_waitcnt lgkmcnt(3)\n"
        "buffer_store_dwordx4 a[84:87], v13, s[40:43], s71 offen offset:0 sc1\n"
        "s_waitcnt lgkmcnt(2)\n"
        "buffer_store_dwordx4 a[88:91], v13, s[40:43], s72 offen offset:0 sc1\n"
        "s_waitcnt lgkmcnt(1)\n"
        "buffer_store_dwordx4 a[92:95], v13, s[40:43], s73 offen offset:0 sc1\n"
        "s_waitcnt lgkmcnt(0)\n"
        "buffer_store_dwordx4 a[96:99], v13, s[40:43], s89 offen offset:0 sc1\n"
        "s_waitcnt vmcnt(0)\n"
        "v_mov_b32 v240, s64\n"
        "v_mov_b32 v241, s65\n"
        "v_cmp_eq_u32 vcc, 0, v0\n"
        "v_mov_b32 v242, 0x40000000\n"
        "s_nop 0\n"
        "v_cndmask_b32 v242, v242, v0, vcc\n"
        "buffer_store_dwordx2 v[240:241], v242, s[40:43], s68 offen sc1\n"
        "s_barrier\n"
        "s_lshl_b32 s78, s3, 24\n"
        "v_lshlrev_b32 v120, 18, v0\n"
        "buffer_load_dword a196, v120, s[40:43], s78 offen\n"
        "buffer_load_dword a197, v120, s[36:39], s78 offen\n"
        "s_lshl_b32 s70, s2, 7\n"
        "s_add_u32 s70, s70, 11534336\n"
        "s_cmp_eq_u32 s3, 0\n"
        "s_cselect_b32 s70, s70, 0x40000000\n"
        "v_mov_b32 v120, s64\n"
        "v_mov_b32 v121, s65\n"
        "v_cmp_eq_u32 vcc, 0, v0\n"
        "v_mov_b32 v122, 0x40000000\n"
        "s_nop 0\n"
        "v_cndmask_b32 v122, v122, v0, vcc\n"
        "buffer_store_dwordx2 v[120:121], v122, s[40:43], s70 offen sc1\n"
        "s_cmp_eq_u32 s2, 255\n"
        "s_cbranch_scc1 L6_%=\n"
        "v_and_b32 v250, 7, v0\n"
        "v_lshlrev_b32 v4, 4, v250\n"
        "v_lshrrev_b32 v251, 3, v0\n"
        "v_mul_u32_u24 v5, 1280, v251\n"
        "v_add_u32 v5, v5, v4\n"
        "v_cmp_ne_u32 vcc, 0, v250\n"
        "v_lshlrev_b32 v6, 4, v251\n"
        "v_mov_b32 v250, 0x40000000\n"
        "v_cndmask_b32 v6, v6, v250, vcc\n"
        "buffer_load_dwordx4 v[176:179], v4, s[60:63], 0 offen offset:0\n"
        "buffer_load_dwordx4 v[180:183], v4, s[60:63], 0 offen offset:128\n"
        "buffer_load_dwordx4 v[184:187], v4, s[60:63], 0 offen offset:256\n"
        "buffer_load_dwordx4 v[188:191], v4, s[60:63], 0 offen offset:384\n"
        "buffer_load_dwordx4 v[192:195], v4, s[60:63], 0 offen offset:512\n"
        "buffer_load_dwordx4 v[196:199], v4, s[60:63], 0 offen offset:640\n"
        "buffer_load_dwordx4 v[200:203], v4, s[60:63], 0 offen offset:768\n"
        "buffer_load_dwordx4 v[204:207], v4, s[60:63], 0 offen offset:896\n"
        "buffer_load_dwordx4 v[208:211], v4, s[60:63], 0 offen offset:1024\n"
        "buffer_load_dwordx4 v[212:215], v4, s[60:63], 0 offen offset:1152\n"
        "s_cmp_eq_u32 s2, 247\n"
        "s_cselect_b32 s88, 2, 1\n"
        "s_lshl_b32 s87, s87, 7\n"
        "s_lshl_b32 s70, s3, 5\n"
        "s_add_u32 s87, s87, s70\n"
        "L7_%=:\n"
        "s_mul_i32 s70, s87, 1280\n"
        "buffer_load_dwordx4 v[16:19], v5, s[8:11], s70 offen offset:0\n"
        "buffer_load_dwordx4 v[20:23], v5, s[8:11], s70 offen offset:128\n"
        "buffer_load_dwordx4 v[24:27], v5, s[8:11], s70 offen offset:256\n"
        "buffer_load_dwordx4 v[28:31], v5, s[8:11], s70 offen offset:384\n"
        "buffer_load_dwordx4 v[32:35], v5, s[8:11], s70 offen offset:512\n"
        "buffer_load_dwordx4 v[36:39], v5, s[8:11], s70 offen offset:640\n"
        "buffer_load_dwordx4 v[40:43], v5, s[8:11], s70 offen offset:768\n"
        "buffer_load_dwordx4 v[44:47], v5, s[8:11], s70 offen offset:896\n"
        "buffer_load_dwordx4 v[48:51], v5, s[8:11], s70 offen offset:1024\n"
        "buffer_load_dwordx4 v[52:55], v5, s[8:11], s70 offen offset:1152\n"
        "s_add_u32 s70, s70, 10240\n"
        "buffer_load_dwordx4 v[56:59], v5, s[8:11], s70 offen offset:0\n"
        "buffer_load_dwordx4 v[60:63], v5, s[8:11], s70 offen offset:128\n"
        "buffer_load_dwordx4 v[64:67], v5, s[8:11], s70 offen offset:256\n"
        "buffer_load_dwordx4 v[68:71], v5, s[8:11], s70 offen offset:384\n"
        "buffer_load_dwordx4 v[72:75], v5, s[8:11], s70 offen offset:512\n"
        "buffer_load_dwordx4 v[76:79], v5, s[8:11], s70 offen offset:640\n"
        "buffer_load_dwordx4 v[80:83], v5, s[8:11], s70 offen offset:768\n"
        "buffer_load_dwordx4 v[84:87], v5, s[8:11], s70 offen offset:896\n"
        "buffer_load_dwordx4 v[88:91], v5, s[8:11], s70 offen offset:1024\n"
        "buffer_load_dwordx4 v[92:95], v5, s[8:11], s70 offen offset:1152\n"
        "s_add_u32 s70, s70, 10240\n"
        "buffer_load_dwordx4 v[96:99], v5, s[8:11], s70 offen offset:0\n"
        "buffer_load_dwordx4 v[100:103], v5, s[8:11], s70 offen offset:128\n"
        "buffer_load_dwordx4 v[104:107], v5, s[8:11], s70 offen offset:256\n"
        "buffer_load_dwordx4 v[108:111], v5, s[8:11], s70 offen offset:384\n"
        "buffer_load_dwordx4 v[112:115], v5, s[8:11], s70 offen offset:512\n"
        "buffer_load_dwordx4 v[116:119], v5, s[8:11], s70 offen offset:640\n"
        "buffer_load_dwordx4 v[120:123], v5, s[8:11], s70 offen offset:768\n"
        "buffer_load_dwordx4 v[124:127], v5, s[8:11], s70 offen offset:896\n"
        "buffer_load_dwordx4 v[128:131], v5, s[8:11], s70 offen offset:1024\n"
        "buffer_load_dwordx4 v[132:135], v5, s[8:11], s70 offen offset:1152\n"
        "s_add_u32 s70, s70, 10240\n"
        "buffer_load_dwordx4 v[136:139], v5, s[8:11], s70 offen offset:0\n"
        "buffer_load_dwordx4 v[140:143], v5, s[8:11], s70 offen offset:128\n"
        "buffer_load_dwordx4 v[144:147], v5, s[8:11], s70 offen offset:256\n"
        "buffer_load_dwordx4 v[148:151], v5, s[8:11], s70 offen offset:384\n"
        "buffer_load_dwordx4 v[152:155], v5, s[8:11], s70 offen offset:512\n"
        "buffer_load_dwordx4 v[156:159], v5, s[8:11], s70 offen offset:640\n"
        "buffer_load_dwordx4 v[160:163], v5, s[8:11], s70 offen offset:768\n"
        "buffer_load_dwordx4 v[164:167], v5, s[8:11], s70 offen offset:896\n"
        "buffer_load_dwordx4 v[168:171], v5, s[8:11], s70 offen offset:1024\n"
        "buffer_load_dwordx4 v[172:175], v5, s[8:11], s70 offen offset:1152\n"
        "s_lshl_b32 s71, s87, 4\n"
        "s_add_u32 s71, s71, 32768\n"
        "v_mov_b32 v216, 0\n"
        "v_mov_b32 v217, 0\n"
        "s_waitcnt vmcnt(39)\n"
        "v_dot2c_f32_bf16 v216, v16, v176\n"
        "v_dot2c_f32_bf16 v217, v17, v177\n"
        "v_dot2c_f32_bf16 v216, v18, v178\n"
        "v_dot2c_f32_bf16 v217, v19, v179\n"
        "s_waitcnt vmcnt(38)\n"
        "v_dot2c_f32_bf16 v216, v20, v180\n"
        "v_dot2c_f32_bf16 v217, v21, v181\n"
        "v_dot2c_f32_bf16 v216, v22, v182\n"
        "v_dot2c_f32_bf16 v217, v23, v183\n"
        "s_waitcnt vmcnt(37)\n"
        "v_dot2c_f32_bf16 v216, v24, v184\n"
        "v_dot2c_f32_bf16 v217, v25, v185\n"
        "v_dot2c_f32_bf16 v216, v26, v186\n"
        "v_dot2c_f32_bf16 v217, v27, v187\n"
        "s_waitcnt vmcnt(36)\n"
        "v_dot2c_f32_bf16 v216, v28, v188\n"
        "v_dot2c_f32_bf16 v217, v29, v189\n"
        "v_dot2c_f32_bf16 v216, v30, v190\n"
        "v_dot2c_f32_bf16 v217, v31, v191\n"
        "s_waitcnt vmcnt(35)\n"
        "v_dot2c_f32_bf16 v216, v32, v192\n"
        "v_dot2c_f32_bf16 v217, v33, v193\n"
        "v_dot2c_f32_bf16 v216, v34, v194\n"
        "v_dot2c_f32_bf16 v217, v35, v195\n"
        "s_waitcnt vmcnt(34)\n"
        "v_dot2c_f32_bf16 v216, v36, v196\n"
        "v_dot2c_f32_bf16 v217, v37, v197\n"
        "v_dot2c_f32_bf16 v216, v38, v198\n"
        "v_dot2c_f32_bf16 v217, v39, v199\n"
        "s_waitcnt vmcnt(33)\n"
        "v_dot2c_f32_bf16 v216, v40, v200\n"
        "v_dot2c_f32_bf16 v217, v41, v201\n"
        "v_dot2c_f32_bf16 v216, v42, v202\n"
        "v_dot2c_f32_bf16 v217, v43, v203\n"
        "s_waitcnt vmcnt(32)\n"
        "v_dot2c_f32_bf16 v216, v44, v204\n"
        "v_dot2c_f32_bf16 v217, v45, v205\n"
        "v_dot2c_f32_bf16 v216, v46, v206\n"
        "v_dot2c_f32_bf16 v217, v47, v207\n"
        "s_waitcnt vmcnt(31)\n"
        "v_dot2c_f32_bf16 v216, v48, v208\n"
        "v_dot2c_f32_bf16 v217, v49, v209\n"
        "v_dot2c_f32_bf16 v216, v50, v210\n"
        "v_dot2c_f32_bf16 v217, v51, v211\n"
        "s_waitcnt vmcnt(30)\n"
        "v_dot2c_f32_bf16 v216, v52, v212\n"
        "v_dot2c_f32_bf16 v217, v53, v213\n"
        "v_dot2c_f32_bf16 v216, v54, v214\n"
        "v_dot2c_f32_bf16 v217, v55, v215\n"
        "s_nop 2\n"
        "v_add_f32 v216, v216, v217\n"
        "s_nop 1\n"
        "v_add_f32_dpp v216, v216, v216 quad_perm:[1,0,3,2] row_mask:0xf bank_mask:0xf\n"
        "s_nop 1\n"
        "v_add_f32_dpp v216, v216, v216 quad_perm:[2,3,0,1] row_mask:0xf bank_mask:0xf\n"
        "s_nop 1\n"
        "v_add_f32_dpp v216, v216, v216 row_half_mirror row_mask:0xf bank_mask:0xf\n"
        "v_cvt_pk_bf16_f32 v216, v216, v216\n"
        "v_lshlrev_b32 v216, 16, v216\n"
        "v_mul_f32 v216, 0xbfb8aa3b, v216\n"
        "v_exp_f32 v216, v216\n"
        "s_nop 0\n"
        "v_add_f32 v216, 1.0, v216\n"
        "v_rcp_f32 v216, v216\n"
        "s_nop 0\n"
        "v_cvt_pk_bf16_f32 v216, v216, v216\n"
        "v_lshlrev_b32 v224, 16, v216\n"
        "v_sub_f32 v216, 1.0, v224\n"
        "v_cvt_pk_bf16_f32 v216, v216, v216\n"
        "v_lshlrev_b32 v226, 16, v216\n"
        "v_mov_b32 v225, s64\n"
        "v_mov_b32 v227, s65\n"
        "buffer_store_dwordx4 v[224:227], v6, s[40:43], s71 offen offset:0 sc1\n"
        "v_mov_b32 v218, 0\n"
        "v_mov_b32 v219, 0\n"
        "s_waitcnt vmcnt(30)\n"
        "v_dot2c_f32_bf16 v218, v56, v176\n"
        "v_dot2c_f32_bf16 v219, v57, v177\n"
        "v_dot2c_f32_bf16 v218, v58, v178\n"
        "v_dot2c_f32_bf16 v219, v59, v179\n"
        "s_waitcnt vmcnt(29)\n"
        "v_dot2c_f32_bf16 v218, v60, v180\n"
        "v_dot2c_f32_bf16 v219, v61, v181\n"
        "v_dot2c_f32_bf16 v218, v62, v182\n"
        "v_dot2c_f32_bf16 v219, v63, v183\n"
        "s_waitcnt vmcnt(28)\n"
        "v_dot2c_f32_bf16 v218, v64, v184\n"
        "v_dot2c_f32_bf16 v219, v65, v185\n"
        "v_dot2c_f32_bf16 v218, v66, v186\n"
        "v_dot2c_f32_bf16 v219, v67, v187\n"
        "s_waitcnt vmcnt(27)\n"
        "v_dot2c_f32_bf16 v218, v68, v188\n"
        "v_dot2c_f32_bf16 v219, v69, v189\n"
        "v_dot2c_f32_bf16 v218, v70, v190\n"
        "v_dot2c_f32_bf16 v219, v71, v191\n"
        "s_waitcnt vmcnt(26)\n"
        "v_dot2c_f32_bf16 v218, v72, v192\n"
        "v_dot2c_f32_bf16 v219, v73, v193\n"
        "v_dot2c_f32_bf16 v218, v74, v194\n"
        "v_dot2c_f32_bf16 v219, v75, v195\n"
        "s_waitcnt vmcnt(25)\n"
        "v_dot2c_f32_bf16 v218, v76, v196\n"
        "v_dot2c_f32_bf16 v219, v77, v197\n"
        "v_dot2c_f32_bf16 v218, v78, v198\n"
        "v_dot2c_f32_bf16 v219, v79, v199\n"
        "s_waitcnt vmcnt(24)\n"
        "v_dot2c_f32_bf16 v218, v80, v200\n"
        "v_dot2c_f32_bf16 v219, v81, v201\n"
        "v_dot2c_f32_bf16 v218, v82, v202\n"
        "v_dot2c_f32_bf16 v219, v83, v203\n"
        "s_waitcnt vmcnt(23)\n"
        "v_dot2c_f32_bf16 v218, v84, v204\n"
        "v_dot2c_f32_bf16 v219, v85, v205\n"
        "v_dot2c_f32_bf16 v218, v86, v206\n"
        "v_dot2c_f32_bf16 v219, v87, v207\n"
        "s_waitcnt vmcnt(22)\n"
        "v_dot2c_f32_bf16 v218, v88, v208\n"
        "v_dot2c_f32_bf16 v219, v89, v209\n"
        "v_dot2c_f32_bf16 v218, v90, v210\n"
        "v_dot2c_f32_bf16 v219, v91, v211\n"
        "s_waitcnt vmcnt(21)\n"
        "v_dot2c_f32_bf16 v218, v92, v212\n"
        "v_dot2c_f32_bf16 v219, v93, v213\n"
        "v_dot2c_f32_bf16 v218, v94, v214\n"
        "v_dot2c_f32_bf16 v219, v95, v215\n"
        "s_nop 2\n"
        "v_add_f32 v218, v218, v219\n"
        "s_nop 1\n"
        "v_add_f32_dpp v218, v218, v218 quad_perm:[1,0,3,2] row_mask:0xf bank_mask:0xf\n"
        "s_nop 1\n"
        "v_add_f32_dpp v218, v218, v218 quad_perm:[2,3,0,1] row_mask:0xf bank_mask:0xf\n"
        "s_nop 1\n"
        "v_add_f32_dpp v218, v218, v218 row_half_mirror row_mask:0xf bank_mask:0xf\n"
        "v_cvt_pk_bf16_f32 v218, v218, v218\n"
        "v_lshlrev_b32 v218, 16, v218\n"
        "v_mul_f32 v218, 0xbfb8aa3b, v218\n"
        "v_exp_f32 v218, v218\n"
        "s_nop 0\n"
        "v_add_f32 v218, 1.0, v218\n"
        "v_rcp_f32 v218, v218\n"
        "s_nop 0\n"
        "v_cvt_pk_bf16_f32 v218, v218, v218\n"
        "v_lshlrev_b32 v228, 16, v218\n"
        "v_sub_f32 v218, 1.0, v228\n"
        "v_cvt_pk_bf16_f32 v218, v218, v218\n"
        "v_lshlrev_b32 v230, 16, v218\n"
        "v_mov_b32 v229, s64\n"
        "v_mov_b32 v231, s65\n"
        "buffer_store_dwordx4 v[228:231], v6, s[40:43], s71 offen offset:128 sc1\n"
        "v_mov_b32 v220, 0\n"
        "v_mov_b32 v221, 0\n"
        "s_waitcnt vmcnt(21)\n"
        "v_dot2c_f32_bf16 v220, v96, v176\n"
        "v_dot2c_f32_bf16 v221, v97, v177\n"
        "v_dot2c_f32_bf16 v220, v98, v178\n"
        "v_dot2c_f32_bf16 v221, v99, v179\n"
        "s_waitcnt vmcnt(20)\n"
        "v_dot2c_f32_bf16 v220, v100, v180\n"
        "v_dot2c_f32_bf16 v221, v101, v181\n"
        "v_dot2c_f32_bf16 v220, v102, v182\n"
        "v_dot2c_f32_bf16 v221, v103, v183\n"
        "s_waitcnt vmcnt(19)\n"
        "v_dot2c_f32_bf16 v220, v104, v184\n"
        "v_dot2c_f32_bf16 v221, v105, v185\n"
        "v_dot2c_f32_bf16 v220, v106, v186\n"
        "v_dot2c_f32_bf16 v221, v107, v187\n"
        "s_waitcnt vmcnt(18)\n"
        "v_dot2c_f32_bf16 v220, v108, v188\n"
        "v_dot2c_f32_bf16 v221, v109, v189\n"
        "v_dot2c_f32_bf16 v220, v110, v190\n"
        "v_dot2c_f32_bf16 v221, v111, v191\n"
        "s_waitcnt vmcnt(17)\n"
        "v_dot2c_f32_bf16 v220, v112, v192\n"
        "v_dot2c_f32_bf16 v221, v113, v193\n"
        "v_dot2c_f32_bf16 v220, v114, v194\n"
        "v_dot2c_f32_bf16 v221, v115, v195\n"
        "s_waitcnt vmcnt(16)\n"
        "v_dot2c_f32_bf16 v220, v116, v196\n"
        "v_dot2c_f32_bf16 v221, v117, v197\n"
        "v_dot2c_f32_bf16 v220, v118, v198\n"
        "v_dot2c_f32_bf16 v221, v119, v199\n"
        "s_waitcnt vmcnt(15)\n"
        "v_dot2c_f32_bf16 v220, v120, v200\n"
        "v_dot2c_f32_bf16 v221, v121, v201\n"
        "v_dot2c_f32_bf16 v220, v122, v202\n"
        "v_dot2c_f32_bf16 v221, v123, v203\n"
        "s_waitcnt vmcnt(14)\n"
        "v_dot2c_f32_bf16 v220, v124, v204\n"
        "v_dot2c_f32_bf16 v221, v125, v205\n"
        "v_dot2c_f32_bf16 v220, v126, v206\n"
        "v_dot2c_f32_bf16 v221, v127, v207\n"
        "s_waitcnt vmcnt(13)\n"
        "v_dot2c_f32_bf16 v220, v128, v208\n"
        "v_dot2c_f32_bf16 v221, v129, v209\n"
        "v_dot2c_f32_bf16 v220, v130, v210\n"
        "v_dot2c_f32_bf16 v221, v131, v211\n"
        "s_waitcnt vmcnt(12)\n"
        "v_dot2c_f32_bf16 v220, v132, v212\n"
        "v_dot2c_f32_bf16 v221, v133, v213\n"
        "v_dot2c_f32_bf16 v220, v134, v214\n"
        "v_dot2c_f32_bf16 v221, v135, v215\n"
        "s_nop 2\n"
        "v_add_f32 v220, v220, v221\n"
        "s_nop 1\n"
        "v_add_f32_dpp v220, v220, v220 quad_perm:[1,0,3,2] row_mask:0xf bank_mask:0xf\n"
        "s_nop 1\n"
        "v_add_f32_dpp v220, v220, v220 quad_perm:[2,3,0,1] row_mask:0xf bank_mask:0xf\n"
        "s_nop 1\n"
        "v_add_f32_dpp v220, v220, v220 row_half_mirror row_mask:0xf bank_mask:0xf\n"
        "v_cvt_pk_bf16_f32 v220, v220, v220\n"
        "v_lshlrev_b32 v220, 16, v220\n"
        "v_mul_f32 v220, 0xbfb8aa3b, v220\n"
        "v_exp_f32 v220, v220\n"
        "s_nop 0\n"
        "v_add_f32 v220, 1.0, v220\n"
        "v_rcp_f32 v220, v220\n"
        "s_nop 0\n"
        "v_cvt_pk_bf16_f32 v220, v220, v220\n"
        "v_lshlrev_b32 v232, 16, v220\n"
        "v_sub_f32 v220, 1.0, v232\n"
        "v_cvt_pk_bf16_f32 v220, v220, v220\n"
        "v_lshlrev_b32 v234, 16, v220\n"
        "v_mov_b32 v233, s64\n"
        "v_mov_b32 v235, s65\n"
        "buffer_store_dwordx4 v[232:235], v6, s[40:43], s71 offen offset:256 sc1\n"
        "v_mov_b32 v222, 0\n"
        "v_mov_b32 v223, 0\n"
        "s_waitcnt vmcnt(12)\n"
        "v_dot2c_f32_bf16 v222, v136, v176\n"
        "v_dot2c_f32_bf16 v223, v137, v177\n"
        "v_dot2c_f32_bf16 v222, v138, v178\n"
        "v_dot2c_f32_bf16 v223, v139, v179\n"
        "s_waitcnt vmcnt(11)\n"
        "v_dot2c_f32_bf16 v222, v140, v180\n"
        "v_dot2c_f32_bf16 v223, v141, v181\n"
        "v_dot2c_f32_bf16 v222, v142, v182\n"
        "v_dot2c_f32_bf16 v223, v143, v183\n"
        "s_waitcnt vmcnt(10)\n"
        "v_dot2c_f32_bf16 v222, v144, v184\n"
        "v_dot2c_f32_bf16 v223, v145, v185\n"
        "v_dot2c_f32_bf16 v222, v146, v186\n"
        "v_dot2c_f32_bf16 v223, v147, v187\n"
        "s_waitcnt vmcnt(9)\n"
        "v_dot2c_f32_bf16 v222, v148, v188\n"
        "v_dot2c_f32_bf16 v223, v149, v189\n"
        "v_dot2c_f32_bf16 v222, v150, v190\n"
        "v_dot2c_f32_bf16 v223, v151, v191\n"
        "s_waitcnt vmcnt(8)\n"
        "v_dot2c_f32_bf16 v222, v152, v192\n"
        "v_dot2c_f32_bf16 v223, v153, v193\n"
        "v_dot2c_f32_bf16 v222, v154, v194\n"
        "v_dot2c_f32_bf16 v223, v155, v195\n"
        "s_waitcnt vmcnt(7)\n"
        "v_dot2c_f32_bf16 v222, v156, v196\n"
        "v_dot2c_f32_bf16 v223, v157, v197\n"
        "v_dot2c_f32_bf16 v222, v158, v198\n"
        "v_dot2c_f32_bf16 v223, v159, v199\n"
        "s_waitcnt vmcnt(6)\n"
        "v_dot2c_f32_bf16 v222, v160, v200\n"
        "v_dot2c_f32_bf16 v223, v161, v201\n"
        "v_dot2c_f32_bf16 v222, v162, v202\n"
        "v_dot2c_f32_bf16 v223, v163, v203\n"
        "s_waitcnt vmcnt(5)\n"
        "v_dot2c_f32_bf16 v222, v164, v204\n"
        "v_dot2c_f32_bf16 v223, v165, v205\n"
        "v_dot2c_f32_bf16 v222, v166, v206\n"
        "v_dot2c_f32_bf16 v223, v167, v207\n"
        "s_waitcnt vmcnt(4)\n"
        "v_dot2c_f32_bf16 v222, v168, v208\n"
        "v_dot2c_f32_bf16 v223, v169, v209\n"
        "v_dot2c_f32_bf16 v222, v170, v210\n"
        "v_dot2c_f32_bf16 v223, v171, v211\n"
        "s_waitcnt vmcnt(3)\n"
        "v_dot2c_f32_bf16 v222, v172, v212\n"
        "v_dot2c_f32_bf16 v223, v173, v213\n"
        "v_dot2c_f32_bf16 v222, v174, v214\n"
        "v_dot2c_f32_bf16 v223, v175, v215\n"
        "s_nop 2\n"
        "v_add_f32 v222, v222, v223\n"
        "s_nop 1\n"
        "v_add_f32_dpp v222, v222, v222 quad_perm:[1,0,3,2] row_mask:0xf bank_mask:0xf\n"
        "s_nop 1\n"
        "v_add_f32_dpp v222, v222, v222 quad_perm:[2,3,0,1] row_mask:0xf bank_mask:0xf\n"
        "s_nop 1\n"
        "v_add_f32_dpp v222, v222, v222 row_half_mirror row_mask:0xf bank_mask:0xf\n"
        "v_cvt_pk_bf16_f32 v222, v222, v222\n"
        "v_lshlrev_b32 v222, 16, v222\n"
        "v_mul_f32 v222, 0xbfb8aa3b, v222\n"
        "v_exp_f32 v222, v222\n"
        "s_nop 0\n"
        "v_add_f32 v222, 1.0, v222\n"
        "v_rcp_f32 v222, v222\n"
        "s_nop 0\n"
        "v_cvt_pk_bf16_f32 v222, v222, v222\n"
        "v_lshlrev_b32 v236, 16, v222\n"
        "v_sub_f32 v222, 1.0, v236\n"
        "v_cvt_pk_bf16_f32 v222, v222, v222\n"
        "v_lshlrev_b32 v238, 16, v222\n"
        "v_mov_b32 v237, s64\n"
        "v_mov_b32 v239, s65\n"
        "buffer_store_dwordx4 v[236:239], v6, s[40:43], s71 offen offset:384 sc1\n"
        "s_add_u32 s87, s87, 128\n"
        "s_sub_u32 s88, s88, 1\n"
        "s_cmp_lg_u32 s88, 0\n"
        "s_cbranch_scc1 L7_%=\n"
        "s_lshl_b32 s70, s2, 2\n"
        "s_add_u32 s70, s70, s3\n"
        "s_cmp_lg_u32 s70, 960\n"
        "s_cbranch_scc1 L8_%=\n"
        "v_lshlrev_b32 v250, 9, v0\n"
        "v_add_u32 v250, 11534336, v250\n"
        "buffer_load_dwordx2 v[16:17], v250, s[40:43], 0 offen offset:0 sc1\n"
        "buffer_load_dwordx2 v[18:19], v250, s[40:43], 0 offen offset:128 sc1\n"
        "buffer_load_dwordx2 v[20:21], v250, s[40:43], 0 offen offset:256 sc1\n"
        "buffer_load_dwordx2 v[22:23], v250, s[40:43], 0 offen offset:384 sc1\n"
        "s_waitcnt vmcnt(3)\n"
        "v_cmp_ne_u64 vcc, s[64:65], v[16:17]\n"
        "s_waitcnt vmcnt(2)\n"
        "v_cmp_ne_u64 s[78:79], s[64:65], v[18:19]\n"
        "s_or_b64 vcc, vcc, s[78:79]\n"
        "s_waitcnt vmcnt(1)\n"
        "v_cmp_ne_u64 s[78:79], s[64:65], v[20:21]\n"
        "s_or_b64 vcc, vcc, s[78:79]\n"
        "s_waitcnt vmcnt(0)\n"
        "v_cmp_ne_u64 s[78:79], s[64:65], v[22:23]\n"
        "s_or_b64 vcc, vcc, s[78:79]\n"
        "s_cbranch_vccz L10_%=\n"
        "s_nop 7\n"
        "s_nop 7\n"
        "s_nop 7\n"
        "s_waitcnt vmcnt(0) lgkmcnt(0)\n"
        "s_mov_b32 s6, 65536\n"
        "L9_%=:\n"
        "s_sleep 4\n"
        "buffer_load_dwordx2 v[16:17], v250, s[40:43], 0 offen offset:0 sc1\n"
        "buffer_load_dwordx2 v[18:19], v250, s[40:43], 0 offen offset:128 sc1\n"
        "buffer_load_dwordx2 v[20:21], v250, s[40:43], 0 offen offset:256 sc1\n"
        "buffer_load_dwordx2 v[22:23], v250, s[40:43], 0 offen offset:384 sc1\n"
        "s_waitcnt vmcnt(3)\n"
        "v_cmp_ne_u64 vcc, s[64:65], v[16:17]\n"
        "s_waitcnt vmcnt(2)\n"
        "v_cmp_ne_u64 s[78:79], s[64:65], v[18:19]\n"
        "s_or_b64 vcc, vcc, s[78:79]\n"
        "s_waitcnt vmcnt(1)\n"
        "v_cmp_ne_u64 s[78:79], s[64:65], v[20:21]\n"
        "s_or_b64 vcc, vcc, s[78:79]\n"
        "s_waitcnt vmcnt(0)\n"
        "v_cmp_ne_u64 s[78:79], s[64:65], v[22:23]\n"
        "s_or_b64 vcc, vcc, s[78:79]\n"
        "s_cbranch_vccz L10_%=\n"
        "s_sub_u32 s6, s6, 1\n"
        "s_cmp_lg_u32 s6, 0\n"
        "s_cbranch_scc1 L9_%=\n"
        "L10_%=:\n"
        "s_mov_b32 s70, 0\n"
        "v_mov_b32 v120, s64\n"
        "v_mov_b32 v121, s65\n"
        "v_cmp_eq_u32 vcc, 0, v0\n"
        "v_mov_b32 v122, 0x40000000\n"
        "s_nop 0\n"
        "v_cndmask_b32 v122, v122, v0, vcc\n"
        "buffer_store_dwordx2 v[120:121], v122, s[40:43], s70 offen sc1\n"
        "L8_%=:\n"
        "s_and_b32 s70, s5, 7\n"
        "s_lshl_b32 s70, s70, 2\n"
        "s_add_u32 s70, s70, s3\n"
        "s_lshl_b32 s70, s70, 8\n"
        "s_add_u32 s70, s70, 11632640\n"
        "v_and_b32 v250, 1, v0\n"
        "v_lshl_add_u32 v250, v250, 7, s70\n"
        "buffer_load_dwordx2 v[16:17], v250, s[40:43], 0 offen sc1\n"
        "s_waitcnt vmcnt(0)\n"
        "v_cmp_ne_u64 vcc, s[64:65], v[16:17]\n"
        "s_cbranch_vccz L12_%=\n"
        "s_nop 7\n"
        "s_nop 7\n"
        "s_nop 7\n"
        "s_waitcnt vmcnt(0) lgkmcnt(0)\n"
        "s_mov_b32 s6, 65536\n"
        "L11_%=:\n"
        "s_sleep 4\n"
        "buffer_load_dwordx2 v[16:17], v250, s[40:43], 0 offen sc1\n"
        "s_waitcnt vmcnt(0)\n"
        "v_cmp_ne_u64 vcc, s[64:65], v[16:17]\n"
        "s_cbranch_vccz L12_%=\n"
        "s_sub_u32 s6, s6, 1\n"
        "s_cmp_lg_u32 s6, 0\n"
        "s_cbranch_scc1 L11_%=\n"
        "L12_%=:\n"
        "s_add_u32 s84, s67, 0\n"
        "buffer_load_dwordx4 a[0:3], v12, s[40:43], s84 offen offset:0 sc1\n"
        "buffer_load_dwordx4 a[4:7], v12, s[40:43], s84 offen offset:1024 sc1\n"
        "buffer_load_dwordx4 a[8:11], v12, s[40:43], s84 offen offset:2048 sc1\n"
        "buffer_load_dwordx4 a[12:15], v12, s[40:43], s84 offen offset:3072 sc1\n"
        "buffer_load_dwordx4 a[16:19], v13, s[40:43], s84 offen offset:0 sc1\n"
        "buffer_load_dwordx4 a[20:23], v13, s[40:43], s84 offen offset:1024 sc1\n"
        "buffer_load_dwordx4 a[24:27], v13, s[40:43], s84 offen offset:2048 sc1\n"
        "buffer_load_dwordx4 a[28:31], v13, s[40:43], s84 offen offset:3072 sc1\n"
        "buffer_load_dwordx4 a[32:35], v14, s[40:43], s84 offen offset:0 sc1\n"
        "buffer_load_dwordx4 a[36:39], v14, s[40:43], s84 offen offset:1024 sc1\n"
        "s_add_u32 s85, s67, 10240\n"
        "buffer_load_dwordx4 a[40:43], v12, s[40:43], s85 offen offset:0 sc1\n"
        "buffer_load_dwordx4 a[44:47], v12, s[40:43], s85 offen offset:1024 sc1\n"
        "buffer_load_dwordx4 a[48:51], v12, s[40:43], s85 offen offset:2048 sc1\n"
        "buffer_load_dwordx4 a[52:55], v12, s[40:43], s85 offen offset:3072 sc1\n"
        "buffer_load_dwordx4 a[56:59], v13, s[40:43], s85 offen offset:0 sc1\n"
        "buffer_load_dwordx4 a[60:63], v13, s[40:43], s85 offen offset:1024 sc1\n"
        "buffer_load_dwordx4 a[64:67], v13, s[40:43], s85 offen offset:2048 sc1\n"
        "buffer_load_dwordx4 a[68:71], v13, s[40:43], s85 offen offset:3072 sc1\n"
        "buffer_load_dwordx4 a[72:75], v14, s[40:43], s85 offen offset:0 sc1\n"
        "buffer_load_dwordx4 a[76:79], v14, s[40:43], s85 offen offset:1024 sc1\n"
        "s_add_u32 s84, s67, 20480\n"
        "buffer_load_dwordx4 a[80:83], v12, s[40:43], s84 offen offset:0 sc1\n"
        "buffer_load_dwordx4 a[84:87], v12, s[40:43], s84 offen offset:1024 sc1\n"
        "buffer_load_dwordx4 a[88:91], v12, s[40:43], s84 offen offset:2048 sc1\n"
        "buffer_load_dwordx4 a[92:95], v12, s[40:43], s84 offen offset:3072 sc1\n"
        "s_mov_b32 s68, 0x40000000\n"
        "s_add_u32 s12, s3, 1\n"
        "s_and_b32 s12, s12, 3\n"
        "s_add_u32 s13, s3, 2\n"
        "s_and_b32 s13, s13, 3\n"
        "s_add_u32 s14, s3, 3\n"
        "s_and_b32 s14, s14, 3\n"
        "s_add_u32 s15, s3, 4\n"
        "s_and_b32 s15, s15, 3\n"
        "s_mul_i32 s70, s66, 163840\n"
        "s_mul_i32 s71, s3, 10240\n"
        "s_add_u32 s70, s70, s71\n"
        "s_add_u32 s70, s70, 0x600000\n"
        "s_mul_i32 s16, s12, 40960\n"
        "s_add_u32 s16, s16, s70\n"
        "s_mul_i32 s17, s13, 40960\n"
        "s_add_u32 s17, s17, s70\n"
        "s_mul_i32 s18, s14, 40960\n"
        "s_add_u32 s18, s18, s70\n"
        "s_mul_i32 s19, s15, 40960\n"
        "s_add_u32 s19, s19, s70\n"
        "s_mul_i32 s71, s3, 640\n"
        "s_lshl_b32 s48, s66, 12\n"
        "s_add_u32 s48, s48, s71\n"
        "s_add_u32 s48, s48, 11796480\n"
        "s_mov_b32 s7, 0x40000000\n"
        "s_branch G2_%=\n"
        "L6_%=:\n"
        "s_movk_i32 s91, 80\n"
        "s_branch FFT_%=\n"
        "G2_%=:\n"
        "s_add_u32 s84, s67, 20480\n"
        "buffer_load_dwordx4 a[96:99], v13, s[40:43], s84 offen offset:0 sc1\n"
        "v_add_u32 v10, s69, v1\n"
        "s_mul_i32 s70, s3, 80\n"
        "v_cvt_f32_u32 v10, v10\n"
        "v_lshl_add_u32 v11, v2, 2, s70\n"
        "v_add_f32 v178, 0x00000000, v10\n"
        "v_cvt_f32_u32 v11, v11\n"
        "v_mul_f32 v178, 0x3acccccd, v178\n"
        "v_add_f32 v186, 0x41800000, v10\n"
        "v_cos_f32 v156, v178\n"
        "v_sin_f32 v157, v178\n"
        "buffer_load_dwordx4 a[100:103], v13, s[40:43], s84 offen offset:1024 sc1\n"
        "v_mul_f32 v179, -v11, v178\n"
        "v_mul_f32 v178, 0x41800000, v178\n"
        "v_fract_f32 v179, v179\n"
        "v_cos_f32 v166, v178\n"
        "v_sin_f32 v167, v178\n"
        "v_add_f32 v178, 0x42000000, v10\n"
        "s_add_u32 s85, s67, 30720\n"
        "v_mul_f32 v186, 0x3acccccd, v186\n"
        "v_mul_f32 v178, 0x3acccccd, v178\n"
        "v_cos_f32 v116, v179\n"
        "buffer_load_dwordx4 a[104:107], v13, s[40:43], s84 offen offset:2048 sc1\n"
        "v_sin_f32 v117, v179\n"
        "v_cos_f32 v158, v186\n"
        "v_sin_f32 v159, v186\n"
        "v_mul_f32 v187, -v11, v186\n"
        "v_cos_f32 v160, v178\n"
        "v_sin_f32 v161, v178\n"
        "v_mul_f32 v179, -v11, v178\n"
        "buffer_load_dwordx4 a[108:111], v13, s[40:43], s84 offen offset:3072 sc1\n"
        "v_mul_f32 v186, 0x41800000, v186\n"
        "v_mul_f32 v178, 0x41800000, v178\n"
        "v_cos_f32 v168, v186\n"
        "v_sin_f32 v169, v186\n"
        "v_cos_f32 v170, v178\n"
        "v_sin_f32 v171, v178\n"
        "s_mul_i32 s70, s66, 86016\n"
        "s_mul_i32 s71, s3, 5120\n"
        "buffer_load_dwordx4 a[112:115], v14, s[40:43], s84 offen offset:0 sc1\n"
        "v_add_f32 v186, 0x42400000, v10\n"
        "v_add_f32 v178, 0x42800000, v10\n"
        "s_add_u32 s70, s70, s71\n"
        "v_fract_f32 v187, v187\n"
        "v_fract_f32 v179, v179\n"
        "v_mul_f32 v186, 0x3acccccd, v186\n"
        "v_mul_f32 v178, 0x3acccccd, v178\n"
        "s_add_u32 s70, s70, 0x300000\n"
        "s_lshl_b32 s71, s66, 10\n"
        "v_cos_f32 v124, v187\n"
        "v_sin_f32 v125, v187\n"
        "buffer_load_dwordx4 a[116:119], v14, s[40:43], s84 offen offset:1024 sc1\n"
        "v_cos_f32 v132, v179\n"
        "v_sin_f32 v133, v179\n"
        "s_mul_i32 s20, s12, 21504\n"
        "v_mul_f32 v187, -v11, v186\n"
        "v_mul_f32 v179, -v11, v178\n"
        "s_add_u32 s71, s71, 32768\n"
        "s_add_u32 s20, s20, s70\n"
        "s_lshl_b32 s24, s12, 8\n"
        "v_fract_f32 v187, v187\n"
        "v_fract_f32 v179, v179\n"
        "buffer_load_dwordx4 a[120:123], v12, s[40:43], s85 offen offset:0 sc1\n"
        "s_mul_i32 s28, s12, 21504\n"
        "s_mul_i32 s21, s13, 21504\n"
        "s_add_u32 s24, s24, s71\n"
        "s_add_u32 s28, s28, 84992\n"
        "v_pk_mul_f32 v[180:181], v[116:117], v[156:157] op_sel_hi:[1,0]\n"
        "v_cos_f32 v162, v186\n"
        "v_sin_f32 v163, v186\n"
        "v_cos_f32 v140, v187\n"
        "v_sin_f32 v141, v187\n"
        "v_cos_f32 v164, v178\n"
        "v_sin_f32 v165, v178\n"
        "v_cos_f32 v148, v179\n"
        "v_sin_f32 v149, v179\n"
        "s_add_u32 s21, s21, s70\n"
        "s_lshl_b32 s25, s13, 8\n"
        "s_mul_i32 s29, s13, 21504\n"
        "s_mul_i32 s22, s14, 21504\n"
        "v_pk_fma_f32 v[118:119], v[116:117], v[156:157], v[180:181] op_sel:[1,1,0] op_sel_hi:[0,1,1] neg_hi:[0,1,0]\n"
        "s_add_u32 s25, s25, s71\n"
        "v_pk_mul_f32 v[188:189], v[124:125], v[158:159] op_sel_hi:[1,0]\n"
        "v_pk_mul_f32 v[180:181], v[132:133], v[160:161] op_sel_hi:[1,0]\n"
        "s_add_u32 s29, s29, 79616\n"
        "s_add_u32 s22, s22, s70\n"
        "s_lshl_b32 s26, s14, 8\n"
        "s_mul_i32 s30, s14, 21504\n"
        "v_pk_mul_f32 v[182:183], v[118:119], v[156:157] op_sel_hi:[1,0]\n"
        "s_mul_i32 s23, s15, 21504\n"
        "v_pk_fma_f32 v[126:127], v[124:125], v[158:159], v[188:189] op_sel:[1,1,0] op_sel_hi:[0,1,1] neg_hi:[0,1,0]\n"
        "v_pk_fma_f32 v[134:135], v[132:133], v[160:161], v[180:181] op_sel:[1,1,0] op_sel_hi:[0,1,1] neg_hi:[0,1,0]\n"
        "v_pk_mul_f32 v[188:189], v[140:141], v[162:163] op_sel_hi:[1,0]\n"
        "v_pk_mul_f32 v[180:181], v[148:149], v[164:165] op_sel_hi:[1,0]\n"
        "s_add_u32 s26, s26, s71\n"
        "s_add_u32 s30, s30, 74240\n"
        "s_add_u32 s23, s23, s70\n"
        "v_pk_fma_f32 v[120:121], v[118:119], v[156:157], v[182:183] op_sel:[1,1,0] op_sel_hi:[0,1,1] neg_hi:[0,1,0]\n"
        "s_lshl_b32 s27, s15, 8\n"
        "v_pk_mul_f32 v[190:191], v[126:127], v[158:159] op_sel_hi:[1,0]\n"
        "v_pk_mul_f32 v[182:183], v[134:135], v[160:161] op_sel_hi:[1,0]\n"
        "s_mul_i32 s31, s15, 21504\n"
        "v_pk_fma_f32 v[142:143], v[140:141], v[162:163], v[188:189] op_sel:[1,1,0] op_sel_hi:[0,1,1] neg_hi:[0,1,0]\n"
        "v_pk_fma_f32 v[150:151], v[148:149], v[164:165], v[180:181] op_sel:[1,1,0] op_sel_hi:[0,1,1] neg_hi:[0,1,0]\n"
        "s_add_u32 s27, s27, s71\n"
        "v_pk_mul_f32 v[184:185], v[120:121], v[156:157] op_sel_hi:[1,0]\n"
        "s_add_u32 s31, s31, 90368\n"
        "v_pk_fma_f32 v[128:129], v[126:127], v[158:159], v[190:191] op_sel:[1,1,0] op_sel_hi:[0,1,1] neg_hi:[0,1,0]\n"
        "v_pk_fma_f32 v[136:137], v[134:135], v[160:161], v[182:183] op_sel:[1,1,0] op_sel_hi:[0,1,1] neg_hi:[0,1,0]\n"
        "s_lshl_b32 s70, s66, 2\n"
        "v_pk_mul_f32 v[190:191], v[142:143], v[162:163] op_sel_hi:[1,0]\n"
        "v_pk_mul_f32 v[182:183], v[150:151], v[164:165] op_sel_hi:[1,0]\n"
        "v_pk_fma_f32 v[122:123], v[120:121], v[156:157], v[184:185] op_sel:[1,1,0] op_sel_hi:[0,1,1] neg_hi:[0,1,0]\n"
        "s_add_u32 s70, s70, s3\n"
        "v_pk_mul_f32 v[176:177], v[128:129], v[158:159] op_sel_hi:[1,0]\n"
        "v_pk_mul_f32 v[184:185], v[136:137], v[160:161] op_sel_hi:[1,0]\n"
        "v_pk_fma_f32 v[144:145], v[142:143], v[162:163], v[190:191] op_sel:[1,1,0] op_sel_hi:[0,1,1] neg_hi:[0,1,0]\n"
        "v_pk_fma_f32 v[152:153], v[150:151], v[164:165], v[182:183] op_sel:[1,1,0] op_sel_hi:[0,1,1] neg_hi:[0,1,0]\n"
        "s_mul_i32 s44, s70, 20480\n"
        "s_lshl_b32 s45, s69, 1\n"
        "v_pk_fma_f32 v[130:131], v[128:129], v[158:159], v[176:177] op_sel:[1,1,0] op_sel_hi:[0,1,1] neg_hi:[0,1,0]\n"
        "v_pk_fma_f32 v[138:139], v[136:137], v[160:161], v[184:185] op_sel:[1,1,0] op_sel_hi:[0,1,1] neg_hi:[0,1,0]\n"
        "v_pk_mul_f32 v[176:177], v[144:145], v[162:163] op_sel_hi:[1,0]\n"
        "v_pk_mul_f32 v[184:185], v[152:153], v[164:165] op_sel_hi:[1,0]\n"
        "s_add_u32 s44, s44, s45\n"
        "s_mul_i32 s47, s70, 21504\n"
        "buffer_load_dwordx4 a[124:127], v12, s[40:43], s85 offen offset:1024 sc1\n"
        "v_pk_fma_f32 v[146:147], v[144:145], v[162:163], v[176:177] op_sel:[1,1,0] op_sel_hi:[0,1,1] neg_hi:[0,1,0]\n"
        "v_pk_fma_f32 v[154:155], v[152:153], v[164:165], v[184:185] op_sel:[1,1,0] op_sel_hi:[0,1,1] neg_hi:[0,1,0]\n"
        "s_lshl_b32 s46, s44, 1\n"
        "v_mul_f32 v186, 0x41800000, v186\n"
        "v_mul_f32 v178, 0x41800000, v178\n"
        "s_add_u32 s47, s47, 0x305000\n"
        "s_lshr_b32 s49, s66, 3\n"
        "v_cvt_pk_bf16_f32 v36, v116, v117\n"
        "v_cvt_pk_bf16_f32 v37, v118, v119\n"
        "v_cvt_pk_bf16_f32 v38, v120, v121\n"
        "v_cvt_pk_bf16_f32 v39, v122, v123\n"
        "v_cvt_pk_bf16_f32 v40, v124, v125\n"
        "v_cvt_pk_bf16_f32 v41, v126, v127\n"
        "v_cvt_pk_bf16_f32 v42, v128, v129\n"
        "v_cvt_pk_bf16_f32 v43, v130, v131\n"
        "v_cvt_pk_bf16_f32 v44, v132, v133\n"
        "v_cvt_pk_bf16_f32 v45, v134, v135\n"
        "v_cvt_pk_bf16_f32 v46, v136, v137\n"
        "v_cvt_pk_bf16_f32 v47, v138, v139\n"
        "v_cvt_pk_bf16_f32 v48, v140, v141\n"
        "v_cvt_pk_bf16_f32 v49, v142, v143\n"
        "v_cvt_pk_bf16_f32 v50, v144, v145\n"
        "v_cvt_pk_bf16_f32 v51, v146, v147\n"
        "v_cvt_pk_bf16_f32 v52, v148, v149\n"
        "v_cvt_pk_bf16_f32 v53, v150, v151\n"
        "v_cvt_pk_bf16_f32 v54, v152, v153\n"
        "v_cvt_pk_bf16_f32 v55, v154, v155\n"
        "v_min_u32 v6, 4, v0\n"
        "v_cos_f32 v172, v186\n"
        "v_sin_f32 v173, v186\n"
        "v_cos_f32 v174, v178\n"
        "v_sin_f32 v175, v178\n"
        "s_lshl_b32 s49, s49, 14\n"
        "v_accvgpr_write_b32 a176, v36\n"
        "v_accvgpr_write_b32 a177, v37\n"
        "v_accvgpr_write_b32 a178, v38\n"
        "v_accvgpr_write_b32 a179, v39\n"
        "v_accvgpr_write_b32 a180, v40\n"
        "v_accvgpr_write_b32 a181, v41\n"
        "v_accvgpr_write_b32 a182, v42\n"
        "v_accvgpr_write_b32 a183, v43\n"
        "v_accvgpr_write_b32 a184, v44\n"
        "v_accvgpr_write_b32 a185, v45\n"
        "v_accvgpr_write_b32 a186, v46\n"
        "v_accvgpr_write_b32 a187, v47\n"
        "v_accvgpr_write_b32 a188, v48\n"
        "v_accvgpr_write_b32 a189, v49\n"
        "v_accvgpr_write_b32 a190, v50\n"
        "v_accvgpr_write_b32 a191, v51\n"
        "v_accvgpr_write_b32 a192, v52\n"
        "v_accvgpr_write_b32 a193, v53\n"
        "v_accvgpr_write_b32 a194, v54\n"
        "v_accvgpr_write_b32 a195, v55\n"
        "s_add_u32 s49, s49, 11665408\n"
        "v_lshl_add_u32 v6, v6, 7, s48\n"
        "s_waitcnt vmcnt(8)\n"
        "v_mov_b32 v192, s64\n"
        "v_mov_b32 v193, s65\n"
        "v_cmp_eq_u32 vcc, 0, v0\n"
        "v_mov_b32 v194, 0x40000000\n"
        "s_nop 0\n"
        "v_cndmask_b32 v194, v194, v0, vcc\n"
        "buffer_store_dwordx2 v[192:193], v194, s[40:43], s68 offen sc1\n"
        "v_add_u32 v244, 0, v0\n"
        "s_mul_i32 s73, s3, 3072\n"
        "v_mul_u32_u24 v8, 205, v244\n"
        "s_add_u32 m0, s73, 40960\n"
        "v_lshrrev_b32 v8, 11, v8\n"
        "v_add_u32 v245, 64, v0\n"
        "v_mul_u32_u24 v8, 1120, v8\n"
        "v_add_u32 v246, 128, v0\n"
        "v_lshl_add_u32 v244, v244, 4, v8\n"
        "v_mul_u32_u24 v8, 205, v245\n"
        "buffer_load_dwordx4 v244, s[8:11], s44 offen offset:0 lds\n"
        "v_lshrrev_b32 v8, 11, v8\n"
        "v_cmp_gt_u32 vcc, 32, v0\n"
        "v_mul_u32_u24 v8, 1120, v8\n"
        "s_mul_i32 s72, s3, 3072\n"
        "v_lshl_add_u32 v245, v245, 4, v8\n"
        "v_mul_u32_u24 v8, 205, v246\n"
        "v_subrev_u32 v245, 1024, v245\n"
        "v_lshrrev_b32 v8, 11, v8\n"
        "s_add_u32 s72, s72, 40960\n"
        "v_mul_u32_u24 v8, 1120, v8\n"
        "buffer_load_dwordx4 v245, s[8:11], s44 offen offset:1024 lds\n"
        "v_lshl_add_u32 v246, v246, 4, v8\n"
        "v_mov_b32 v8, 0x40000000\n"
        "v_subrev_u32 v246, 2048, v246\n"
        "s_add_u32 s85, s67, 30720\n"
        "v_cndmask_b32 v246, v8, v246, vcc\n"
        "v_add_u32 v244, 0, v0\n"
        "s_movk_i32 s70, 2560\n"
        "v_mul_u32_u24 v236, 205, v244\n"
        "s_movk_i32 s71, 336\n"
        "v_lshrrev_b32 v236, 12, v236\n"
        "buffer_load_dwordx4 v246, s[8:11], s44 offen offset:2048 lds\n"
        "v_mul_u32_u24 v237, 20, v236\n"
        "v_add_u32 v245, 64, v0\n"
        "v_sub_u32 v244, v244, v237\n"
        "v_add_u32 v246, 128, v0\n"
        "v_lshlrev_b32 v244, 4, v244\n"
        "v_mul_u32_u24 v238, 205, v245\n"
        "v_mad_u32_u24 v249, v236, s70, v244\n"
        "v_mad_u32_u24 v244, v236, s71, v244\n"
        "v_mul_u32_u24 v236, 205, v246\n"
        "v_pk_mul_f32 v[186:187], v[116:117], v[166:167] op_sel_hi:[1,0]\n"
        "buffer_load_dwordx4 a[128:131], v12, s[40:43], s85 offen offset:2048 sc1\n"
        "v_pk_mul_f32 v[188:189], v[118:119], v[166:167] op_sel_hi:[1,0]\n"
        "v_pk_mul_f32 v[190:191], v[120:121], v[166:167] op_sel_hi:[1,0]\n"
        "v_pk_mul_f32 v[176:177], v[122:123], v[166:167] op_sel_hi:[1,0]\n"
        "v_pk_mul_f32 v[178:179], v[124:125], v[168:169] op_sel_hi:[1,0]\n"
        "v_pk_mul_f32 v[180:181], v[126:127], v[168:169] op_sel_hi:[1,0]\n"
        "v_pk_mul_f32 v[182:183], v[128:129], v[168:169] op_sel_hi:[1,0]\n"
        "v_pk_mul_f32 v[184:185], v[130:131], v[168:169] op_sel_hi:[1,0]\n"
        "v_lshrrev_b32 v238, 12, v238\n"
        "v_lshrrev_b32 v236, 12, v236\n"
        "v_pk_fma_f32 v[116:117], v[116:117], v[166:167], v[186:187] op_sel:[1,1,0] op_sel_hi:[0,1,1] neg_hi:[0,1,0]\n"
        "buffer_load_dwordx4 a[132:135], v12, s[40:43], s85 offen offset:3072 sc1\n"
        "v_pk_fma_f32 v[118:119], v[118:119], v[166:167], v[188:189] op_sel:[1,1,0] op_sel_hi:[0,1,1] neg_hi:[0,1,0]\n"
        "v_pk_fma_f32 v[120:121], v[120:121], v[166:167], v[190:191] op_sel:[1,1,0] op_sel_hi:[0,1,1] neg_hi:[0,1,0]\n"
        "v_pk_fma_f32 v[122:123], v[122:123], v[166:167], v[176:177] op_sel:[1,1,0] op_sel_hi:[0,1,1] neg_hi:[0,1,0]\n"
        "v_pk_fma_f32 v[124:125], v[124:125], v[168:169], v[178:179] op_sel:[1,1,0] op_sel_hi:[0,1,1] neg_hi:[0,1,0]\n"
        "v_pk_fma_f32 v[126:127], v[126:127], v[168:169], v[180:181] op_sel:[1,1,0] op_sel_hi:[0,1,1] neg_hi:[0,1,0]\n"
        "v_pk_fma_f32 v[128:129], v[128:129], v[168:169], v[182:183] op_sel:[1,1,0] op_sel_hi:[0,1,1] neg_hi:[0,1,0]\n"
        "v_pk_fma_f32 v[130:131], v[130:131], v[168:169], v[184:185] op_sel:[1,1,0] op_sel_hi:[0,1,1] neg_hi:[0,1,0]\n"
        "v_pk_mul_f32 v[186:187], v[132:133], v[170:171] op_sel_hi:[1,0]\n"
        "v_pk_mul_f32 v[188:189], v[134:135], v[170:171] op_sel_hi:[1,0]\n"
        "v_pk_mul_f32 v[190:191], v[136:137], v[170:171] op_sel_hi:[1,0]\n"
        "buffer_load_dwordx4 a[136:139], v13, s[40:43], s85 offen offset:0 sc1\n"
        "v_pk_mul_f32 v[176:177], v[138:139], v[170:171] op_sel_hi:[1,0]\n"
        "v_pk_mul_f32 v[178:179], v[140:141], v[172:173] op_sel_hi:[1,0]\n"
        "v_pk_mul_f32 v[180:181], v[142:143], v[172:173] op_sel_hi:[1,0]\n"
        "v_pk_mul_f32 v[182:183], v[144:145], v[172:173] op_sel_hi:[1,0]\n"
        "v_pk_mul_f32 v[184:185], v[146:147], v[172:173] op_sel_hi:[1,0]\n"
        "v_mul_u32_u24 v239, 20, v238\n"
        "v_mul_u32_u24 v237, 20, v236\n"
        "v_pk_fma_f32 v[132:133], v[132:133], v[170:171], v[186:187] op_sel:[1,1,0] op_sel_hi:[0,1,1] neg_hi:[0,1,0]\n"
        "v_pk_fma_f32 v[134:135], v[134:135], v[170:171], v[188:189] op_sel:[1,1,0] op_sel_hi:[0,1,1] neg_hi:[0,1,0]\n"
        "v_pk_fma_f32 v[136:137], v[136:137], v[170:171], v[190:191] op_sel:[1,1,0] op_sel_hi:[0,1,1] neg_hi:[0,1,0]\n"
        "buffer_load_dwordx4 a[140:143], v13, s[40:43], s85 offen offset:1024 sc1\n"
        "v_pk_fma_f32 v[138:139], v[138:139], v[170:171], v[176:177] op_sel:[1,1,0] op_sel_hi:[0,1,1] neg_hi:[0,1,0]\n"
        "v_pk_fma_f32 v[140:141], v[140:141], v[172:173], v[178:179] op_sel:[1,1,0] op_sel_hi:[0,1,1] neg_hi:[0,1,0]\n"
        "v_pk_fma_f32 v[142:143], v[142:143], v[172:173], v[180:181] op_sel:[1,1,0] op_sel_hi:[0,1,1] neg_hi:[0,1,0]\n"
        "v_pk_fma_f32 v[144:145], v[144:145], v[172:173], v[182:183] op_sel:[1,1,0] op_sel_hi:[0,1,1] neg_hi:[0,1,0]\n"
        "v_pk_fma_f32 v[146:147], v[146:147], v[172:173], v[184:185] op_sel:[1,1,0] op_sel_hi:[0,1,1] neg_hi:[0,1,0]\n"
        "v_pk_mul_f32 v[186:187], v[148:149], v[174:175] op_sel_hi:[1,0]\n"
        "v_pk_mul_f32 v[188:189], v[150:151], v[174:175] op_sel_hi:[1,0]\n"
        "v_pk_mul_f32 v[190:191], v[152:153], v[174:175] op_sel_hi:[1,0]\n"
        "v_pk_mul_f32 v[176:177], v[154:155], v[174:175] op_sel_hi:[1,0]\n"
        "v_pk_mul_f32 v[178:179], v[116:117], v[166:167] op_sel_hi:[1,0]\n"
        "buffer_load_dwordx4 a[144:147], v13, s[40:43], s85 offen offset:2048 sc1\n"
        "v_pk_mul_f32 v[180:181], v[118:119], v[166:167] op_sel_hi:[1,0]\n"
        "v_pk_mul_f32 v[182:183], v[120:121], v[166:167] op_sel_hi:[1,0]\n"
        "v_pk_mul_f32 v[184:185], v[122:123], v[166:167] op_sel_hi:[1,0]\n"
        "v_sub_u32 v245, v245, v239\n"
        "v_sub_u32 v246, v246, v237\n"
        "v_cvt_pk_bf16_f32 v64, v116, v117\n"
        "v_cvt_pk_bf16_f32 v65, v118, v119\n"
        "v_cvt_pk_bf16_f32 v66, v120, v121\n"
        "v_cvt_pk_bf16_f32 v67, v122, v123\n"
        "v_pk_fma_f32 v[148:149], v[148:149], v[174:175], v[186:187] op_sel:[1,1,0] op_sel_hi:[0,1,1] neg_hi:[0,1,0]\n"
        "buffer_load_dwordx4 a[148:151], v13, s[40:43], s85 offen offset:3072 sc1\n"
        "v_pk_fma_f32 v[150:151], v[150:151], v[174:175], v[188:189] op_sel:[1,1,0] op_sel_hi:[0,1,1] neg_hi:[0,1,0]\n"
        "v_pk_fma_f32 v[152:153], v[152:153], v[174:175], v[190:191] op_sel:[1,1,0] op_sel_hi:[0,1,1] neg_hi:[0,1,0]\n"
        "v_pk_fma_f32 v[154:155], v[154:155], v[174:175], v[176:177] op_sel:[1,1,0] op_sel_hi:[0,1,1] neg_hi:[0,1,0]\n"
        "v_pk_fma_f32 v[116:117], v[116:117], v[166:167], v[178:179] op_sel:[1,1,0] op_sel_hi:[0,1,1] neg_hi:[0,1,0]\n"
        "v_pk_fma_f32 v[118:119], v[118:119], v[166:167], v[180:181] op_sel:[1,1,0] op_sel_hi:[0,1,1] neg_hi:[0,1,0]\n"
        "v_pk_fma_f32 v[120:121], v[120:121], v[166:167], v[182:183] op_sel:[1,1,0] op_sel_hi:[0,1,1] neg_hi:[0,1,0]\n"
        "v_pk_fma_f32 v[122:123], v[122:123], v[166:167], v[184:185] op_sel:[1,1,0] op_sel_hi:[0,1,1] neg_hi:[0,1,0]\n"
        "v_lshlrev_b32 v245, 4, v245\n"
        "v_lshlrev_b32 v246, 4, v246\n"
        "v_pk_mul_f32 v[186:187], v[124:125], v[168:169] op_sel_hi:[1,0]\n"
        "buffer_load_dwordx4 a[152:155], v14, s[40:43], s85 offen offset:0 sc1\n"
        "v_pk_mul_f32 v[188:189], v[126:127], v[168:169] op_sel_hi:[1,0]\n"
        "v_pk_mul_f32 v[190:191], v[128:129], v[168:169] op_sel_hi:[1,0]\n"
        "v_pk_mul_f32 v[176:177], v[130:131], v[168:169] op_sel_hi:[1,0]\n"
        "v_pk_mul_f32 v[178:179], v[132:133], v[170:171] op_sel_hi:[1,0]\n"
        "v_pk_mul_f32 v[180:181], v[134:135], v[170:171] op_sel_hi:[1,0]\n"
        "v_pk_mul_f32 v[182:183], v[136:137], v[170:171] op_sel_hi:[1,0]\n"
        "v_pk_mul_f32 v[184:185], v[138:139], v[170:171] op_sel_hi:[1,0]\n"
        "v_add_u32 v247, 192, v0\n"
        "v_add_u32 v248, 256, v0\n"
        "v_cvt_pk_bf16_f32 v68, v124, v125\n"
        "v_cvt_pk_bf16_f32 v69, v126, v127\n"
        "v_cvt_pk_bf16_f32 v70, v128, v129\n"
        "v_cvt_pk_bf16_f32 v71, v130, v131\n"
        "v_cvt_pk_bf16_f32 v72, v132, v133\n"
        "v_cvt_pk_bf16_f32 v73, v134, v135\n"
        "v_cvt_pk_bf16_f32 v74, v136, v137\n"
        "v_cvt_pk_bf16_f32 v75, v138, v139\n"
        "v_mad_u32_u24 v250, v238, s70, v245\n"
        "v_mad_u32_u24 v251, v236, s70, v246\n"
        "v_pk_fma_f32 v[124:125], v[124:125], v[168:169], v[186:187] op_sel:[1,1,0] op_sel_hi:[0,1,1] neg_hi:[0,1,0]\n"
        "v_pk_fma_f32 v[126:127], v[126:127], v[168:169], v[188:189] op_sel:[1,1,0] op_sel_hi:[0,1,1] neg_hi:[0,1,0]\n"
        "v_pk_fma_f32 v[128:129], v[128:129], v[168:169], v[190:191] op_sel:[1,1,0] op_sel_hi:[0,1,1] neg_hi:[0,1,0]\n"
        "v_pk_fma_f32 v[130:131], v[130:131], v[168:169], v[176:177] op_sel:[1,1,0] op_sel_hi:[0,1,1] neg_hi:[0,1,0]\n"
        "v_pk_fma_f32 v[132:133], v[132:133], v[170:171], v[178:179] op_sel:[1,1,0] op_sel_hi:[0,1,1] neg_hi:[0,1,0]\n"
        "v_pk_fma_f32 v[134:135], v[134:135], v[170:171], v[180:181] op_sel:[1,1,0] op_sel_hi:[0,1,1] neg_hi:[0,1,0]\n"
        "v_pk_fma_f32 v[136:137], v[136:137], v[170:171], v[182:183] op_sel:[1,1,0] op_sel_hi:[0,1,1] neg_hi:[0,1,0]\n"
        "v_pk_fma_f32 v[138:139], v[138:139], v[170:171], v[184:185] op_sel:[1,1,0] op_sel_hi:[0,1,1] neg_hi:[0,1,0]\n"
        "v_mad_u32_u24 v245, v238, s71, v245\n"
        "v_mad_u32_u24 v246, v236, s71, v246\n"
        "v_pk_mul_f32 v[186:187], v[140:141], v[172:173] op_sel_hi:[1,0]\n"
        "v_pk_mul_f32 v[188:189], v[142:143], v[172:173] op_sel_hi:[1,0]\n"
        "v_pk_mul_f32 v[190:191], v[144:145], v[172:173] op_sel_hi:[1,0]\n"
        "v_pk_mul_f32 v[176:177], v[146:147], v[172:173] op_sel_hi:[1,0]\n"
        "v_pk_mul_f32 v[178:179], v[148:149], v[174:175] op_sel_hi:[1,0]\n"
        "v_pk_mul_f32 v[180:181], v[150:151], v[174:175] op_sel_hi:[1,0]\n"
        "v_pk_mul_f32 v[182:183], v[152:153], v[174:175] op_sel_hi:[1,0]\n"
        "v_pk_mul_f32 v[184:185], v[154:155], v[174:175] op_sel_hi:[1,0]\n"
        "v_mul_u32_u24 v238, 205, v247\n"
        "v_mul_u32_u24 v236, 205, v248\n"
        "v_cvt_pk_bf16_f32 v76, v140, v141\n"
        "v_cvt_pk_bf16_f32 v77, v142, v143\n"
        "v_cvt_pk_bf16_f32 v78, v144, v145\n"
        "v_cvt_pk_bf16_f32 v79, v146, v147\n"
        "v_cvt_pk_bf16_f32 v80, v148, v149\n"
        "v_cvt_pk_bf16_f32 v81, v150, v151\n"
        "v_cvt_pk_bf16_f32 v82, v152, v153\n"
        "v_cvt_pk_bf16_f32 v83, v154, v155\n"
        "v_pk_fma_f32 v[140:141], v[140:141], v[172:173], v[186:187] op_sel:[1,1,0] op_sel_hi:[0,1,1] neg_hi:[0,1,0]\n"
        "v_pk_fma_f32 v[142:143], v[142:143], v[172:173], v[188:189] op_sel:[1,1,0] op_sel_hi:[0,1,1] neg_hi:[0,1,0]\n"
        "v_pk_fma_f32 v[144:145], v[144:145], v[172:173], v[190:191] op_sel:[1,1,0] op_sel_hi:[0,1,1] neg_hi:[0,1,0]\n"
        "v_pk_fma_f32 v[146:147], v[146:147], v[172:173], v[176:177] op_sel:[1,1,0] op_sel_hi:[0,1,1] neg_hi:[0,1,0]\n"
        "v_pk_fma_f32 v[148:149], v[148:149], v[174:175], v[178:179] op_sel:[1,1,0] op_sel_hi:[0,1,1] neg_hi:[0,1,0]\n"
        "v_pk_fma_f32 v[150:151], v[150:151], v[174:175], v[180:181] op_sel:[1,1,0] op_sel_hi:[0,1,1] neg_hi:[0,1,0]\n"
        "v_pk_fma_f32 v[152:153], v[152:153], v[174:175], v[182:183] op_sel:[1,1,0] op_sel_hi:[0,1,1] neg_hi:[0,1,0]\n"
        "v_pk_fma_f32 v[154:155], v[154:155], v[174:175], v[184:185] op_sel:[1,1,0] op_sel_hi:[0,1,1] neg_hi:[0,1,0]\n"
        "v_lshrrev_b32 v238, 12, v238\n"
        "v_lshrrev_b32 v236, 12, v236\n"
        "v_pk_mul_f32 v[186:187], v[116:117], v[166:167] op_sel_hi:[1,0]\n"
        "v_pk_mul_f32 v[188:189], v[118:119], v[166:167] op_sel_hi:[1,0]\n"
        "v_pk_mul_f32 v[190:191], v[120:121], v[166:167] op_sel_hi:[1,0]\n"
        "v_pk_mul_f32 v[176:177], v[122:123], v[166:167] op_sel_hi:[1,0]\n"
        "v_pk_mul_f32 v[178:179], v[124:125], v[168:169] op_sel_hi:[1,0]\n"
        "v_pk_mul_f32 v[180:181], v[126:127], v[168:169] op_sel_hi:[1,0]\n"
        "v_pk_mul_f32 v[182:183], v[128:129], v[168:169] op_sel_hi:[1,0]\n"
        "v_pk_mul_f32 v[184:185], v[130:131], v[168:169] op_sel_hi:[1,0]\n"
        "v_cvt_pk_bf16_f32 v84, v116, v117\n"
        "v_cvt_pk_bf16_f32 v85, v118, v119\n"
        "v_cvt_pk_bf16_f32 v86, v120, v121\n"
        "v_cvt_pk_bf16_f32 v87, v122, v123\n"
        "v_cvt_pk_bf16_f32 v88, v124, v125\n"
        "v_cvt_pk_bf16_f32 v89, v126, v127\n"
        "v_cvt_pk_bf16_f32 v90, v128, v129\n"
        "v_cvt_pk_bf16_f32 v91, v130, v131\n"
        "v_mul_u32_u24 v239, 20, v238\n"
        "v_mul_u32_u24 v237, 20, v236\n"
        "v_pk_fma_f32 v[116:117], v[116:117], v[166:167], v[186:187] op_sel:[1,1,0] op_sel_hi:[0,1,1] neg_hi:[0,1,0]\n"
        "v_pk_fma_f32 v[118:119], v[118:119], v[166:167], v[188:189] op_sel:[1,1,0] op_sel_hi:[0,1,1] neg_hi:[0,1,0]\n"
        "v_pk_fma_f32 v[120:121], v[120:121], v[166:167], v[190:191] op_sel:[1,1,0] op_sel_hi:[0,1,1] neg_hi:[0,1,0]\n"
        "v_pk_fma_f32 v[122:123], v[122:123], v[166:167], v[176:177] op_sel:[1,1,0] op_sel_hi:[0,1,1] neg_hi:[0,1,0]\n"
        "v_pk_fma_f32 v[124:125], v[124:125], v[168:169], v[178:179] op_sel:[1,1,0] op_sel_hi:[0,1,1] neg_hi:[0,1,0]\n"
        "v_pk_fma_f32 v[126:127], v[126:127], v[168:169], v[180:181] op_sel:[1,1,0] op_sel_hi:[0,1,1] neg_hi:[0,1,0]\n"
        "v_pk_fma_f32 v[128:129], v[128:129], v[168:169], v[182:183] op_sel:[1,1,0] op_sel_hi:[0,1,1] neg_hi:[0,1,0]\n"
        "v_pk_fma_f32 v[130:131], v[130:131], v[168:169], v[184:185] op_sel:[1,1,0] op_sel_hi:[0,1,1] neg_hi:[0,1,0]\n"
        "v_pk_mul_f32 v[186:187], v[132:133], v[170:171] op_sel_hi:[1,0]\n"
        "v_pk_mul_f32 v[188:189], v[134:135], v[170:171] op_sel_hi:[1,0]\n"
        "v_pk_mul_f32 v[190:191], v[136:137], v[170:171] op_sel_hi:[1,0]\n"
        "v_pk_mul_f32 v[176:177], v[138:139], v[170:171] op_sel_hi:[1,0]\n"
        "v_pk_mul_f32 v[178:179], v[140:141], v[172:173] op_sel_hi:[1,0]\n"
        "v_pk_mul_f32 v[180:181], v[142:143], v[172:173] op_sel_hi:[1,0]\n"
        "v_pk_mul_f32 v[182:183], v[144:145], v[172:173] op_sel_hi:[1,0]\n"
        "v_pk_mul_f32 v[184:185], v[146:147], v[172:173] op_sel_hi:[1,0]\n"
        "v_sub_u32 v247, v247, v239\n"
        "v_sub_u32 v248, v248, v237\n"
        "v_cvt_pk_bf16_f32 v92, v132, v133\n"
        "v_cvt_pk_bf16_f32 v93, v134, v135\n"
        "v_cvt_pk_bf16_f32 v94, v136, v137\n"
        "v_cvt_pk_bf16_f32 v95, v138, v139\n"
        "v_cvt_pk_bf16_f32 v96, v140, v141\n"
        "v_cvt_pk_bf16_f32 v97, v142, v143\n"
        "v_cvt_pk_bf16_f32 v98, v144, v145\n"
        "v_cvt_pk_bf16_f32 v99, v146, v147\n"
        "v_pk_fma_f32 v[132:133], v[132:133], v[170:171], v[186:187] op_sel:[1,1,0] op_sel_hi:[0,1,1] neg_hi:[0,1,0]\n"
        "v_pk_fma_f32 v[134:135], v[134:135], v[170:171], v[188:189] op_sel:[1,1,0] op_sel_hi:[0,1,1] neg_hi:[0,1,0]\n"
        "v_pk_fma_f32 v[136:137], v[136:137], v[170:171], v[190:191] op_sel:[1,1,0] op_sel_hi:[0,1,1] neg_hi:[0,1,0]\n"
        "v_pk_fma_f32 v[138:139], v[138:139], v[170:171], v[176:177] op_sel:[1,1,0] op_sel_hi:[0,1,1] neg_hi:[0,1,0]\n"
        "v_pk_fma_f32 v[140:141], v[140:141], v[172:173], v[178:179] op_sel:[1,1,0] op_sel_hi:[0,1,1] neg_hi:[0,1,0]\n"
        "v_pk_fma_f32 v[142:143], v[142:143], v[172:173], v[180:181] op_sel:[1,1,0] op_sel_hi:[0,1,1] neg_hi:[0,1,0]\n"
        "v_pk_fma_f32 v[144:145], v[144:145], v[172:173], v[182:183] op_sel:[1,1,0] op_sel_hi:[0,1,1] neg_hi:[0,1,0]\n"
        "v_pk_fma_f32 v[146:147], v[146:147], v[172:173], v[184:185] op_sel:[1,1,0] op_sel_hi:[0,1,1] neg_hi:[0,1,0]\n"
        "v_pk_mul_f32 v[186:187], v[148:149], v[174:175] op_sel_hi:[1,0]\n"
        "v_pk_mul_f32 v[188:189], v[150:151], v[174:175] op_sel_hi:[1,0]\n"
        "v_pk_mul_f32 v[190:191], v[152:153], v[174:175] op_sel_hi:[1,0]\n"
        "v_pk_mul_f32 v[176:177], v[154:155], v[174:175] op_sel_hi:[1,0]\n"
        "v_pk_mul_f32 v[178:179], v[116:117], v[166:167] op_sel_hi:[1,0]\n"
        "v_pk_mul_f32 v[180:181], v[118:119], v[166:167] op_sel_hi:[1,0]\n"
        "v_pk_mul_f32 v[182:183], v[120:121], v[166:167] op_sel_hi:[1,0]\n"
        "v_pk_mul_f32 v[184:185], v[122:123], v[166:167] op_sel_hi:[1,0]\n"
        "v_lshlrev_b32 v247, 4, v247\n"
        "v_lshlrev_b32 v248, 4, v248\n"
        "v_cvt_pk_bf16_f32 v100, v148, v149\n"
        "v_cvt_pk_bf16_f32 v101, v150, v151\n"
        "v_cvt_pk_bf16_f32 v102, v152, v153\n"
        "v_cvt_pk_bf16_f32 v103, v154, v155\n"
        "v_cvt_pk_bf16_f32 v36, v116, v117\n"
        "v_cvt_pk_bf16_f32 v37, v118, v119\n"
        "v_cvt_pk_bf16_f32 v38, v120, v121\n"
        "v_cvt_pk_bf16_f32 v39, v122, v123\n"
        "v_pk_fma_f32 v[148:149], v[148:149], v[174:175], v[186:187] op_sel:[1,1,0] op_sel_hi:[0,1,1] neg_hi:[0,1,0]\n"
        "v_pk_fma_f32 v[150:151], v[150:151], v[174:175], v[188:189] op_sel:[1,1,0] op_sel_hi:[0,1,1] neg_hi:[0,1,0]\n"
        "v_pk_fma_f32 v[152:153], v[152:153], v[174:175], v[190:191] op_sel:[1,1,0] op_sel_hi:[0,1,1] neg_hi:[0,1,0]\n"
        "v_pk_fma_f32 v[154:155], v[154:155], v[174:175], v[176:177] op_sel:[1,1,0] op_sel_hi:[0,1,1] neg_hi:[0,1,0]\n"
        "v_pk_fma_f32 v[116:117], v[116:117], v[166:167], v[178:179] op_sel:[1,1,0] op_sel_hi:[0,1,1] neg_hi:[0,1,0]\n"
        "v_pk_fma_f32 v[118:119], v[118:119], v[166:167], v[180:181] op_sel:[1,1,0] op_sel_hi:[0,1,1] neg_hi:[0,1,0]\n"
        "v_pk_fma_f32 v[120:121], v[120:121], v[166:167], v[182:183] op_sel:[1,1,0] op_sel_hi:[0,1,1] neg_hi:[0,1,0]\n"
        "v_pk_fma_f32 v[122:123], v[122:123], v[166:167], v[184:185] op_sel:[1,1,0] op_sel_hi:[0,1,1] neg_hi:[0,1,0]\n"
        "v_pk_mul_f32 v[186:187], v[124:125], v[168:169] op_sel_hi:[1,0]\n"
        "v_pk_mul_f32 v[188:189], v[126:127], v[168:169] op_sel_hi:[1,0]\n"
        "v_pk_mul_f32 v[190:191], v[128:129], v[168:169] op_sel_hi:[1,0]\n"
        "v_pk_mul_f32 v[176:177], v[130:131], v[168:169] op_sel_hi:[1,0]\n"
        "v_pk_mul_f32 v[178:179], v[132:133], v[170:171] op_sel_hi:[1,0]\n"
        "v_pk_mul_f32 v[180:181], v[134:135], v[170:171] op_sel_hi:[1,0]\n"
        "v_pk_mul_f32 v[182:183], v[136:137], v[170:171] op_sel_hi:[1,0]\n"
        "v_pk_mul_f32 v[184:185], v[138:139], v[170:171] op_sel_hi:[1,0]\n"
        "v_mad_u32_u24 v252, v238, s70, v247\n"
        "v_mad_u32_u24 v253, v236, s70, v248\n"
        "v_min_u32 v7, 20, v0\n"
        "s_mul_i32 s70, s3, 20\n"
        "buffer_load_dwordx4 a[156:159], v14, s[40:43], s85 offen offset:1024 sc1\n"
        "v_cvt_pk_bf16_f32 v40, v124, v125\n"
        "v_cvt_pk_bf16_f32 v41, v126, v127\n"
        "v_cvt_pk_bf16_f32 v42, v128, v129\n"
        "v_cvt_pk_bf16_f32 v43, v130, v131\n"
        "v_cvt_pk_bf16_f32 v44, v132, v133\n"
        "v_cvt_pk_bf16_f32 v45, v134, v135\n"
        "v_cvt_pk_bf16_f32 v46, v136, v137\n"
        "v_cvt_pk_bf16_f32 v47, v138, v139\n"
        "v_pk_fma_f32 v[124:125], v[124:125], v[168:169], v[186:187] op_sel:[1,1,0] op_sel_hi:[0,1,1] neg_hi:[0,1,0]\n"
        "v_pk_fma_f32 v[126:127], v[126:127], v[168:169], v[188:189] op_sel:[1,1,0] op_sel_hi:[0,1,1] neg_hi:[0,1,0]\n"
        "v_pk_fma_f32 v[128:129], v[128:129], v[168:169], v[190:191] op_sel:[1,1,0] op_sel_hi:[0,1,1] neg_hi:[0,1,0]\n"
        "v_pk_fma_f32 v[130:131], v[130:131], v[168:169], v[176:177] op_sel:[1,1,0] op_sel_hi:[0,1,1] neg_hi:[0,1,0]\n"
        "v_pk_fma_f32 v[132:133], v[132:133], v[170:171], v[178:179] op_sel:[1,1,0] op_sel_hi:[0,1,1] neg_hi:[0,1,0]\n"
        "v_pk_fma_f32 v[134:135], v[134:135], v[170:171], v[180:181] op_sel:[1,1,0] op_sel_hi:[0,1,1] neg_hi:[0,1,0]\n"
        "v_pk_fma_f32 v[136:137], v[136:137], v[170:171], v[182:183] op_sel:[1,1,0] op_sel_hi:[0,1,1] neg_hi:[0,1,0]\n"
        "v_pk_fma_f32 v[138:139], v[138:139], v[170:171], v[184:185] op_sel:[1,1,0] op_sel_hi:[0,1,1] neg_hi:[0,1,0]\n"
        "v_mad_u32_u24 v248, v236, s71, v248\n"
        "v_pk_mul_f32 v[186:187], v[140:141], v[172:173] op_sel_hi:[1,0]\n"
        "v_pk_mul_f32 v[188:189], v[142:143], v[172:173] op_sel_hi:[1,0]\n"
        "v_pk_mul_f32 v[190:191], v[144:145], v[172:173] op_sel_hi:[1,0]\n"
        "v_pk_mul_f32 v[176:177], v[146:147], v[172:173] op_sel_hi:[1,0]\n"
        "v_pk_mul_f32 v[178:179], v[148:149], v[174:175] op_sel_hi:[1,0]\n"
        "v_pk_mul_f32 v[180:181], v[150:151], v[174:175] op_sel_hi:[1,0]\n"
        "v_pk_mul_f32 v[182:183], v[152:153], v[174:175] op_sel_hi:[1,0]\n"
        "v_pk_mul_f32 v[184:185], v[154:155], v[174:175] op_sel_hi:[1,0]\n"
        "v_add_u32 v236, s70, v7\n"
        "v_cmp_eq_u32 vcc, 20, v7\n"
        "v_mov_b32 v237, 80\n"
        "s_mul_i32 s73, s3, 21504\n"
        "v_cvt_pk_bf16_f32 v48, v140, v141\n"
        "v_cvt_pk_bf16_f32 v49, v142, v143\n"
        "v_cvt_pk_bf16_f32 v50, v144, v145\n"
        "v_cvt_pk_bf16_f32 v51, v146, v147\n"
        "v_cvt_pk_bf16_f32 v52, v148, v149\n"
        "v_cvt_pk_bf16_f32 v53, v150, v151\n"
        "v_cvt_pk_bf16_f32 v54, v152, v153\n"
        "v_cvt_pk_bf16_f32 v55, v154, v155\n"
        "v_lshlrev_b32 v9, 3, v2\n"
        "v_mul_u32_u24 v8, 160, v1\n"
        "v_pk_fma_f32 v[140:141], v[140:141], v[172:173], v[186:187] op_sel:[1,1,0] op_sel_hi:[0,1,1] neg_hi:[0,1,0]\n"
        "v_pk_fma_f32 v[142:143], v[142:143], v[172:173], v[188:189] op_sel:[1,1,0] op_sel_hi:[0,1,1] neg_hi:[0,1,0]\n"
        "v_pk_fma_f32 v[144:145], v[144:145], v[172:173], v[190:191] op_sel:[1,1,0] op_sel_hi:[0,1,1] neg_hi:[0,1,0]\n"
        "v_pk_fma_f32 v[146:147], v[146:147], v[172:173], v[176:177] op_sel:[1,1,0] op_sel_hi:[0,1,1] neg_hi:[0,1,0]\n"
        "v_pk_fma_f32 v[148:149], v[148:149], v[174:175], v[178:179] op_sel:[1,1,0] op_sel_hi:[0,1,1] neg_hi:[0,1,0]\n"
        "v_pk_fma_f32 v[150:151], v[150:151], v[174:175], v[180:181] op_sel:[1,1,0] op_sel_hi:[0,1,1] neg_hi:[0,1,0]\n"
        "v_pk_fma_f32 v[152:153], v[152:153], v[174:175], v[182:183] op_sel:[1,1,0] op_sel_hi:[0,1,1] neg_hi:[0,1,0]\n"
        "v_pk_fma_f32 v[154:155], v[154:155], v[174:175], v[184:185] op_sel:[1,1,0] op_sel_hi:[0,1,1] neg_hi:[0,1,0]\n"
        "v_mad_u32_u24 v247, v238, s71, v247\n"
        "v_cndmask_b32 v7, v236, v237, vcc\n"
        "s_add_u32 s73, s73, 74240\n"
        "v_add3_u32 v255, v8, v9, s72\n"
        "v_lshlrev_b32 v254, 4, v1\n"
        "v_mov_b32 v243, v9\n"
        "v_accvgpr_write_b32 a224, v36\n"
        "v_accvgpr_write_b32 a225, v37\n"
        "v_accvgpr_write_b32 a226, v38\n"
        "v_accvgpr_write_b32 a227, v39\n"
        "v_accvgpr_write_b32 a228, v40\n"
        "v_accvgpr_write_b32 a229, v41\n"
        "v_accvgpr_write_b32 a230, v42\n"
        "v_accvgpr_write_b32 a231, v43\n"
        "v_accvgpr_write_b32 a232, v44\n"
        "v_accvgpr_write_b32 a233, v45\n"
        "v_accvgpr_write_b32 a234, v46\n"
        "v_accvgpr_write_b32 a235, v47\n"
        "v_accvgpr_write_b32 a236, v48\n"
        "v_accvgpr_write_b32 a237, v49\n"
        "v_accvgpr_write_b32 a238, v50\n"
        "v_accvgpr_write_b32 a239, v51\n"
        "v_accvgpr_write_b32 a240, v52\n"
        "v_accvgpr_write_b32 a241, v53\n"
        "v_accvgpr_write_b32 a242, v54\n"
        "v_accvgpr_write_b32 a243, v55\n"
        "v_cvt_pk_bf16_f32 v56, v116, v117\n"
        "v_cvt_pk_bf16_f32 v57, v118, v119\n"
        "v_cvt_pk_bf16_f32 v58, v120, v121\n"
        "v_cvt_pk_bf16_f32 v59, v122, v123\n"
        "v_cvt_pk_bf16_f32 v60, v124, v125\n"
        "v_cvt_pk_bf16_f32 v61, v126, v127\n"
        "v_cvt_pk_bf16_f32 v62, v128, v129\n"
        "v_cvt_pk_bf16_f32 v63, v130, v131\n"
        "v_cvt_pk_bf16_f32 v104, v132, v133\n"
        "v_cvt_pk_bf16_f32 v105, v134, v135\n"
        "v_cvt_pk_bf16_f32 v106, v136, v137\n"
        "v_cvt_pk_bf16_f32 v107, v138, v139\n"
        "v_cvt_pk_bf16_f32 v108, v140, v141\n"
        "v_cvt_pk_bf16_f32 v109, v142, v143\n"
        "v_cvt_pk_bf16_f32 v110, v144, v145\n"
        "v_cvt_pk_bf16_f32 v111, v146, v147\n"
        "v_cvt_pk_bf16_f32 v112, v148, v149\n"
        "v_cvt_pk_bf16_f32 v113, v150, v151\n"
        "v_cvt_pk_bf16_f32 v114, v152, v153\n"
        "v_cvt_pk_bf16_f32 v115, v154, v155\n"
        "v_add_u32 v244, s73, v244\n"
        "v_add_u32 v245, s73, v245\n"
        "v_add_u32 v246, s73, v246\n"
        "v_add_u32 v247, s73, v247\n"
        "v_add_u32 v248, s73, v248\n"
        "v_lshl_add_u32 v7, v7, 7, s49\n"
        "buffer_load_dwordx2 v[4:5], v6, s[40:43], 0 offen sc1\n"
        "s_waitcnt vmcnt(0)\n"
        "v_cmp_ne_u64 vcc, s[64:65], v[4:5]\n"
        "s_cbranch_vccz L14_%=\n"
        "s_nop 7\n"
        "s_nop 7\n"
        "s_nop 7\n"
        "s_waitcnt vmcnt(0) lgkmcnt(0)\n"
        "s_mov_b32 s6, 65536\n"
        "L13_%=:\n"
        "s_sleep 4\n"
        "buffer_load_dwordx2 v[4:5], v6, s[40:43], 0 offen sc1\n"
        "s_waitcnt vmcnt(0)\n"
        "v_cmp_ne_u64 vcc, s[64:65], v[4:5]\n"
        "s_cbranch_vccz L14_%=\n"
        "s_sub_u32 s6, s6, 1\n"
        "s_cmp_lg_u32 s6, 0\n"
        "s_cbranch_scc1 L13_%=\n"
        "L14_%=:\n"
        "buffer_load_dwordx4 a[200:203], v12, s[40:43], s16 offen offset:0 sc1\n"
        "buffer_load_dwordx4 a[204:207], v12, s[40:43], s16 offen offset:1024 sc1\n"
        "buffer_load_dwordx4 a[208:211], v12, s[40:43], s16 offen offset:2048 sc1\n"
        "buffer_load_dwordx4 a[212:215], v12, s[40:43], s16 offen offset:3072 sc1\n"
        "buffer_load_dwordx4 a[216:219], v13, s[40:43], s16 offen offset:0 sc1\n"
        "buffer_load_dwordx4 a[220:223], v13, s[40:43], s16 offen offset:1024 sc1\n"
        "buffer_load_dwordx4 v[36:39], v13, s[40:43], s16 offen offset:2048 sc1\n"
        "buffer_load_dwordx4 v[40:43], v13, s[40:43], s16 offen offset:3072 sc1\n"
        "s_add_u32 s84, s67, 40960\n"
        "buffer_load_dwordx4 a[160:163], v12, s[40:43], s84 offen offset:0 sc1\n"
        "buffer_load_dwordx4 a[164:167], v12, s[40:43], s84 offen offset:1024 sc1\n"
        "buffer_load_dwordx4 a[168:171], v12, s[40:43], s84 offen offset:2048 sc1\n"
        "buffer_load_dwordx4 a[172:175], v12, s[40:43], s84 offen offset:3072 sc1\n"
        "buffer_load_dwordx4 v[16:19], v13, s[40:43], s84 offen offset:0 sc1\n"
        "buffer_load_dwordx4 v[44:47], v14, s[40:43], s16 offen offset:0 sc1\n"
        "s_add_u32 s84, s67, 40960\n"
        "buffer_load_dwordx4 v[20:23], v13, s[40:43], s84 offen offset:1024 sc1\n"
        "buffer_load_dwordx4 v[24:27], v13, s[40:43], s84 offen offset:2048 sc1\n"
        "buffer_load_dwordx4 v[28:31], v13, s[40:43], s84 offen offset:3072 sc1\n"
        "buffer_load_dwordx4 v[32:35], v14, s[40:43], s84 offen offset:0 sc1\n"
        "buffer_load_dwordx4 a[196:199], v14, s[40:43], s84 offen offset:1024 sc1\n"
        "buffer_load_dwordx4 v[48:51], v14, s[40:43], s16 offen offset:1024 sc1\n"
        "buffer_load_dwordx4 v[52:55], v12, s[40:43], s17 offen offset:0 sc1\n"
        "buffer_load_dwordx4 a[244:247], v12, s[40:43], s17 offen offset:1024 sc1\n"
        "s_branch JOIN_%=\n"
        "DFT_%=:\n"
        "s_mul_i32 s91, s4, 10\n"
        "s_add_u32 s91, s91, s5\n"
        "s_sub_u32 s91, s91, 20\n"
        "s_lshl_b32 s70, s91, 2\n"
        "s_mul_i32 s86, s70, 205\n"
        "s_lshr_b32 s86, s86, 10\n"
        "s_add_u32 s71, s70, 3\n"
        "s_mul_i32 s71, s71, 205\n"
        "s_lshr_b32 s73, s71, 10\n"
        "s_add_u32 s70, s70, s3\n"
        "s_mul_i32 s87, s70, 205\n"
        "s_lshr_b32 s87, s87, 10\n"
        "s_mul_i32 s71, s87, 5\n"
        "s_sub_u32 s88, s70, s71\n"
        "s_lshr_b32 s89, s4, 1\n"
        "v_add_u32 v250, 0, v0\n"
        "v_mul_u32_u24 v250, 205, v250\n"
        "v_lshrrev_b32 v250, 14, v250\n"
        "v_and_b32 v251, 1, v250\n"
        "v_and_b32 v250, 2, v250\n"
        "v_lshlrev_b32 v251, 1, v251\n"
        "v_lshl_or_b32 v250, v250, 2, v251\n"
        "v_xor_b32 v250, v250, v0\n"
        "v_lshlrev_b32 v4, 4, v250\n"
        "v_add_u32 v250, 64, v0\n"
        "v_mul_u32_u24 v250, 205, v250\n"
        "v_lshrrev_b32 v250, 14, v250\n"
        "v_and_b32 v251, 1, v250\n"
        "v_and_b32 v250, 2, v250\n"
        "v_lshlrev_b32 v251, 1, v251\n"
        "v_lshl_or_b32 v250, v250, 2, v251\n"
        "v_xor_b32 v250, v250, v0\n"
        "v_lshlrev_b32 v5, 4, v250\n"
        "v_add_u32 v250, 128, v0\n"
        "v_mul_u32_u24 v250, 205, v250\n"
        "v_lshrrev_b32 v250, 14, v250\n"
        "v_and_b32 v251, 1, v250\n"
        "v_and_b32 v250, 2, v250\n"
        "v_lshlrev_b32 v251, 1, v251\n"
        "v_lshl_or_b32 v250, v250, 2, v251\n"
        "v_xor_b32 v250, v250, v0\n"
        "v_lshlrev_b32 v6, 4, v250\n"
        "v_add_u32 v250, 192, v0\n"
        "v_mul_u32_u24 v250, 205, v250\n"
        "v_lshrrev_b32 v250, 14, v250\n"
        "v_and_b32 v251, 1, v250\n"
        "v_and_b32 v250, 2, v250\n"
        "v_lshlrev_b32 v251, 1, v251\n"
        "v_lshl_or_b32 v250, v250, 2, v251\n"
        "v_xor_b32 v250, v250, v0\n"
        "v_lshlrev_b32 v7, 4, v250\n"
        "v_add_u32 v250, 256, v0\n"
        "v_mul_u32_u24 v250, 205, v250\n"
        "v_lshrrev_b32 v250, 14, v250\n"
        "v_and_b32 v251, 1, v250\n"
        "v_and_b32 v250, 2, v250\n"
        "v_lshlrev_b32 v251, 1, v251\n"
        "v_lshl_or_b32 v250, v250, 2, v251\n"
        "v_xor_b32 v250, v250, v0\n"
        "v_lshlrev_b32 v8, 4, v250\n"
        "v_add_u32 v8, 0x1000, v8\n"
        "s_and_b32 s70, s86, 15\n"
        "s_lshl_b32 s70, s70, 2\n"
        "s_lshl_b32 s71, s89, 9\n"
        "s_add_u32 s70, s70, s71\n"
        "s_lshl_b32 s71, s3, 7\n"
        "s_add_u32 s70, s70, s71\n"
        "s_mul_i32 s70, s70, 1280\n"
        "s_mul_i32 s71, s3, 10240\n"
        "s_waitcnt lgkmcnt(0)\n"
        "s_load_dwordx2 s[64:65], s[40:41], 0\n"
        "s_add_u32 m0, s71, 0\n"
        "s_add_u32 s72, s70, 0\n"
        "buffer_load_dwordx4 v4, s[8:11], s72 offen offset:0 lds\n"
        "buffer_load_dwordx4 v5, s[8:11], s72 offen offset:1024 lds\n"
        "buffer_load_dwordx4 v6, s[8:11], s72 offen offset:2048 lds\n"
        "buffer_load_dwordx4 v7, s[8:11], s72 offen offset:3072 lds\n"
        "s_add_u32 m0, m0, 4096\n"
        "s_nop 0\n"
        "buffer_load_dwordx4 v8, s[8:11], s72 offen offset:0 lds\n"
        "s_add_u32 m0, s71, 5120\n"
        "s_add_u32 s72, s70, 81920\n"
        "buffer_load_dwordx4 v4, s[8:11], s72 offen offset:0 lds\n"
        "buffer_load_dwordx4 v5, s[8:11], s72 offen offset:1024 lds\n"
        "buffer_load_dwordx4 v6, s[8:11], s72 offen offset:2048 lds\n"
        "buffer_load_dwordx4 v7, s[8:11], s72 offen offset:3072 lds\n"
        "s_add_u32 m0, m0, 4096\n"
        "s_nop 0\n"
        "buffer_load_dwordx4 v8, s[8:11], s72 offen offset:0 lds\n"
        "s_cmp_eq_u32 s73, s86\n"
        "s_cbranch_scc1 L15_%=\n"
        "s_add_u32 m0, s71, 40960\n"
        "s_add_u32 s72, s70, 5120\n"
        "buffer_load_dwordx4 v4, s[8:11], s72 offen offset:0 lds\n"
        "buffer_load_dwordx4 v5, s[8:11], s72 offen offset:1024 lds\n"
        "buffer_load_dwordx4 v6, s[8:11], s72 offen offset:2048 lds\n"
        "buffer_load_dwordx4 v7, s[8:11], s72 offen offset:3072 lds\n"
        "s_add_u32 m0, m0, 4096\n"
        "s_nop 0\n"
        "buffer_load_dwordx4 v8, s[8:11], s72 offen offset:0 lds\n"
        "s_add_u32 m0, s71, 46080\n"
        "s_add_u32 s72, s70, 87040\n"
        "buffer_load_dwordx4 v4, s[8:11], s72 offen offset:0 lds\n"
        "buffer_load_dwordx4 v5, s[8:11], s72 offen offset:1024 lds\n"
        "buffer_load_dwordx4 v6, s[8:11], s72 offen offset:2048 lds\n"
        "buffer_load_dwordx4 v7, s[8:11], s72 offen offset:3072 lds\n"
        "s_add_u32 m0, m0, 4096\n"
        "s_nop 0\n"
        "buffer_load_dwordx4 v8, s[8:11], s72 offen offset:0 lds\n"
        "L15_%=:\n"
        "s_cmp_ge_u32 s91, 64\n"
        "s_cselect_b32 s7, 0x40000000, 0\n"
        "v_add_u32 v250, 0, v0\n"
        "s_lshl_b32 s70, s91, 2\n"
        "v_mul_u32_u24 v251, 1525, v250\n"
        "s_add_u32 s70, s70, s3\n"
        "v_lshrrev_b32 v251, 16, v251\n"
        "s_lshr_b32 s90, s70, 3\n"
        "v_mul_u32_u24 v252, 43, v251\n"
        "s_and_b32 s70, s70, 7\n"
        "v_sub_u32 v250, v250, v252\n"
        "s_lshl_b32 s71, s70, 7\n"
        "v_and_b32 v252, 3, v251\n"
        "s_add_u32 s71, s71, s90\n"
        "v_lshrrev_b32 v251, 2, v251\n"
        "v_mul_u32_u24 v252, 20544, v252\n"
        "s_mul_i32 s71, s71, 642\n"
        "v_mul_u32_u24 v251, 657408, v251\n"
        "v_lshl_add_u32 v250, v250, 4, v252\n"
        "s_andn2_b32 s71, s71, 15\n"
        "s_mul_i32 s72, s3, 12288\n"
        "v_add_u32 v250, v250, v251\n"
        "s_add_u32 s73, s71, s7\n"
        "s_add_u32 s72, s72, 81920\n"
        "v_subrev_u32 v144, 0, v250\n"
        "s_add_u32 m0, s72, 0\n"
        "s_nop 0\n"
        "buffer_load_dwordx4 v144, s[12:15], s73 offen offset:0 nt lds\n"
        "v_add_u32 v250, 64, v0\n"
        "v_cvt_f32_u32 v242, v2\n"
        "v_mul_u32_u24 v251, 1525, v250\n"
        "v_and_b32 v254, 3, v1\n"
        "v_lshrrev_b32 v251, 16, v251\n"
        "v_mov_b32 v247, 0xbf803f80\n"
        "v_mul_u32_u24 v252, 43, v251\n"
        "v_sub_u32 v250, v250, v252\n"
        "v_and_b32 v252, 3, v251\n"
        "v_lshrrev_b32 v251, 2, v251\n"
        "v_mul_u32_u24 v252, 20544, v252\n"
        "v_mul_u32_u24 v251, 657408, v251\n"
        "v_lshl_add_u32 v250, v250, 4, v252\n"
        "v_add_u32 v250, v250, v251\n"
        "v_subrev_u32 v145, 1024, v250\n"
        "v_add_u32 v250, 128, v0\n"
        "v_mul_u32_u24 v251, 1525, v250\n"
        "v_lshrrev_b32 v251, 16, v251\n"
        "buffer_load_dwordx4 v145, s[12:15], s73 offen offset:1024 nt lds\n"
        "v_mul_u32_u24 v252, 43, v251\n"
        "v_sub_u32 v250, v250, v252\n"
        "v_and_b32 v252, 3, v251\n"
        "v_lshrrev_b32 v251, 2, v251\n"
        "v_mul_u32_u24 v252, 20544, v252\n"
        "v_mul_u32_u24 v251, 657408, v251\n"
        "v_lshl_add_u32 v250, v250, 4, v252\n"
        "v_add_u32 v250, v250, v251\n"
        "v_subrev_u32 v146, 2048, v250\n"
        "v_add_u32 v250, 192, v0\n"
        "v_mul_u32_u24 v251, 1525, v250\n"
        "v_lshrrev_b32 v251, 16, v251\n"
        "v_mul_u32_u24 v252, 43, v251\n"
        "v_sub_u32 v250, v250, v252\n"
        "v_and_b32 v252, 3, v251\n"
        "v_lshrrev_b32 v251, 2, v251\n"
        "v_mul_u32_u24 v252, 20544, v252\n"
        "buffer_load_dwordx4 v146, s[12:15], s73 offen offset:2048 nt lds\n"
        "v_mul_u32_u24 v251, 657408, v251\n"
        "v_lshl_add_u32 v250, v250, 4, v252\n"
        "v_add_u32 v250, v250, v251\n"
        "v_subrev_u32 v147, 3072, v250\n"
        "v_add_u32 v250, 256, v0\n"
        "v_mul_u32_u24 v251, 1525, v250\n"
        "v_lshrrev_b32 v251, 16, v251\n"
        "v_mul_u32_u24 v252, 43, v251\n"
        "v_sub_u32 v250, v250, v252\n"
        "v_and_b32 v252, 3, v251\n"
        "v_lshrrev_b32 v251, 2, v251\n"
        "v_mul_u32_u24 v252, 20544, v252\n"
        "v_mul_u32_u24 v251, 657408, v251\n"
        "v_lshl_add_u32 v250, v250, 4, v252\n"
        "v_add_u32 v250, v250, v251\n"
        "v_subrev_u32 v148, 0, v250\n"
        "v_add_u32 v250, 320, v0\n"
        "buffer_load_dwordx4 v147, s[12:15], s73 offen offset:3072 nt lds\n"
        "s_add_u32 m0, m0, 4096\n"
        "v_mul_u32_u24 v251, 1525, v250\n"
        "v_lshrrev_b32 v251, 16, v251\n"
        "v_mul_u32_u24 v252, 43, v251\n"
        "v_sub_u32 v250, v250, v252\n"
        "v_and_b32 v252, 3, v251\n"
        "v_lshrrev_b32 v251, 2, v251\n"
        "v_mul_u32_u24 v252, 20544, v252\n"
        "v_mul_u32_u24 v251, 657408, v251\n"
        "v_lshl_add_u32 v250, v250, 4, v252\n"
        "v_and_b32 v252, 16, v0\n"
        "v_add_u32 v250, v250, v251\n"
        "v_and_b32 v251, 31, v0\n"
        "v_subrev_u32 v149, 1024, v250\n"
        "v_lshrrev_b32 v250, 2, v1\n"
        "v_mul_u32_u24 v250, 688, v250\n"
        "v_lshl_add_u32 v250, v252, 1, v250\n"
        "v_lshrrev_b32 v252, 5, v0\n"
        "buffer_load_dwordx4 v148, s[12:15], s73 offen offset:0 nt lds\n"
        "v_mul_u32_u24 v253, 2752, v252\n"
        "v_lshl_add_u32 v250, v254, 3, v250\n"
        "v_add_u32 v252, 4, v252\n"
        "v_lshlrev_b32 v252, 6, v252\n"
        "buffer_load_dwordx4 v149, s[12:15], s73 offen offset:1024 nt lds\n"
        "s_add_u32 m0, s72, 6144\n"
        "s_mul_i32 s72, s3, 12288\n"
        "buffer_load_dwordx4 v144, s[16:19], s73 offen offset:0 nt lds\n"
        "buffer_load_dwordx4 v145, s[16:19], s73 offen offset:1024 nt lds\n"
        "buffer_load_dwordx4 v146, s[16:19], s73 offen offset:2048 nt lds\n"
        "buffer_load_dwordx4 v147, s[16:19], s73 offen offset:3072 nt lds\n"
        "s_add_u32 m0, m0, 4096\n"
        "s_lshl_b32 s70, s91, 2\n"
        "s_add_u32 s70, s70, s3\n"
        "s_mul_i32 s70, s70, 10496\n"
        "s_add_u32 s84, s70, 0xdfff00\n"
        "s_add_u32 s85, s84, 8192\n"
        "s_and_b32 s90, s90, 7\n"
        "s_add_u32 s72, s72, 81920\n"
        "s_lshl_b32 s70, s88, 4\n"
        "v_add_u32 v240, s70, v1\n"
        "s_sub_u32 s70, s87, s86\n"
        "v_cvt_f32_u32 v241, v240\n"
        "v_cmp_eq_u32 s[74:75], 0, v240\n"
        "v_mul_f32 v208, 0xbacccccd, v241\n"
        "v_mul_f32 v241, 0x3bcccccd, v241\n"
        "v_add_f32 v209, v208, v208\n"
        "v_mul_f32 v242, v242, v241\n"
        "v_cos_f32 v6, v209\n"
        "v_sin_f32 v7, v209\n"
        "v_add_f32 v209, v209, v208\n"
        "v_cos_f32 v4, v208\n"
        "v_cos_f32 v8, v209\n"
        "v_mul_f32 v242, 0x41000000, v242\n"
        "v_sin_f32 v5, v208\n"
        "v_fmamk_f32 v210, v241, 0x00000000, v242\n"
        "v_fmamk_f32 v211, v241, 0x3f800000, v242\n"
        "v_mov_b32 v208, 0xbf3504f3\n"
        "v_cos_f32 v212, v210\n"
        "v_sin_f32 v214, v210\n"
        "v_cos_f32 v213, v211\n"
        "v_sin_f32 v215, v211\n"
        "buffer_load_dwordx4 v148, s[16:19], s73 offen offset:0 nt lds\n"
        "v_cndmask_b32_e64 v10, v4, v208, s[74:75]\n"
        "v_cndmask_b32_e64 v243, v8, v208, s[74:75]\n"
        "v_and_b32 v208, 3, v1\n"
        "v_sin_f32 v9, v209\n"
        "v_lshrrev_b32 v209, 2, v1\n"
        "v_and_b32 v245, 1, v208\n"
        "v_fmamk_f32 v210, v241, 0x42080000, v242\n"
        "v_fmamk_f32 v211, v241, 0x420c0000, v242\n"
        "v_cvt_pk_bf16_f32 v16, v212, v213\n"
        "v_cvt_pk_bf16_f32 v36, -v214, -v215\n"
        "v_lshlrev_b32 v209, 3, v209\n"
        "v_lshlrev_b32 v245, 5, v245\n"
        "v_and_b32 v246, 2, v208\n"
        "v_cos_f32 v212, v210\n"
        "v_sin_f32 v214, v210\n"
        "v_cos_f32 v213, v211\n"
        "v_sin_f32 v215, v211\n"
        "v_lshl_add_u32 v209, v2, 6, v209\n"
        "v_lshl_or_b32 v245, v246, 6, v245\n"
        "v_mul_u32_u24 v208, 1280, v208\n"
        "s_mul_i32 s70, s70, 40960\n"
        "v_fmamk_f32 v210, v241, 0x42880000, v242\n"
        "v_fmamk_f32 v211, v241, 0x428a0000, v242\n"
        "v_xor_b32 v209, v209, v245\n"
        "v_add_u32 v208, s70, v208\n"
        "v_cvt_pk_bf16_f32 v21, v212, v213\n"
        "v_cvt_pk_bf16_f32 v41, -v214, -v215\n"
        "v_cos_f32 v212, v210\n"
        "v_sin_f32 v214, v210\n"
        "v_cos_f32 v213, v211\n"
        "v_sin_f32 v215, v211\n"
        "v_add_u32 v245, v208, v209\n"
        "v_xor_b32 v209, 32, v209\n"
        "v_fmamk_f32 v210, v241, 0x42cc0000, v242\n"
        "v_add_u32 v246, v208, v209\n"
        "v_lshrrev_b32 v209, 2, v240\n"
        "v_cvt_pk_bf16_f32 v26, v212, v213\n"
        "v_cvt_pk_bf16_f32 v46, -v214, -v215\n"
        "v_cos_f32 v212, v210\n"
        "v_sin_f32 v214, v210\n"
        "v_mul_u32_u24 v208, 10496, v2\n"
        "v_lshl_add_u32 v209, v209, 2, v240\n"
        "v_subrev_u32 v251, s90, v251\n"
        "v_sub_u32 v210, 0, v240\n"
        "v_fmamk_f32 v216, v241, 0x40000000, v242\n"
        "v_fmamk_f32 v217, v241, 0x40400000, v242\n"
        "v_fmamk_f32 v222, v241, 0x40800000, v242\n"
        "v_fmamk_f32 v223, v241, 0x40a00000, v242\n"
        "v_fmamk_f32 v228, v241, 0x40c00000, v242\n"
        "v_fmamk_f32 v229, v241, 0x40e00000, v242\n"
        "v_fmamk_f32 v234, v241, 0x42000000, v242\n"
        "v_fmamk_f32 v235, v241, 0x42040000, v242\n"
        "v_add3_u32 v150, s72, v250, v253\n"
        "v_lshl_add_u32 v248, v209, 4, v208\n"
        "v_ashrrev_i32 v250, 2, v251\n"
        "v_ashrrev_i32 v209, 2, v210\n"
        "v_sin_f32 v220, v216\n"
        "v_sin_f32 v221, v217\n"
        "v_sin_f32 v226, v222\n"
        "v_sin_f32 v227, v223\n"
        "v_sin_f32 v232, v228\n"
        "v_sin_f32 v233, v229\n"
        "v_sin_f32 v238, v234\n"
        "v_sin_f32 v239, v235\n"
        "v_lshl_add_u32 v250, v250, 2, v251\n"
        "v_lshl_add_u32 v209, v209, 2, v210\n"
        "v_cos_f32 v218, v216\n"
        "v_cos_f32 v219, v217\n"
        "v_cos_f32 v224, v222\n"
        "v_cos_f32 v225, v223\n"
        "v_cos_f32 v230, v228\n"
        "v_cos_f32 v231, v229\n"
        "v_cos_f32 v236, v234\n"
        "v_cos_f32 v237, v235\n"
        "v_lshl_add_u32 v250, v250, 4, v252\n"
        "v_fmamk_f32 v216, v241, 0x42100000, v242\n"
        "v_fmamk_f32 v217, v241, 0x42140000, v242\n"
        "v_fmamk_f32 v222, v241, 0x42180000, v242\n"
        "v_fmamk_f32 v223, v241, 0x421c0000, v242\n"
        "v_fmamk_f32 v228, v241, 0x42800000, v242\n"
        "v_fmamk_f32 v229, v241, 0x42820000, v242\n"
        "v_fmamk_f32 v234, v241, 0x42840000, v242\n"
        "v_fmamk_f32 v235, v241, 0x42860000, v242\n"
        "v_lshl_add_u32 v208, v209, 4, v208\n"
        "v_cndmask_b32_e64 v209, 0, -1, s[74:75]\n"
        "v_cvt_pk_bf16_f32 v37, -v220, -v221\n"
        "v_cvt_pk_bf16_f32 v38, -v226, -v227\n"
        "v_cvt_pk_bf16_f32 v39, -v232, -v233\n"
        "v_cvt_pk_bf16_f32 v40, -v238, -v239\n"
        "v_add_u32 v153, s7, v250\n"
        "v_mov_b32 v252, 0x40000000\n"
        "v_cmp_gt_i32 vcc, 0, v251\n"
        "v_sin_f32 v220, v216\n"
        "v_sin_f32 v221, v217\n"
        "v_sin_f32 v226, v222\n"
        "v_sin_f32 v227, v223\n"
        "v_sin_f32 v232, v228\n"
        "v_sin_f32 v233, v229\n"
        "v_sin_f32 v238, v234\n"
        "v_sin_f32 v239, v235\n"
        "v_add_u32 v249, 64, v248\n"
        "v_and_b32 v210, 7680, v209\n"
        "v_cvt_pk_bf16_f32 v17, v218, v219\n"
        "v_cvt_pk_bf16_f32 v18, v224, v225\n"
        "v_cvt_pk_bf16_f32 v19, v230, v231\n"
        "v_cvt_pk_bf16_f32 v20, v236, v237\n"
        "v_cndmask_b32 v151, v153, v252, vcc\n"
        "v_cos_f32 v218, v216\n"
        "v_cos_f32 v219, v217\n"
        "v_cos_f32 v224, v222\n"
        "v_cos_f32 v225, v223\n"
        "v_cos_f32 v230, v228\n"
        "v_cos_f32 v231, v229\n"
        "v_cos_f32 v236, v234\n"
        "v_cos_f32 v237, v235\n"
        "v_cmp_le_i32 vcc, 8, v251\n"
        "v_add_u32 v249, v249, v210\n"
        "v_add_u32 v251, 10304, v208\n"
        "v_and_b32 v210, 7680, v209\n"
        "v_fmamk_f32 v216, v241, 0x428c0000, v242\n"
        "v_fmamk_f32 v217, v241, 0x428e0000, v242\n"
        "v_fmamk_f32 v222, v241, 0x42c00000, v242\n"
        "v_fmamk_f32 v223, v241, 0x42c20000, v242\n"
        "v_fmamk_f32 v228, v241, 0x42c40000, v242\n"
        "v_fmamk_f32 v229, v241, 0x42c60000, v242\n"
        "v_fmamk_f32 v234, v241, 0x42c80000, v242\n"
        "v_fmamk_f32 v235, v241, 0x42ca0000, v242\n"
        "v_cndmask_b32 v152, v153, v252, vcc\n"
        "v_cvt_pk_bf16_f32 v42, -v220, -v221\n"
        "v_cvt_pk_bf16_f32 v43, -v226, -v227\n"
        "v_cvt_pk_bf16_f32 v44, -v232, -v233\n"
        "v_cvt_pk_bf16_f32 v45, -v238, -v239\n"
        "v_sub_u32 v251, v251, v210\n"
        "v_sin_f32 v220, v216\n"
        "v_sin_f32 v221, v217\n"
        "v_sin_f32 v226, v222\n"
        "v_sin_f32 v227, v223\n"
        "v_sin_f32 v232, v228\n"
        "v_sin_f32 v233, v229\n"
        "v_sin_f32 v238, v234\n"
        "v_sin_f32 v239, v235\n"
        "v_add_u32 v252, 5120, v248\n"
        "v_and_b32 v210, 2560, v209\n"
        "v_cvt_pk_bf16_f32 v22, v218, v219\n"
        "v_cvt_pk_bf16_f32 v23, v224, v225\n"
        "v_cvt_pk_bf16_f32 v24, v230, v231\n"
        "v_cvt_pk_bf16_f32 v25, v236, v237\n"
        "v_cos_f32 v218, v216\n"
        "v_cos_f32 v219, v217\n"
        "v_cos_f32 v224, v222\n"
        "v_cos_f32 v225, v223\n"
        "v_cos_f32 v230, v228\n"
        "v_cos_f32 v231, v229\n"
        "v_cos_f32 v236, v234\n"
        "v_cos_f32 v237, v235\n"
        "v_add_u32 v252, v252, v210\n"
        "v_add_u32 v254, 5120, v208\n"
        "v_and_b32 v210, 2560, v209\n"
        "v_fmamk_f32 v211, v241, 0x42ce0000, v242\n"
        "v_fmamk_f32 v216, v241, 0x43000000, v242\n"
        "v_fmamk_f32 v217, v241, 0x43010000, v242\n"
        "v_fmamk_f32 v222, v241, 0x43020000, v242\n"
        "v_fmamk_f32 v223, v241, 0x43030000, v242\n"
        "v_fmamk_f32 v228, v241, 0x43040000, v242\n"
        "v_fmamk_f32 v229, v241, 0x43050000, v242\n"
        "v_fmamk_f32 v234, v241, 0x43060000, v242\n"
        "v_fmamk_f32 v235, v241, 0x43070000, v242\n"
        "v_sub_u32 v254, v254, v210\n"
        "v_cvt_pk_bf16_f32 v47, -v220, -v221\n"
        "v_cvt_pk_bf16_f32 v48, -v226, -v227\n"
        "v_cvt_pk_bf16_f32 v49, -v232, -v233\n"
        "v_cvt_pk_bf16_f32 v50, -v238, -v239\n"
        "v_add_u32 v255, 5184, v208\n"
        "v_and_b32 v210, 64, v209\n"
        "v_sin_f32 v215, v211\n"
        "v_sin_f32 v220, v216\n"
        "v_sin_f32 v221, v217\n"
        "v_sin_f32 v226, v222\n"
        "v_sin_f32 v227, v223\n"
        "v_sin_f32 v232, v228\n"
        "v_sin_f32 v233, v229\n"
        "v_sin_f32 v238, v234\n"
        "v_sin_f32 v239, v235\n"
        "v_cvt_pk_bf16_f32 v27, v218, v219\n"
        "v_cvt_pk_bf16_f32 v28, v224, v225\n"
        "v_cvt_pk_bf16_f32 v29, v230, v231\n"
        "v_cvt_pk_bf16_f32 v30, v236, v237\n"
        "v_add_u32 v250, 10240, v208\n"
        "v_add_u32 v253, 5184, v248\n"
        "v_sub_u32 v255, v255, v210\n"
        "buffer_load_dwordx4 v149, s[16:19], s73 offen offset:1024 nt lds\n"
        "v_cos_f32 v213, v211\n"
        "v_cos_f32 v218, v216\n"
        "v_cos_f32 v219, v217\n"
        "v_cos_f32 v224, v222\n"
        "v_cos_f32 v225, v223\n"
        "v_cos_f32 v230, v228\n"
        "v_cos_f32 v231, v229\n"
        "v_cos_f32 v236, v234\n"
        "v_cos_f32 v237, v235\n"
        "v_cvt_pk_bf16_f32 v51, -v214, -v215\n"
        "v_cvt_pk_bf16_f32 v52, -v220, -v221\n"
        "v_cvt_pk_bf16_f32 v53, -v226, -v227\n"
        "v_cvt_pk_bf16_f32 v54, -v232, -v233\n"
        "v_cvt_pk_bf16_f32 v55, -v238, -v239\n"
        "v_add_u32 v154, 0x1000, v153\n"
        "v_cndmask_b32_e64 v36, v36, v247, s[74:75]\n"
        "v_cndmask_b32_e64 v37, v37, v247, s[74:75]\n"
        "v_cndmask_b32_e64 v38, v38, v247, s[74:75]\n"
        "v_cndmask_b32_e64 v39, v39, v247, s[74:75]\n"
        "v_cndmask_b32_e64 v40, v40, v247, s[74:75]\n"
        "v_cndmask_b32_e64 v41, v41, v247, s[74:75]\n"
        "v_cndmask_b32_e64 v42, v42, v247, s[74:75]\n"
        "v_cndmask_b32_e64 v43, v43, v247, s[74:75]\n"
        "v_cndmask_b32_e64 v44, v44, v247, s[74:75]\n"
        "v_cndmask_b32_e64 v45, v45, v247, s[74:75]\n"
        "v_cndmask_b32_e64 v46, v46, v247, s[74:75]\n"
        "v_cndmask_b32_e64 v47, v47, v247, s[74:75]\n"
        "v_cndmask_b32_e64 v48, v48, v247, s[74:75]\n"
        "v_cndmask_b32_e64 v49, v49, v247, s[74:75]\n"
        "v_cndmask_b32_e64 v50, v50, v247, s[74:75]\n"
        "v_cvt_pk_bf16_f32 v31, v212, v213\n"
        "v_cndmask_b32_e64 v51, v51, v247, s[74:75]\n"
        "v_cvt_pk_bf16_f32 v32, v218, v219\n"
        "v_cndmask_b32_e64 v52, v52, v247, s[74:75]\n"
        "v_cvt_pk_bf16_f32 v33, v224, v225\n"
        "v_cndmask_b32_e64 v53, v53, v247, s[74:75]\n"
        "v_cvt_pk_bf16_f32 v34, v230, v231\n"
        "v_cndmask_b32_e64 v54, v54, v247, s[74:75]\n"
        "v_cvt_pk_bf16_f32 v35, v236, v237\n"
        "v_cndmask_b32_e64 v55, v55, v247, s[74:75]\n"
        "v_cndmask_b32_e64 v244, 1.0, 0, s[74:75]\n"
        "v_permlane16_swap_b32 v248, v249\n"
        "v_permlane16_swap_b32 v250, v251\n"
        "v_permlane16_swap_b32 v252, v253\n"
        "v_permlane16_swap_b32 v254, v255\n"
        "s_waitcnt lgkmcnt(0)\n"
        "s_mul_hi_u32 s70, s64, 0x4c957f2d\n"
        "s_mul_i32 s71, s64, 0x5851f42d\n"
        "s_mul_i32 s72, s65, 0x4c957f2d\n"
        "s_mul_i32 s64, s64, 0x4c957f2d\n"
        "s_add_u32 s70, s70, s71\n"
        "s_add_u32 s70, s70, s72\n"
        "s_add_u32 s64, s64, 1\n"
        "s_addc_u32 s65, s70, 0\n"
        "s_waitcnt vmcnt(12)\n"
        "s_barrier\n"
        "s_lshl_b32 s78, s3, 24\n"
        "v_lshlrev_b32 v208, 18, v0\n"
        "buffer_load_dword a196, v208, s[40:43], s78 offen\n"
        "buffer_load_dword a197, v208, s[36:39], s78 offen\n"
        "s_lshl_b32 s70, s2, 7\n"
        "s_add_u32 s70, s70, 11534336\n"
        "s_cmp_eq_u32 s3, 0\n"
        "s_cselect_b32 s70, s70, 0x40000000\n"
        "v_mov_b32 v208, s64\n"
        "v_mov_b32 v209, s65\n"
        "v_cmp_eq_u32 vcc, 0, v0\n"
        "v_mov_b32 v210, 0x40000000\n"
        "s_nop 0\n"
        "v_cndmask_b32 v210, v210, v0, vcc\n"
        "buffer_store_dwordx2 v[208:209], v210, s[40:43], s70 offen sc1\n"
        "ds_read_b64_tr_b16 a[200:201], v245 offset:0\n"
        "ds_read_b64_tr_b16 a[202:203], v246 offset:0\n"
        "ds_read_b64_tr_b16 a[204:205], v245 offset:256\n"
        "ds_read_b64_tr_b16 a[206:207], v246 offset:256\n"
        "ds_read_b64_tr_b16 a[208:209], v245 offset:512\n"
        "ds_read_b64_tr_b16 a[210:211], v246 offset:512\n"
        "ds_read_b64_tr_b16 a[212:213], v245 offset:768\n"
        "ds_read_b64_tr_b16 a[214:215], v246 offset:768\n"
        "ds_read_b64_tr_b16 a[216:217], v245 offset:1024\n"
        "ds_read_b64_tr_b16 a[218:219], v246 offset:1024\n"
        "s_waitcnt lgkmcnt(8)\n"
        "v_mfma_f32_16x16x32_bf16 v[56:59], a[200:203], v[16:19], 0\n"
        "v_mfma_f32_16x16x32_bf16 v[88:91], a[200:203], v[36:39], 0\n"
        "s_waitcnt lgkmcnt(6)\n"
        "v_mfma_f32_16x16x32_bf16 v[56:59], a[204:207], v[20:23], v[56:59]\n"
        "v_mfma_f32_16x16x32_bf16 v[88:91], a[204:207], v[40:43], v[88:91]\n"
        "s_waitcnt lgkmcnt(4)\n"
        "v_mfma_f32_16x16x32_bf16 v[56:59], a[208:211], v[24:27], v[56:59]\n"
        "v_mfma_f32_16x16x32_bf16 v[88:91], a[208:211], v[44:47], v[88:91]\n"
        "s_waitcnt lgkmcnt(2)\n"
        "v_mfma_f32_16x16x32_bf16 v[56:59], a[212:215], v[28:31], v[56:59]\n"
        "v_mfma_f32_16x16x32_bf16 v[88:91], a[212:215], v[48:51], v[88:91]\n"
        "s_waitcnt lgkmcnt(0)\n"
        "v_mfma_f32_16x16x32_bf16 v[56:59], a[216:219], v[32:35], v[56:59]\n"
        "v_mfma_f32_16x16x32_bf16 v[88:91], a[216:219], v[52:55], v[88:91]\n"
        "s_nop 6\n"
        "v_mul_f32 v212, v4, v57\n"
        "v_fma_f32 v208, v6, v58, v56\n"
        "v_mul_f32 v213, v10, v89\n"
        "v_fma_f32 v209, v6, v90, v88\n"
        "v_fma_f32 v212, -v5, v89, v212\n"
        "v_fmac_f32 v213, v5, v57\n"
        "v_fma_f32 v208, -v7, v90, v208\n"
        "v_fmac_f32 v209, v7, v58\n"
        "v_fma_f32 v214, v8, v59, v212\n"
        "v_fma_f32 v215, v243, v91, v213\n"
        "v_fma_f32 v210, 2.0, v56, -v208\n"
        "v_fma_f32 v211, 2.0, v88, -v209\n"
        "v_fma_f32 v214, -v9, v91, v214\n"
        "v_fmac_f32 v215, v9, v59\n"
        "v_cndmask_b32_e64 v211, v211, -v210, s[74:75]\n"
        "v_cndmask_b32_e64 v209, v209, v90, s[74:75]\n"
        "v_cndmask_b32_e64 v210, v210, v88, s[74:75]\n"
        "v_fma_f32 v216, 2.0, v212, -v214\n"
        "v_fma_f32 v217, 2.0, v213, -v215\n"
        "v_add_f32 v120, v208, v214\n"
        "v_add_f32 v121, v209, v215\n"
        "v_sub_f32 v122, v208, v214\n"
        "v_sub_f32 v123, v215, v209\n"
        "v_add_f32 v124, v210, v217\n"
        "v_fma_f32 v125, v244, v211, -v216\n"
        "v_sub_f32 v126, v210, v217\n"
        "v_fma_f32 v127, -v244, v216, -v211\n"
        "s_waitcnt vmcnt(3)\n"
        "ds_read_b64_tr_b16 a[0:1], v150 offset:0\n"
        "ds_read_b64_tr_b16 a[2:3], v150 offset:6144\n"
        "ds_read_b64_tr_b16 a[4:5], v150 offset:64\n"
        "ds_read_b64_tr_b16 a[6:7], v150 offset:6208\n"
        "ds_read_b64_tr_b16 a[8:9], v150 offset:128\n"
        "ds_read_b64_tr_b16 a[10:11], v150 offset:6272\n"
        "ds_read_b64_tr_b16 a[12:13], v150 offset:192\n"
        "ds_read_b64_tr_b16 a[14:15], v150 offset:6336\n"
        "ds_read_b64_tr_b16 a[16:17], v150 offset:256\n"
        "ds_read_b64_tr_b16 a[18:19], v150 offset:6400\n"
        "ds_read_b64_tr_b16 a[20:21], v150 offset:320\n"
        "ds_read_b64_tr_b16 a[22:23], v150 offset:6464\n"
        "ds_read_b64_tr_b16 a[24:25], v150 offset:384\n"
        "ds_read_b64_tr_b16 a[26:27], v150 offset:6528\n"
        "ds_read_b64_tr_b16 a[28:29], v150 offset:448\n"
        "ds_read_b64_tr_b16 a[30:31], v150 offset:6592\n"
        "ds_read_b64_tr_b16 a[32:33], v150 offset:512\n"
        "ds_read_b64_tr_b16 a[34:35], v150 offset:6656\n"
        "ds_read_b64_tr_b16 a[36:37], v150 offset:576\n"
        "ds_read_b64_tr_b16 a[38:39], v150 offset:6720\n"
        "ds_read_b64_tr_b16 a[40:41], v150 offset:640\n"
        "ds_read_b64_tr_b16 a[42:43], v150 offset:6784\n"
        "ds_read_b64_tr_b16 a[220:221], v245 offset:5120\n"
        "ds_read_b64_tr_b16 a[222:223], v246 offset:5120\n"
        "ds_read_b64_tr_b16 a[224:225], v245 offset:5376\n"
        "ds_read_b64_tr_b16 a[226:227], v246 offset:5376\n"
        "ds_read_b64_tr_b16 a[228:229], v245 offset:5632\n"
        "ds_read_b64_tr_b16 a[230:231], v246 offset:5632\n"
        "ds_read_b64_tr_b16 a[232:233], v245 offset:5888\n"
        "ds_read_b64_tr_b16 a[234:235], v246 offset:5888\n"
        "ds_read_b64_tr_b16 a[236:237], v245 offset:6144\n"
        "ds_read_b64_tr_b16 a[238:239], v246 offset:6144\n"
        "ds_read_b64_tr_b16 a[240:241], v245 offset:10240\n"
        "s_waitcnt lgkmcnt(15)\n"
        "buffer_store_dwordx4 a[0:3], v151, s[40:43], s84 offen offset:0 sc1\n"
        "ds_read_b64_tr_b16 a[242:243], v246 offset:10240\n"
        "ds_read_b64_tr_b16 a[244:245], v245 offset:10496\n"
        "ds_read_b64_tr_b16 a[246:247], v246 offset:10496\n"
        "ds_read_b64_tr_b16 a[248:249], v245 offset:10752\n"
        "ds_read_b64_tr_b16 a[250:251], v246 offset:10752\n"
        "ds_read_b64_tr_b16 a[252:253], v245 offset:11008\n"
        "ds_read_b64_tr_b16 a[254:255], v246 offset:11008\n"
        "ds_read_b64_tr_b16 a[200:201], v245 offset:11264\n"
        "ds_read_b64_tr_b16 a[202:203], v246 offset:11264\n"
        "ds_read_b64_tr_b16 a[204:205], v245 offset:15360\n"
        "ds_read_b64_tr_b16 a[206:207], v246 offset:15360\n"
        "ds_read_b64_tr_b16 a[208:209], v245 offset:15616\n"
        "ds_read_b64_tr_b16 a[210:211], v246 offset:15616\n"
        "ds_read_b64_tr_b16 a[212:213], v245 offset:15872\n"
        "ds_read_b64_tr_b16 a[214:215], v246 offset:15872\n"
        "ds_read_b64_tr_b16 a[216:217], v245 offset:16128\n"
        "ds_read_b64_tr_b16 a[218:219], v246 offset:16128\n"
        "s_and_b32 s70, s87, 15\n"
        "s_waitcnt lgkmcnt(15)\n"
        "v_mfma_f32_16x16x32_bf16 v[60:63], a[220:223], v[16:19], 0\n"
        "s_lshl_b32 s70, s70, 2\n"
        "s_lshl_b32 s71, s89, 6\n"
        "v_mfma_f32_16x16x32_bf16 v[92:95], a[220:223], v[36:39], 0\n"
        "ds_read_b64_tr_b16 a[220:221], v245 offset:16384\n"
        "ds_read_b64_tr_b16 a[222:223], v246 offset:16384\n"
        "v_mfma_f32_16x16x32_bf16 v[60:63], a[224:227], v[20:23], v[60:63]\n"
        "s_add_u32 s70, s70, s71\n"
        "s_mul_i32 s70, s70, 10496\n"
        "v_mfma_f32_16x16x32_bf16 v[92:95], a[224:227], v[40:43], v[92:95]\n"
        "buffer_store_dwordx4 a[4:7], v153, s[40:43], s84 offen offset:1024 sc1\n"
        "v_mfma_f32_16x16x32_bf16 v[60:63], a[228:231], v[24:27], v[60:63]\n"
        "ds_read_b64_tr_b16 a[224:225], v245 offset:20480\n"
        "ds_read_b64_tr_b16 a[226:227], v246 offset:20480\n"
        "v_mfma_f32_16x16x32_bf16 v[92:95], a[228:231], v[44:47], v[92:95]\n"
        "ds_read_b64_tr_b16 a[228:229], v245 offset:20736\n"
        "ds_read_b64_tr_b16 a[230:231], v246 offset:20736\n"
        "v_mfma_f32_16x16x32_bf16 v[60:63], a[232:235], v[28:31], v[60:63]\n"
        "s_add_u32 s70, s70, 0x40000\n"
        "s_lshl_b32 s68, s91, 7\n"
        "v_mfma_f32_16x16x32_bf16 v[92:95], a[232:235], v[48:51], v[92:95]\n"
        "ds_read_b64_tr_b16 a[232:233], v245 offset:20992\n"
        "ds_read_b64_tr_b16 a[234:235], v246 offset:20992\n"
        "v_mfma_f32_16x16x32_bf16 v[60:63], a[236:239], v[32:35], v[60:63]\n"
        "s_add_u32 s68, s68, 11567104\n"
        "s_cmp_eq_u32 s3, 0\n"
        "v_mfma_f32_16x16x32_bf16 v[92:95], a[236:239], v[52:55], v[92:95]\n"
        "ds_read_b64_tr_b16 a[236:237], v245 offset:21248\n"
        "ds_read_b64_tr_b16 a[238:239], v246 offset:21248\n"
        "v_mfma_f32_16x16x32_bf16 v[64:67], a[240:243], v[16:19], 0\n"
        "s_nop 1\n"
        "v_mul_f32 v222, v4, v61\n"
        "v_fma_f32 v218, v6, v62, v60\n"
        "v_mfma_f32_16x16x32_bf16 v[96:99], a[240:243], v[36:39], 0\n"
        "buffer_store_dwordx4 a[8:11], v153, s[40:43], s84 offen offset:2048 sc1\n"
        "s_waitcnt lgkmcnt(15)\n"
        "v_mfma_f32_16x16x32_bf16 v[68:71], a[204:207], v[16:19], 0\n"
        "ds_read_b64_tr_b16 a[240:241], v245 offset:21504\n"
        "ds_read_b64_tr_b16 a[242:243], v246 offset:21504\n"
        "v_mfma_f32_16x16x32_bf16 v[100:103], a[204:207], v[36:39], 0\n"
        "v_mul_f32 v223, v10, v93\n"
        "v_fma_f32 v222, -v5, v93, v222\n"
        "v_mfma_f32_16x16x32_bf16 v[64:67], a[244:247], v[20:23], v[64:67]\n"
        "v_fmac_f32 v223, v5, v61\n"
        "v_fma_f32 v224, v8, v63, v222\n"
        "v_mfma_f32_16x16x32_bf16 v[96:99], a[244:247], v[40:43], v[96:99]\n"
        "ds_read_b64_tr_b16 a[244:245], v245 offset:25600\n"
        "ds_read_b64_tr_b16 a[246:247], v246 offset:25600\n"
        "s_waitcnt lgkmcnt(15)\n"
        "v_mfma_f32_16x16x32_bf16 v[68:71], a[208:211], v[20:23], v[68:71]\n"
        "v_fma_f32 v225, v243, v95, v223\n"
        "v_fma_f32 v219, v6, v94, v92\n"
        "v_mfma_f32_16x16x32_bf16 v[100:103], a[208:211], v[40:43], v[100:103]\n"
        "v_fma_f32 v224, -v9, v95, v224\n"
        "v_fmac_f32 v225, v9, v63\n"
        "v_mfma_f32_16x16x32_bf16 v[64:67], a[248:251], v[24:27], v[64:67]\n"
        "v_fmac_f32 v219, v7, v62\n"
        "v_fma_f32 v226, 2.0, v222, -v224\n"
        "v_mfma_f32_16x16x32_bf16 v[96:99], a[248:251], v[44:47], v[96:99]\n"
        "buffer_store_dwordx4 a[12:15], v153, s[40:43], s84 offen offset:3072 sc1\n"
        "v_mfma_f32_16x16x32_bf16 v[68:71], a[212:215], v[24:27], v[68:71]\n"
        "ds_read_b64_tr_b16 a[248:249], v245 offset:25856\n"
        "ds_read_b64_tr_b16 a[250:251], v246 offset:25856\n"
        "v_mfma_f32_16x16x32_bf16 v[100:103], a[212:215], v[44:47], v[100:103]\n"
        "v_fma_f32 v227, 2.0, v223, -v225\n"
        "v_fma_f32 v218, -v7, v94, v218\n"
        "v_mfma_f32_16x16x32_bf16 v[64:67], a[252:255], v[28:31], v[64:67]\n"
        "v_fma_f32 v221, 2.0, v92, -v219\n"
        "v_cndmask_b32_e64 v219, v219, v94, s[74:75]\n"
        "v_mfma_f32_16x16x32_bf16 v[96:99], a[252:255], v[48:51], v[96:99]\n"
        "ds_read_b64_tr_b16 a[252:253], v245 offset:26112\n"
        "ds_read_b64_tr_b16 a[254:255], v246 offset:26112\n"
        "s_waitcnt lgkmcnt(15)\n"
        "v_mfma_f32_16x16x32_bf16 v[68:71], a[216:219], v[28:31], v[68:71]\n"
        "v_fma_f32 v220, 2.0, v60, -v218\n"
        "v_add_f32 v128, v218, v224\n"
        "v_mfma_f32_16x16x32_bf16 v[100:103], a[216:219], v[48:51], v[100:103]\n"
        "v_add_f32 v129, v219, v225\n"
        "v_sub_f32 v130, v218, v224\n"
        "s_waitcnt lgkmcnt(14)\n"
        "v_mfma_f32_16x16x32_bf16 v[72:75], a[224:227], v[16:19], 0\n"
        "v_sub_f32 v131, v225, v219\n"
        "v_cndmask_b32_e64 v221, v221, -v220, s[74:75]\n"
        "v_mfma_f32_16x16x32_bf16 v[104:107], a[224:227], v[36:39], 0\n"
        "buffer_store_dwordx4 a[16:19], v154, s[40:43], s84 offen offset:0 sc1\n"
        "v_mfma_f32_16x16x32_bf16 v[64:67], a[200:203], v[32:35], v[64:67]\n"
        "v_cndmask_b32_e64 v220, v220, v92, s[74:75]\n"
        "v_fma_f32 v133, v244, v221, -v226\n"
        "v_mfma_f32_16x16x32_bf16 v[96:99], a[200:203], v[52:55], v[96:99]\n"
        "ds_read_b64_tr_b16 a[200:201], v245 offset:26368\n"
        "ds_read_b64_tr_b16 a[202:203], v246 offset:26368\n"
        "v_mfma_f32_16x16x32_bf16 v[68:71], a[220:223], v[32:35], v[68:71]\n"
        "ds_read_b64_tr_b16 a[204:205], v245 offset:26624\n"
        "ds_read_b64_tr_b16 a[206:207], v246 offset:26624\n"
        "v_mfma_f32_16x16x32_bf16 v[100:103], a[220:223], v[52:55], v[100:103]\n"
        "ds_read_b64_tr_b16 a[208:209], v245 offset:30720\n"
        "ds_read_b64_tr_b16 a[210:211], v246 offset:30720\n"
        "s_waitcnt lgkmcnt(15)\n"
        "v_mfma_f32_16x16x32_bf16 v[72:75], a[228:231], v[20:23], v[72:75]\n"
        "ds_read_b64_tr_b16 a[212:213], v245 offset:30976\n"
        "ds_read_b64_tr_b16 a[214:215], v246 offset:30976\n"
        "v_mfma_f32_16x16x32_bf16 v[104:107], a[228:231], v[40:43], v[104:107]\n"
        "ds_read_b64_tr_b16 a[216:217], v245 offset:31232\n"
        "ds_read_b64_tr_b16 a[218:219], v246 offset:31232\n"
        "v_mfma_f32_16x16x32_bf16 v[72:75], a[232:235], v[24:27], v[72:75]\n"
        "ds_read_b64_tr_b16 a[220:221], v245 offset:31488\n"
        "ds_read_b64_tr_b16 a[222:223], v246 offset:31488\n"
        "v_mfma_f32_16x16x32_bf16 v[104:107], a[232:235], v[44:47], v[104:107]\n"
        "buffer_store_dwordx4 a[20:23], v154, s[40:43], s84 offen offset:1024 sc1\n"
        "s_waitcnt lgkmcnt(15)\n"
        "v_mfma_f32_16x16x32_bf16 v[72:75], a[236:239], v[28:31], v[72:75]\n"
        "ds_read_b64_tr_b16 a[224:225], v245 offset:31744\n"
        "ds_read_b64_tr_b16 a[226:227], v246 offset:31744\n"
        "v_mfma_f32_16x16x32_bf16 v[104:107], a[236:239], v[48:51], v[104:107]\n"
        "ds_read_b64_tr_b16 a[228:229], v245 offset:35840\n"
        "ds_read_b64_tr_b16 a[230:231], v246 offset:35840\n"
        "v_mfma_f32_16x16x32_bf16 v[72:75], a[240:243], v[32:35], v[72:75]\n"
        "ds_read_b64_tr_b16 a[232:233], v245 offset:36096\n"
        "ds_read_b64_tr_b16 a[234:235], v246 offset:36096\n"
        "v_mfma_f32_16x16x32_bf16 v[104:107], a[240:243], v[52:55], v[104:107]\n"
        "ds_read_b64_tr_b16 a[236:237], v245 offset:36352\n"
        "ds_read_b64_tr_b16 a[238:239], v246 offset:36352\n"
        "v_mfma_f32_16x16x32_bf16 v[76:79], a[244:247], v[16:19], 0\n"
        "ds_read_b64_tr_b16 a[240:241], v245 offset:36608\n"
        "ds_read_b64_tr_b16 a[242:243], v246 offset:36608\n"
        "v_mfma_f32_16x16x32_bf16 v[108:111], a[244:247], v[36:39], 0\n"
        "ds_read_b64_tr_b16 a[244:245], v245 offset:36864\n"
        "ds_read_b64_tr_b16 a[246:247], v246 offset:36864\n"
        "s_waitcnt lgkmcnt(15)\n"
        "v_mfma_f32_16x16x32_bf16 v[76:79], a[248:251], v[20:23], v[76:79]\n"
        "v_mul_f32 v212, v4, v65\n"
        "v_mul_f32 v213, v10, v97\n"
        "v_mfma_f32_16x16x32_bf16 v[108:111], a[248:251], v[40:43], v[108:111]\n"
        "buffer_store_dwordx4 a[24:27], v154, s[40:43], s84 offen offset:2048 sc1\n"
        "v_mfma_f32_16x16x32_bf16 v[80:83], a[208:211], v[16:19], 0\n"
        "v_mul_f32 v222, v4, v69\n"
        "v_mul_f32 v223, v10, v101\n"
        "v_mfma_f32_16x16x32_bf16 v[112:115], a[208:211], v[36:39], 0\n"
        "v_fma_f32 v212, -v5, v97, v212\n"
        "v_fmac_f32 v213, v5, v65\n"
        "s_waitcnt lgkmcnt(8)\n"
        "v_mfma_f32_16x16x32_bf16 v[84:87], a[228:231], v[16:19], 0\n"
        "v_fma_f32 v222, -v5, v101, v222\n"
        "v_fmac_f32 v223, v5, v69\n"
        "v_mfma_f32_16x16x32_bf16 v[116:119], a[228:231], v[36:39], 0\n"
        "v_fma_f32 v214, v8, v67, v212\n"
        "v_fma_f32 v215, v243, v99, v213\n"
        "v_mfma_f32_16x16x32_bf16 v[76:79], a[252:255], v[24:27], v[76:79]\n"
        "v_fma_f32 v224, v8, v71, v222\n"
        "v_fma_f32 v225, v243, v103, v223\n"
        "v_mfma_f32_16x16x32_bf16 v[108:111], a[252:255], v[44:47], v[108:111]\n"
        "v_fma_f32 v209, v6, v98, v96\n"
        "v_fma_f32 v219, v6, v102, v100\n"
        "v_mfma_f32_16x16x32_bf16 v[80:83], a[212:215], v[20:23], v[80:83]\n"
        "v_fma_f32 v214, -v9, v99, v214\n"
        "v_fmac_f32 v215, v9, v67\n"
        "v_mfma_f32_16x16x32_bf16 v[112:115], a[212:215], v[40:43], v[112:115]\n"
        "buffer_store_dwordx4 a[28:31], v154, s[40:43], s84 offen offset:3072 sc1\n"
        "s_waitcnt lgkmcnt(6)\n"
        "v_mfma_f32_16x16x32_bf16 v[84:87], a[232:235], v[20:23], v[84:87]\n"
        "v_fma_f32 v224, -v9, v103, v224\n"
        "v_fmac_f32 v225, v9, v71\n"
        "v_mfma_f32_16x16x32_bf16 v[116:119], a[232:235], v[40:43], v[116:119]\n"
        "v_fmac_f32 v209, v7, v66\n"
        "v_fmac_f32 v219, v7, v70\n"
        "v_mfma_f32_16x16x32_bf16 v[76:79], a[200:203], v[28:31], v[76:79]\n"
        "v_fma_f32 v208, v6, v66, v64\n"
        "v_fma_f32 v218, v6, v70, v68\n"
        "v_mfma_f32_16x16x32_bf16 v[108:111], a[200:203], v[48:51], v[108:111]\n"
        "v_add_f32 v132, v220, v227\n"
        "v_sub_f32 v134, v220, v227\n"
        "v_mfma_f32_16x16x32_bf16 v[80:83], a[216:219], v[24:27], v[80:83]\n"
        "v_fma_f32 v135, -v244, v226, -v221\n"
        "v_fma_f32 v216, 2.0, v212, -v214\n"
        "v_mfma_f32_16x16x32_bf16 v[112:115], a[216:219], v[44:47], v[112:115]\n"
        "v_fma_f32 v217, 2.0, v213, -v215\n"
        "v_fma_f32 v226, 2.0, v222, -v224\n"
        "s_waitcnt lgkmcnt(4)\n"
        "v_mfma_f32_16x16x32_bf16 v[84:87], a[236:239], v[24:27], v[84:87]\n"
        "v_fma_f32 v227, 2.0, v223, -v225\n"
        "v_mul_f32 v212, v4, v73\n"
        "v_mfma_f32_16x16x32_bf16 v[116:119], a[236:239], v[44:47], v[116:119]\n"
        "buffer_store_dwordx4 a[32:35], v153, s[40:43], s85 offen offset:0 sc1\n"
        "v_mfma_f32_16x16x32_bf16 v[76:79], a[204:207], v[32:35], v[76:79]\n"
        "v_mul_f32 v213, v10, v105\n"
        "v_fma_f32 v211, 2.0, v96, -v209\n"
        "v_mfma_f32_16x16x32_bf16 v[108:111], a[204:207], v[52:55], v[108:111]\n"
        "v_fma_f32 v221, 2.0, v100, -v219\n"
        "v_fma_f32 v208, -v7, v98, v208\n"
        "v_mfma_f32_16x16x32_bf16 v[80:83], a[220:223], v[28:31], v[80:83]\n"
        "s_nop 1\n"
        "v_mul_f32 v222, v4, v77\n"
        "v_cndmask_b32_e64 v209, v209, v98, s[74:75]\n"
        "v_mfma_f32_16x16x32_bf16 v[112:115], a[220:223], v[48:51], v[112:115]\n"
        "v_mul_f32 v223, v10, v109\n"
        "v_fma_f32 v218, -v7, v102, v218\n"
        "s_waitcnt lgkmcnt(2)\n"
        "v_mfma_f32_16x16x32_bf16 v[84:87], a[240:243], v[28:31], v[84:87]\n"
        "v_cndmask_b32_e64 v219, v219, v102, s[74:75]\n"
        "v_fma_f32 v212, -v5, v105, v212\n"
        "v_mfma_f32_16x16x32_bf16 v[116:119], a[240:243], v[48:51], v[116:119]\n"
        "v_fmac_f32 v213, v5, v73\n"
        "v_fma_f32 v222, -v5, v109, v222\n"
        "v_mfma_f32_16x16x32_bf16 v[80:83], a[224:227], v[32:35], v[80:83]\n"
        "v_fmac_f32 v223, v5, v77\n"
        "v_cvt_pk_bf16_f32 v180, v121, v129\n"
        "v_mfma_f32_16x16x32_bf16 v[112:115], a[224:227], v[52:55], v[112:115]\n"
        "buffer_store_dwordx4 a[36:39], v153, s[40:43], s85 offen offset:1024 sc1\n"
        "s_waitcnt lgkmcnt(0)\n"
        "v_mfma_f32_16x16x32_bf16 v[84:87], a[244:247], v[32:35], v[84:87]\n"
        "v_cvt_pk_bf16_f32 v188, v123, v131\n"
        "v_fma_f32 v210, 2.0, v64, -v208\n"
        "v_mfma_f32_16x16x32_bf16 v[116:119], a[244:247], v[52:55], v[116:119]\n"
        "v_add_f32 v137, v209, v215\n"
        "v_sub_f32 v139, v215, v209\n"
        "v_fma_f32 v220, 2.0, v68, -v218\n"
        "v_add_f32 v121, v219, v225\n"
        "v_sub_f32 v123, v225, v219\n"
        "v_cvt_pk_bf16_f32 v176, v120, v128\n"
        "v_cvt_pk_bf16_f32 v184, v122, v130\n"
        "v_fma_f32 v209, v6, v106, v104\n"
        "v_fma_f32 v219, v6, v110, v108\n"
        "v_add_f32 v136, v208, v214\n"
        "v_sub_f32 v138, v208, v214\n"
        "v_add_f32 v120, v218, v224\n"
        "v_sub_f32 v122, v218, v224\n"
        "v_fma_f32 v214, v8, v75, v212\n"
        "v_fma_f32 v215, v243, v107, v213\n"
        "v_fma_f32 v224, v8, v79, v222\n"
        "v_fma_f32 v225, v243, v111, v223\n"
        "v_cndmask_b32_e64 v211, v211, -v210, s[74:75]\n"
        "v_cndmask_b32_e64 v221, v221, -v220, s[74:75]\n"
        "v_fma_f32 v208, v6, v74, v72\n"
        "v_fmac_f32 v209, v7, v74\n"
        "v_fma_f32 v218, v6, v78, v76\n"
        "v_fmac_f32 v219, v7, v78\n"
        "v_cndmask_b32_e64 v210, v210, v96, s[74:75]\n"
        "v_cndmask_b32_e64 v220, v220, v100, s[74:75]\n"
        "v_fma_f32 v214, -v9, v107, v214\n"
        "v_fmac_f32 v215, v9, v75\n"
        "v_fma_f32 v224, -v9, v111, v224\n"
        "v_fmac_f32 v225, v9, v79\n"
        "v_cvt_pk_bf16_f32 v196, v125, v133\n"
        "v_cvt_pk_bf16_f32 v204, v127, v135\n"
        "v_fma_f32 v141, v244, v211, -v216\n"
        "v_fma_f32 v143, -v244, v216, -v211\n"
        "v_fma_f32 v125, v244, v221, -v226\n"
        "v_fma_f32 v127, -v244, v226, -v221\n"
        "v_cvt_pk_bf16_f32 v192, v124, v132\n"
        "v_cvt_pk_bf16_f32 v200, v126, v134\n"
        "v_fma_f32 v208, -v7, v106, v208\n"
        "v_fma_f32 v211, 2.0, v104, -v209\n"
        "v_fma_f32 v218, -v7, v110, v218\n"
        "v_fma_f32 v221, 2.0, v108, -v219\n"
        "v_add_f32 v140, v210, v217\n"
        "v_sub_f32 v142, v210, v217\n"
        "v_add_f32 v124, v220, v227\n"
        "v_sub_f32 v126, v220, v227\n"
        "v_cndmask_b32_e64 v209, v209, v106, s[74:75]\n"
        "v_cndmask_b32_e64 v219, v219, v110, s[74:75]\n"
        "v_fma_f32 v216, 2.0, v212, -v214\n"
        "v_fma_f32 v217, 2.0, v213, -v215\n"
        "v_fma_f32 v226, 2.0, v222, -v224\n"
        "v_fma_f32 v227, 2.0, v223, -v225\n"
        "v_mul_f32 v212, v4, v81\n"
        "v_mul_f32 v213, v10, v113\n"
        "v_mul_f32 v222, v4, v85\n"
        "v_mul_f32 v223, v10, v117\n"
        "v_cvt_pk_bf16_f32 v177, v136, v120\n"
        "v_cvt_pk_bf16_f32 v181, v137, v121\n"
        "v_cvt_pk_bf16_f32 v185, v138, v122\n"
        "v_cvt_pk_bf16_f32 v189, v139, v123\n"
        "v_fma_f32 v210, 2.0, v72, -v208\n"
        "v_fma_f32 v220, 2.0, v76, -v218\n"
        "v_add_f32 v128, v208, v214\n"
        "v_add_f32 v129, v209, v215\n"
        "v_sub_f32 v130, v208, v214\n"
        "v_sub_f32 v131, v215, v209\n"
        "v_add_f32 v136, v218, v224\n"
        "v_add_f32 v137, v219, v225\n"
        "v_sub_f32 v138, v218, v224\n"
        "v_sub_f32 v139, v225, v219\n"
        "v_fma_f32 v208, v6, v82, v80\n"
        "v_fma_f32 v209, v6, v114, v112\n"
        "v_fma_f32 v218, v6, v86, v84\n"
        "v_fma_f32 v219, v6, v118, v116\n"
        "v_fma_f32 v212, -v5, v113, v212\n"
        "v_fmac_f32 v213, v5, v81\n"
        "v_fma_f32 v222, -v5, v117, v222\n"
        "v_fmac_f32 v223, v5, v85\n"
        "v_cndmask_b32_e64 v211, v211, -v210, s[74:75]\n"
        "v_cndmask_b32_e64 v221, v221, -v220, s[74:75]\n"
        "v_cndmask_b32_e64 v210, v210, v104, s[74:75]\n"
        "v_cndmask_b32_e64 v220, v220, v108, s[74:75]\n"
        "v_fma_f32 v208, -v7, v114, v208\n"
        "v_fmac_f32 v209, v7, v82\n"
        "v_fma_f32 v218, -v7, v118, v218\n"
        "v_fmac_f32 v219, v7, v86\n"
        "v_fma_f32 v214, v8, v83, v212\n"
        "v_fma_f32 v215, v243, v115, v213\n"
        "v_fma_f32 v224, v8, v87, v222\n"
        "v_fma_f32 v225, v243, v119, v223\n"
        "v_cvt_pk_bf16_f32 v193, v140, v124\n"
        "v_cvt_pk_bf16_f32 v197, v141, v125\n"
        "v_cvt_pk_bf16_f32 v201, v142, v126\n"
        "v_cvt_pk_bf16_f32 v205, v143, v127\n"
        "v_add_f32 v132, v210, v217\n"
        "v_fma_f32 v133, v244, v211, -v216\n"
        "v_sub_f32 v134, v210, v217\n"
        "v_fma_f32 v135, -v244, v216, -v211\n"
        "v_add_f32 v140, v220, v227\n"
        "v_fma_f32 v141, v244, v221, -v226\n"
        "v_sub_f32 v142, v220, v227\n"
        "v_fma_f32 v143, -v244, v226, -v221\n"
        "v_fma_f32 v210, 2.0, v80, -v208\n"
        "v_fma_f32 v211, 2.0, v112, -v209\n"
        "v_fma_f32 v220, 2.0, v84, -v218\n"
        "v_fma_f32 v221, 2.0, v116, -v219\n"
        "v_fma_f32 v214, -v9, v115, v214\n"
        "v_fmac_f32 v215, v9, v83\n"
        "v_fma_f32 v224, -v9, v119, v224\n"
        "v_fmac_f32 v225, v9, v87\n"
        "v_cndmask_b32_e64 v211, v211, -v210, s[74:75]\n"
        "v_cndmask_b32_e64 v221, v221, -v220, s[74:75]\n"
        "v_cndmask_b32_e64 v209, v209, v114, s[74:75]\n"
        "v_cndmask_b32_e64 v210, v210, v112, s[74:75]\n"
        "v_fma_f32 v216, 2.0, v212, -v214\n"
        "v_fma_f32 v217, 2.0, v213, -v215\n"
        "v_cndmask_b32_e64 v219, v219, v118, s[74:75]\n"
        "v_cndmask_b32_e64 v220, v220, v116, s[74:75]\n"
        "v_fma_f32 v226, 2.0, v222, -v224\n"
        "v_fma_f32 v227, 2.0, v223, -v225\n"
        "v_cvt_pk_bf16_f32 v178, v128, v136\n"
        "v_cvt_pk_bf16_f32 v182, v129, v137\n"
        "v_cvt_pk_bf16_f32 v186, v130, v138\n"
        "v_cvt_pk_bf16_f32 v190, v131, v139\n"
        "v_cvt_pk_bf16_f32 v194, v132, v140\n"
        "v_cvt_pk_bf16_f32 v198, v133, v141\n"
        "v_cvt_pk_bf16_f32 v202, v134, v142\n"
        "v_cvt_pk_bf16_f32 v206, v135, v143\n"
        "v_add_f32 v120, v208, v214\n"
        "v_add_f32 v121, v209, v215\n"
        "v_sub_f32 v122, v208, v214\n"
        "v_sub_f32 v123, v215, v209\n"
        "v_add_f32 v124, v210, v217\n"
        "v_fma_f32 v125, v244, v211, -v216\n"
        "v_sub_f32 v126, v210, v217\n"
        "v_fma_f32 v127, -v244, v216, -v211\n"
        "v_add_f32 v128, v218, v224\n"
        "v_add_f32 v129, v219, v225\n"
        "v_sub_f32 v130, v218, v224\n"
        "v_sub_f32 v131, v225, v219\n"
        "v_add_f32 v132, v220, v227\n"
        "v_fma_f32 v133, v244, v221, -v226\n"
        "v_sub_f32 v134, v220, v227\n"
        "v_fma_f32 v135, -v244, v226, -v221\n"
        "v_cvt_pk_bf16_f32 v179, v120, v128\n"
        "v_cvt_pk_bf16_f32 v183, v121, v129\n"
        "v_cvt_pk_bf16_f32 v187, v122, v130\n"
        "v_cvt_pk_bf16_f32 v191, v123, v131\n"
        "v_cvt_pk_bf16_f32 v195, v124, v132\n"
        "v_cvt_pk_bf16_f32 v199, v125, v133\n"
        "v_cvt_pk_bf16_f32 v203, v126, v134\n"
        "v_cvt_pk_bf16_f32 v207, v127, v135\n"
        "buffer_store_dwordx4 a[40:43], v152, s[40:43], s85 offen offset:2048 sc1\n"
        "v_permlane16_swap_b32 v176, v180\n"
        "v_permlane16_swap_b32 v184, v188\n"
        "v_permlane16_swap_b32 v192, v196\n"
        "v_permlane16_swap_b32 v200, v204\n"
        "v_permlane16_swap_b32 v177, v181\n"
        "v_permlane16_swap_b32 v185, v189\n"
        "v_permlane16_swap_b32 v193, v197\n"
        "v_permlane16_swap_b32 v201, v205\n"
        "v_permlane16_swap_b32 v178, v182\n"
        "v_permlane16_swap_b32 v186, v190\n"
        "v_permlane16_swap_b32 v194, v198\n"
        "v_permlane16_swap_b32 v202, v206\n"
        "v_permlane16_swap_b32 v179, v183\n"
        "v_permlane16_swap_b32 v187, v191\n"
        "v_permlane16_swap_b32 v195, v199\n"
        "v_permlane16_swap_b32 v203, v207\n"
        "s_cselect_b32 s68, s68, 0x40000000\n"
        "buffer_store_dwordx4 v[176:179], v248, s[40:43], s70 offen sc1\n"
        "buffer_store_dwordx4 v[180:183], v249, s[40:43], s70 offen sc1\n"
        "buffer_store_dwordx4 v[184:187], v250, s[40:43], s70 offen sc1\n"
        "buffer_store_dwordx4 v[188:191], v251, s[40:43], s70 offen sc1\n"
        "buffer_store_dwordx4 v[192:195], v252, s[40:43], s70 offen sc1\n"
        "buffer_store_dwordx4 v[196:199], v253, s[40:43], s70 offen sc1\n"
        "buffer_store_dwordx4 v[200:203], v254, s[40:43], s70 offen sc1\n"
        "buffer_store_dwordx4 v[204:207], v255, s[40:43], s70 offen sc1\n"
        "v_lshrrev_b32 v240, 2, v1\n"
        "v_and_b32 v241, 3, v1\n"
        "v_cvt_f32_u32 v6, v2\n"
        "v_lshl_add_u32 v240, v240, 3, v241\n"
        "v_mul_f32 v6, 0x41000000, v6\n"
        "v_cvt_f32_u32 v4, v240\n"
        "s_cmp_eq_u32 s91, 80\n"
        "v_mul_f32 v7, 0x3d000000, v4\n"
        "v_add_f32 v5, 4.0, v4\n"
        "v_mul_f32 v9, v6, v7\n"
        "v_mul_f32 v8, 0x3d000000, v5\n"
        "v_fmamk_f32 v208, v7, 0x00000000, v9\n"
        "v_fmamk_f32 v209, v7, 0x3f800000, v9\n"
        "v_fmamk_f32 v216, v7, 0x40000000, v9\n"
        "v_fmamk_f32 v217, v7, 0x40400000, v9\n"
        "v_fmamk_f32 v224, v7, 0x40800000, v9\n"
        "v_fmamk_f32 v225, v7, 0x40a00000, v9\n"
        "v_fmamk_f32 v232, v7, 0x40c00000, v9\n"
        "v_fmamk_f32 v233, v7, 0x40e00000, v9\n"
        "v_sin_f32 v212, v208\n"
        "v_sin_f32 v213, v209\n"
        "v_sin_f32 v220, v216\n"
        "v_sin_f32 v221, v217\n"
        "v_sin_f32 v228, v224\n"
        "v_sin_f32 v229, v225\n"
        "v_sin_f32 v236, v232\n"
        "v_sin_f32 v237, v233\n"
        "v_cos_f32 v210, v208\n"
        "v_cos_f32 v218, v216\n"
        "v_cos_f32 v226, v224\n"
        "v_cos_f32 v234, v232\n"
        "v_cvt_pk_bf16_f32 v208, -v212, -v213\n"
        "v_cvt_pk_bf16_f32 v216, -v220, -v221\n"
        "v_cvt_pk_bf16_f32 v224, -v228, -v229\n"
        "v_cvt_pk_bf16_f32 v232, -v236, -v237\n"
        "v_mul_f32 v10, v6, v8\n"
        "v_cos_f32 v211, v209\n"
        "v_accvgpr_write_b32 a216, v208\n"
        "v_cos_f32 v219, v217\n"
        "v_accvgpr_write_b32 a217, v216\n"
        "v_cos_f32 v227, v225\n"
        "v_accvgpr_write_b32 a218, v224\n"
        "v_cos_f32 v235, v233\n"
        "v_accvgpr_write_b32 a219, v232\n"
        "v_fmamk_f32 v208, v8, 0x00000000, v10\n"
        "v_fmamk_f32 v209, v8, 0x3f800000, v10\n"
        "v_fmamk_f32 v216, v8, 0x40000000, v10\n"
        "v_fmamk_f32 v217, v8, 0x40400000, v10\n"
        "v_fmamk_f32 v224, v8, 0x40800000, v10\n"
        "v_fmamk_f32 v225, v8, 0x40a00000, v10\n"
        "v_fmamk_f32 v232, v8, 0x40c00000, v10\n"
        "v_fmamk_f32 v233, v8, 0x40e00000, v10\n"
        "v_mul_f32 v7, 0x3d000000, v7\n"
        "v_mul_f32 v8, 0x3d000000, v8\n"
        "s_cselect_b32 s70, 0, 1\n"
        "s_add_u32 s71, s91, s3\n"
        "v_mul_f32 v9, v6, v7\n"
        "v_mul_f32 v10, v6, v8\n"
        "s_min_u32 s70, s70, s71\n"
        "s_cmp_eq_u32 s70, 0\n"
        "v_cvt_pk_bf16_f32 v214, v210, v211\n"
        "v_cvt_pk_bf16_f32 v215, v212, v213\n"
        "v_cvt_pk_bf16_f32 v222, v218, v219\n"
        "v_cvt_pk_bf16_f32 v223, v220, v221\n"
        "v_cvt_pk_bf16_f32 v230, v226, v227\n"
        "v_cvt_pk_bf16_f32 v231, v228, v229\n"
        "v_cvt_pk_bf16_f32 v238, v234, v235\n"
        "v_cvt_pk_bf16_f32 v239, v236, v237\n"
        "v_fmamk_f32 v116, v7, 0x00000000, v9\n"
        "v_fmamk_f32 v117, v7, 0x3f800000, v9\n"
        "v_fmamk_f32 v118, v7, 0x40000000, v9\n"
        "v_fmamk_f32 v119, v7, 0x40400000, v9\n"
        "v_fmamk_f32 v132, v7, 0x40800000, v9\n"
        "v_fmamk_f32 v133, v7, 0x40a00000, v9\n"
        "v_fmamk_f32 v134, v7, 0x40c00000, v9\n"
        "v_fmamk_f32 v135, v7, 0x40e00000, v9\n"
        "v_fmamk_f32 v124, v8, 0x00000000, v10\n"
        "v_fmamk_f32 v125, v8, 0x3f800000, v10\n"
        "v_fmamk_f32 v126, v8, 0x40000000, v10\n"
        "v_fmamk_f32 v127, v8, 0x40400000, v10\n"
        "v_fmamk_f32 v140, v8, 0x40800000, v10\n"
        "v_fmamk_f32 v141, v8, 0x40a00000, v10\n"
        "v_fmamk_f32 v142, v8, 0x40c00000, v10\n"
        "v_fmamk_f32 v143, v8, 0x40e00000, v10\n"
        "v_cos_f32 v210, v208\n"
        "v_sin_f32 v212, v208\n"
        "v_cos_f32 v211, v209\n"
        "v_sin_f32 v213, v209\n"
        "v_cos_f32 v218, v216\n"
        "v_sin_f32 v220, v216\n"
        "v_cos_f32 v219, v217\n"
        "v_sin_f32 v221, v217\n"
        "v_cos_f32 v226, v224\n"
        "v_sin_f32 v228, v224\n"
        "v_cos_f32 v227, v225\n"
        "v_sin_f32 v229, v225\n"
        "v_cos_f32 v234, v232\n"
        "v_sin_f32 v236, v232\n"
        "v_cos_f32 v235, v233\n"
        "v_sin_f32 v237, v233\n"
        "s_cselect_b32 s73, 0x3a64f92e, s73\n"
        "s_cmp_lg_u32 s70, 0\n"
        "v_cos_f32 v112, v116\n"
        "v_cos_f32 v113, v117\n"
        "v_cos_f32 v114, v118\n"
        "v_cos_f32 v115, v119\n"
        "v_cos_f32 v128, v132\n"
        "v_cos_f32 v129, v133\n"
        "v_cos_f32 v130, v134\n"
        "v_cos_f32 v131, v135\n"
        "v_cos_f32 v120, v124\n"
        "v_cos_f32 v121, v125\n"
        "v_cos_f32 v122, v126\n"
        "v_cos_f32 v123, v127\n"
        "v_cos_f32 v136, v140\n"
        "v_cos_f32 v137, v141\n"
        "v_cos_f32 v138, v142\n"
        "v_cos_f32 v139, v143\n"
        "v_sin_f32 v116, v116\n"
        "v_sin_f32 v117, v117\n"
        "v_sin_f32 v118, v118\n"
        "v_sin_f32 v119, v119\n"
        "v_sin_f32 v132, v132\n"
        "v_sin_f32 v133, v133\n"
        "v_sin_f32 v134, v134\n"
        "v_sin_f32 v135, v135\n"
        "v_sin_f32 v124, v124\n"
        "v_sin_f32 v125, v125\n"
        "v_sin_f32 v126, v126\n"
        "v_sin_f32 v127, v127\n"
        "v_sin_f32 v140, v140\n"
        "v_sin_f32 v141, v141\n"
        "v_sin_f32 v142, v142\n"
        "v_sin_f32 v143, v143\n"
        "s_cselect_b32 s73, 0x3aa1e89b, s73\n"
        "v_accvgpr_write_b32 a200, v214\n"
        "v_accvgpr_write_b32 a208, v215\n"
        "v_accvgpr_write_b32 a201, v222\n"
        "v_accvgpr_write_b32 a209, v223\n"
        "v_accvgpr_write_b32 a202, v230\n"
        "v_accvgpr_write_b32 a210, v231\n"
        "v_accvgpr_write_b32 a203, v238\n"
        "v_accvgpr_write_b32 a211, v239\n"
        "v_mov_b32 v242, s73\n"
        "v_cvt_pk_bf16_f32 v214, v210, v211\n"
        "v_cvt_pk_bf16_f32 v215, v212, v213\n"
        "v_cvt_pk_bf16_f32 v208, -v212, -v213\n"
        "v_cvt_pk_bf16_f32 v222, v218, v219\n"
        "v_cvt_pk_bf16_f32 v223, v220, v221\n"
        "v_cvt_pk_bf16_f32 v216, -v220, -v221\n"
        "v_cvt_pk_bf16_f32 v230, v226, v227\n"
        "v_cvt_pk_bf16_f32 v231, v228, v229\n"
        "v_cvt_pk_bf16_f32 v224, -v228, -v229\n"
        "v_cvt_pk_bf16_f32 v238, v234, v235\n"
        "v_cvt_pk_bf16_f32 v239, v236, v237\n"
        "v_cvt_pk_bf16_f32 v232, -v236, -v237\n"
        "v_accvgpr_write_b32 a204, v214\n"
        "v_accvgpr_write_b32 a212, v215\n"
        "v_accvgpr_write_b32 a220, v208\n"
        "v_accvgpr_write_b32 a205, v222\n"
        "v_accvgpr_write_b32 a213, v223\n"
        "v_accvgpr_write_b32 a221, v216\n"
        "v_accvgpr_write_b32 a206, v230\n"
        "v_accvgpr_write_b32 a214, v231\n"
        "v_accvgpr_write_b32 a222, v224\n"
        "v_accvgpr_write_b32 a207, v238\n"
        "v_accvgpr_write_b32 a215, v239\n"
        "v_accvgpr_write_b32 a223, v232\n"
        "v_pk_mul_f32 v[112:113], v[112:113], v[242:243] op_sel_hi:[1,0]\n"
        "v_pk_mul_f32 v[114:115], v[114:115], v[242:243] op_sel_hi:[1,0]\n"
        "v_pk_mul_f32 v[116:117], v[116:117], v[242:243] op_sel_hi:[1,0]\n"
        "v_pk_mul_f32 v[118:119], v[118:119], v[242:243] op_sel_hi:[1,0]\n"
        "v_pk_mul_f32 v[128:129], v[128:129], v[242:243] op_sel_hi:[1,0]\n"
        "v_pk_mul_f32 v[130:131], v[130:131], v[242:243] op_sel_hi:[1,0]\n"
        "v_pk_mul_f32 v[132:133], v[132:133], v[242:243] op_sel_hi:[1,0]\n"
        "v_pk_mul_f32 v[134:135], v[134:135], v[242:243] op_sel_hi:[1,0]\n"
        "v_pk_mul_f32 v[120:121], v[120:121], v[242:243] op_sel_hi:[1,0]\n"
        "v_pk_mul_f32 v[122:123], v[122:123], v[242:243] op_sel_hi:[1,0]\n"
        "v_pk_mul_f32 v[124:125], v[124:125], v[242:243] op_sel_hi:[1,0]\n"
        "v_pk_mul_f32 v[126:127], v[126:127], v[242:243] op_sel_hi:[1,0]\n"
        "v_pk_mul_f32 v[136:137], v[136:137], v[242:243] op_sel_hi:[1,0]\n"
        "v_pk_mul_f32 v[138:139], v[138:139], v[242:243] op_sel_hi:[1,0]\n"
        "v_pk_mul_f32 v[140:141], v[140:141], v[242:243] op_sel_hi:[1,0]\n"
        "v_pk_mul_f32 v[142:143], v[142:143], v[242:243] op_sel_hi:[1,0]\n"
        "s_waitcnt vmcnt(0)\n"
        "s_barrier\n"
        "v_mov_b32 v208, s64\n"
        "v_mov_b32 v209, s65\n"
        "v_cmp_eq_u32 vcc, 0, v0\n"
        "v_mov_b32 v210, 0x40000000\n"
        "s_nop 0\n"
        "v_cndmask_b32 v210, v210, v0, vcc\n"
        "buffer_store_dwordx2 v[208:209], v210, s[40:43], s68 offen sc1\n"
        "s_branch L16_%=\n"
        "FFT_%=:\n"
        "s_cmp_eq_u32 s91, 80\n"
        "s_cselect_b32 s70, 0, 1\n"
        "s_add_u32 s71, s91, s3\n"
        "s_min_u32 s70, s70, s71\n"
        "s_cmp_eq_u32 s70, 0\n"
        "s_cselect_b32 s73, 0x3a64f92e, s73\n"
        "s_cmp_lg_u32 s70, 0\n"
        "s_cselect_b32 s73, 0x3aa1e89b, s73\n"
        "v_mov_b32 v242, s73\n"
        "v_lshrrev_b32 v240, 2, v1\n"
        "v_and_b32 v241, 3, v1\n"
        "v_lshl_add_u32 v240, v240, 3, v241\n"
        "v_cvt_f32_u32 v4, v240\n"
        "v_add_f32 v5, 4.0, v4\n"
        "v_cvt_f32_u32 v6, v2\n"
        "v_mul_f32 v6, 0x41000000, v6\n"
        "v_mul_f32 v7, 0x3d000000, v4\n"
        "v_mul_f32 v9, v6, v7\n"
        "v_fmamk_f32 v208, v7, 0x00000000, v9\n"
        "v_fmamk_f32 v209, v7, 0x3f800000, v9\n"
        "v_cos_f32 v210, v208\n"
        "v_sin_f32 v212, v208\n"
        "v_cos_f32 v211, v209\n"
        "v_sin_f32 v213, v209\n"
        "v_cvt_pk_bf16_f32 v214, v210, v211\n"
        "v_cvt_pk_bf16_f32 v215, v212, v213\n"
        "v_cvt_pk_bf16_f32 v208, -v212, -v213\n"
        "v_accvgpr_write_b32 a200, v214\n"
        "v_accvgpr_write_b32 a208, v215\n"
        "v_accvgpr_write_b32 a216, v208\n"
        "v_fmamk_f32 v216, v7, 0x40000000, v9\n"
        "v_fmamk_f32 v217, v7, 0x40400000, v9\n"
        "v_cos_f32 v218, v216\n"
        "v_sin_f32 v220, v216\n"
        "v_cos_f32 v219, v217\n"
        "v_sin_f32 v221, v217\n"
        "v_cvt_pk_bf16_f32 v222, v218, v219\n"
        "v_cvt_pk_bf16_f32 v223, v220, v221\n"
        "v_cvt_pk_bf16_f32 v216, -v220, -v221\n"
        "v_accvgpr_write_b32 a201, v222\n"
        "v_accvgpr_write_b32 a209, v223\n"
        "v_accvgpr_write_b32 a217, v216\n"
        "v_fmamk_f32 v224, v7, 0x40800000, v9\n"
        "v_fmamk_f32 v225, v7, 0x40a00000, v9\n"
        "v_cos_f32 v226, v224\n"
        "v_sin_f32 v228, v224\n"
        "v_cos_f32 v227, v225\n"
        "v_sin_f32 v229, v225\n"
        "v_cvt_pk_bf16_f32 v230, v226, v227\n"
        "v_cvt_pk_bf16_f32 v231, v228, v229\n"
        "v_cvt_pk_bf16_f32 v224, -v228, -v229\n"
        "v_accvgpr_write_b32 a202, v230\n"
        "v_accvgpr_write_b32 a210, v231\n"
        "v_accvgpr_write_b32 a218, v224\n"
        "v_fmamk_f32 v232, v7, 0x40c00000, v9\n"
        "v_fmamk_f32 v233, v7, 0x40e00000, v9\n"
        "v_cos_f32 v234, v232\n"
        "v_sin_f32 v236, v232\n"
        "v_cos_f32 v235, v233\n"
        "v_sin_f32 v237, v233\n"
        "v_cvt_pk_bf16_f32 v238, v234, v235\n"
        "v_cvt_pk_bf16_f32 v239, v236, v237\n"
        "v_cvt_pk_bf16_f32 v232, -v236, -v237\n"
        "v_accvgpr_write_b32 a203, v238\n"
        "v_accvgpr_write_b32 a211, v239\n"
        "v_accvgpr_write_b32 a219, v232\n"
        "v_mul_f32 v8, 0x3d000000, v5\n"
        "v_mul_f32 v10, v6, v8\n"
        "v_fmamk_f32 v208, v8, 0x00000000, v10\n"
        "v_fmamk_f32 v209, v8, 0x3f800000, v10\n"
        "v_cos_f32 v210, v208\n"
        "v_sin_f32 v212, v208\n"
        "v_cos_f32 v211, v209\n"
        "v_sin_f32 v213, v209\n"
        "v_cvt_pk_bf16_f32 v214, v210, v211\n"
        "v_cvt_pk_bf16_f32 v215, v212, v213\n"
        "v_cvt_pk_bf16_f32 v208, -v212, -v213\n"
        "v_accvgpr_write_b32 a204, v214\n"
        "v_accvgpr_write_b32 a212, v215\n"
        "v_accvgpr_write_b32 a220, v208\n"
        "v_fmamk_f32 v216, v8, 0x40000000, v10\n"
        "v_fmamk_f32 v217, v8, 0x40400000, v10\n"
        "v_cos_f32 v218, v216\n"
        "v_sin_f32 v220, v216\n"
        "v_cos_f32 v219, v217\n"
        "v_sin_f32 v221, v217\n"
        "v_cvt_pk_bf16_f32 v222, v218, v219\n"
        "v_cvt_pk_bf16_f32 v223, v220, v221\n"
        "v_cvt_pk_bf16_f32 v216, -v220, -v221\n"
        "v_accvgpr_write_b32 a205, v222\n"
        "v_accvgpr_write_b32 a213, v223\n"
        "v_accvgpr_write_b32 a221, v216\n"
        "v_fmamk_f32 v224, v8, 0x40800000, v10\n"
        "v_fmamk_f32 v225, v8, 0x40a00000, v10\n"
        "v_cos_f32 v226, v224\n"
        "v_sin_f32 v228, v224\n"
        "v_cos_f32 v227, v225\n"
        "v_sin_f32 v229, v225\n"
        "v_cvt_pk_bf16_f32 v230, v226, v227\n"
        "v_cvt_pk_bf16_f32 v231, v228, v229\n"
        "v_cvt_pk_bf16_f32 v224, -v228, -v229\n"
        "v_accvgpr_write_b32 a206, v230\n"
        "v_accvgpr_write_b32 a214, v231\n"
        "v_accvgpr_write_b32 a222, v224\n"
        "v_fmamk_f32 v232, v8, 0x40c00000, v10\n"
        "v_fmamk_f32 v233, v8, 0x40e00000, v10\n"
        "v_cos_f32 v234, v232\n"
        "v_sin_f32 v236, v232\n"
        "v_cos_f32 v235, v233\n"
        "v_sin_f32 v237, v233\n"
        "v_cvt_pk_bf16_f32 v238, v234, v235\n"
        "v_cvt_pk_bf16_f32 v239, v236, v237\n"
        "v_cvt_pk_bf16_f32 v232, -v236, -v237\n"
        "v_accvgpr_write_b32 a207, v238\n"
        "v_accvgpr_write_b32 a215, v239\n"
        "v_accvgpr_write_b32 a223, v232\n"
        "v_mul_f32 v7, 0x3d000000, v7\n"
        "v_mul_f32 v9, v6, v7\n"
        "v_fmamk_f32 v116, v7, 0x00000000, v9\n"
        "v_cos_f32 v112, v116\n"
        "v_sin_f32 v116, v116\n"
        "v_fmamk_f32 v117, v7, 0x3f800000, v9\n"
        "v_cos_f32 v113, v117\n"
        "v_sin_f32 v117, v117\n"
        "v_fmamk_f32 v118, v7, 0x40000000, v9\n"
        "v_cos_f32 v114, v118\n"
        "v_sin_f32 v118, v118\n"
        "v_fmamk_f32 v119, v7, 0x40400000, v9\n"
        "v_cos_f32 v115, v119\n"
        "v_sin_f32 v119, v119\n"
        "v_pk_mul_f32 v[112:113], v[112:113], v[242:243] op_sel_hi:[1,0]\n"
        "v_pk_mul_f32 v[114:115], v[114:115], v[242:243] op_sel_hi:[1,0]\n"
        "v_pk_mul_f32 v[116:117], v[116:117], v[242:243] op_sel_hi:[1,0]\n"
        "v_pk_mul_f32 v[118:119], v[118:119], v[242:243] op_sel_hi:[1,0]\n"
        "v_fmamk_f32 v132, v7, 0x40800000, v9\n"
        "v_cos_f32 v128, v132\n"
        "v_sin_f32 v132, v132\n"
        "v_fmamk_f32 v133, v7, 0x40a00000, v9\n"
        "v_cos_f32 v129, v133\n"
        "v_sin_f32 v133, v133\n"
        "v_fmamk_f32 v134, v7, 0x40c00000, v9\n"
        "v_cos_f32 v130, v134\n"
        "v_sin_f32 v134, v134\n"
        "v_fmamk_f32 v135, v7, 0x40e00000, v9\n"
        "v_cos_f32 v131, v135\n"
        "v_sin_f32 v135, v135\n"
        "v_pk_mul_f32 v[128:129], v[128:129], v[242:243] op_sel_hi:[1,0]\n"
        "v_pk_mul_f32 v[130:131], v[130:131], v[242:243] op_sel_hi:[1,0]\n"
        "v_pk_mul_f32 v[132:133], v[132:133], v[242:243] op_sel_hi:[1,0]\n"
        "v_pk_mul_f32 v[134:135], v[134:135], v[242:243] op_sel_hi:[1,0]\n"
        "v_mul_f32 v8, 0x3d000000, v8\n"
        "v_mul_f32 v10, v6, v8\n"
        "v_fmamk_f32 v124, v8, 0x00000000, v10\n"
        "v_cos_f32 v120, v124\n"
        "v_sin_f32 v124, v124\n"
        "v_fmamk_f32 v125, v8, 0x3f800000, v10\n"
        "v_cos_f32 v121, v125\n"
        "v_sin_f32 v125, v125\n"
        "v_fmamk_f32 v126, v8, 0x40000000, v10\n"
        "v_cos_f32 v122, v126\n"
        "v_sin_f32 v126, v126\n"
        "v_fmamk_f32 v127, v8, 0x40400000, v10\n"
        "v_cos_f32 v123, v127\n"
        "v_sin_f32 v127, v127\n"
        "v_pk_mul_f32 v[120:121], v[120:121], v[242:243] op_sel_hi:[1,0]\n"
        "v_pk_mul_f32 v[122:123], v[122:123], v[242:243] op_sel_hi:[1,0]\n"
        "v_pk_mul_f32 v[124:125], v[124:125], v[242:243] op_sel_hi:[1,0]\n"
        "v_pk_mul_f32 v[126:127], v[126:127], v[242:243] op_sel_hi:[1,0]\n"
        "v_fmamk_f32 v140, v8, 0x40800000, v10\n"
        "v_cos_f32 v136, v140\n"
        "v_sin_f32 v140, v140\n"
        "v_fmamk_f32 v141, v8, 0x40a00000, v10\n"
        "v_cos_f32 v137, v141\n"
        "v_sin_f32 v141, v141\n"
        "v_fmamk_f32 v142, v8, 0x40c00000, v10\n"
        "v_cos_f32 v138, v142\n"
        "v_sin_f32 v142, v142\n"
        "v_fmamk_f32 v143, v8, 0x40e00000, v10\n"
        "v_cos_f32 v139, v143\n"
        "v_sin_f32 v143, v143\n"
        "v_pk_mul_f32 v[136:137], v[136:137], v[242:243] op_sel_hi:[1,0]\n"
        "v_pk_mul_f32 v[138:139], v[138:139], v[242:243] op_sel_hi:[1,0]\n"
        "v_pk_mul_f32 v[140:141], v[140:141], v[242:243] op_sel_hi:[1,0]\n"
        "v_pk_mul_f32 v[142:143], v[142:143], v[242:243] op_sel_hi:[1,0]\n"
        "L16_%=:\n"
        "s_mul_i32 s70, s3, 2560\n"
        "v_min_u32 v240, 19, v0\n"
        "s_add_u32 s70, s70, 11567104\n"
        "v_lshl_add_u32 v240, v240, 7, s70\n"
        "s_and_b32 s70, s5, 7\n"
        "s_lshl_b32 s70, s70, 2\n"
        "s_add_u32 s70, s70, s3\n"
        "s_lshl_b32 s70, s70, 8\n"
        "v_and_b32 v246, 1, v0\n"
        "s_add_u32 s70, s70, 11632640\n"
        "v_lshl_add_u32 v246, v246, 7, s70\n"
        "s_and_b32 s70, s3, 1\n"
        "s_lshr_b32 s71, s3, 1\n"
        "s_lshl_b32 s72, s70, 5\n"
        "v_bfe_u32 v208, v0, 3, 2\n"
        "v_lshrrev_b32 v209, 5, v0\n"
        "s_add_u32 s72, s72, s71\n"
        "s_lshl_b32 s70, s70, 3\n"
        "v_lshl_add_u32 v208, v209, 3, v208\n"
        "v_and_b32 v210, 7, v0\n"
        "s_add_u32 s70, s70, s71\n"
        "v_lshl_add_u32 v209, v208, 3, s72\n"
        "v_lshl_add_u32 v208, v208, 1, s70\n"
        "v_xor_b32 v210, v210, v2\n"
        "s_lshl_b32 s70, s91, 7\n"
        "v_mul_u32_u24 v209, 10496, v209\n"
        "v_mul_u32_u24 v208, 10496, v208\n"
        "v_lshl_add_u32 v211, v210, 4, s70\n"
        "s_lshl_b32 s70, s91, 2\n"
        "v_add_u32 v250, v209, v211\n"
        "v_add_u32 v251, v208, v211\n"
        "v_and_b32 v211, 3, v210\n"
        "s_sub_u32 s70, s70, 1\n"
        "v_add_u32 v211, s70, v211\n"
        "v_mul_f32 v6, 0x3c800000, v6\n"
        "v_cmp_le_u32 vcc, 319, v211\n"
        "v_fmamk_f32 v7, v4, 0x3a000000, v6\n"
        "s_nop 0\n"
        "v_cndmask_b32_e64 v211, 0, 4, vcc\n"
        "v_fmamk_f32 v8, v5, 0x3a000000, v6\n"
        "v_and_b32 v209, v211, v210\n"
        "v_add_f32 v36, 0x00000000, v7\n"
        "v_add_f32 v37, 0x3c800000, v7\n"
        "v_add_f32 v38, 0x3d000000, v7\n"
        "v_add_f32 v39, 0x3d400000, v7\n"
        "v_add_f32 v84, 0x3d800000, v7\n"
        "v_add_f32 v85, 0x3da00000, v7\n"
        "v_add_f32 v86, 0x3dc00000, v7\n"
        "v_add_f32 v87, 0x3de00000, v7\n"
        "v_add_f32 v60, 0x00000000, v8\n"
        "v_add_f32 v61, 0x3c800000, v8\n"
        "v_add_f32 v62, 0x3d000000, v8\n"
        "v_add_f32 v63, 0x3d400000, v8\n"
        "v_add_f32 v108, 0x3d800000, v8\n"
        "v_add_f32 v109, 0x3da00000, v8\n"
        "v_add_f32 v110, 0x3dc00000, v8\n"
        "v_add_f32 v111, 0x3de00000, v8\n"
        "v_xor_b32 v252, 64, v251\n"
        "v_lshl_or_b32 v251, v209, 28, v251\n"
        "v_cos_f32 v32, v36\n"
        "v_cos_f32 v33, v37\n"
        "v_cos_f32 v34, v38\n"
        "v_cos_f32 v35, v39\n"
        "v_cos_f32 v80, v84\n"
        "v_cos_f32 v81, v85\n"
        "v_cos_f32 v82, v86\n"
        "v_cos_f32 v83, v87\n"
        "v_cos_f32 v56, v60\n"
        "v_cos_f32 v57, v61\n"
        "v_cos_f32 v58, v62\n"
        "v_cos_f32 v59, v63\n"
        "v_cos_f32 v104, v108\n"
        "v_cos_f32 v105, v109\n"
        "v_cos_f32 v106, v110\n"
        "v_cos_f32 v107, v111\n"
        "v_xor_b32 v209, v209, v211\n"
        "v_sin_f32 v36, v36\n"
        "v_sin_f32 v37, v37\n"
        "v_sin_f32 v38, v38\n"
        "v_sin_f32 v39, v39\n"
        "v_sin_f32 v84, v84\n"
        "v_sin_f32 v85, v85\n"
        "v_sin_f32 v86, v86\n"
        "v_sin_f32 v87, v87\n"
        "v_sin_f32 v60, v60\n"
        "v_sin_f32 v61, v61\n"
        "v_sin_f32 v62, v62\n"
        "v_sin_f32 v63, v63\n"
        "v_sin_f32 v108, v108\n"
        "v_sin_f32 v109, v109\n"
        "v_sin_f32 v110, v110\n"
        "v_sin_f32 v111, v111\n"
        "v_xor_b32 v253, 64, v250\n"
        "v_lshl_or_b32 v252, v209, 28, v252\n"
        "buffer_load_dwordx2 v[248:249], v246, s[40:43], 0 offen sc1\n"
        "v_add_u32 v204, s69, v1\n"
        "s_mul_i32 s70, s3, 80\n"
        "v_cvt_f32_u32 v204, v204\n"
        "v_lshl_add_u32 v205, v2, 2, s70\n"
        "v_add_f32 v214, 0x00000000, v204\n"
        "v_cvt_f32_u32 v205, v205\n"
        "v_mul_f32 v214, 0x3acccccd, v214\n"
        "v_add_f32 v222, 0x41800000, v204\n"
        "v_cos_f32 v184, v214\n"
        "v_sin_f32 v185, v214\n"
        "v_mul_f32 v215, -v205, v214\n"
        "v_mul_f32 v214, 0x41800000, v214\n"
        "v_fract_f32 v215, v215\n"
        "v_cos_f32 v194, v214\n"
        "v_sin_f32 v195, v214\n"
        "v_add_f32 v214, 0x42000000, v204\n"
        "v_mul_f32 v222, 0x3acccccd, v222\n"
        "v_mul_f32 v214, 0x3acccccd, v214\n"
        "v_cos_f32 v144, v215\n"
        "v_sin_f32 v145, v215\n"
        "v_cos_f32 v186, v222\n"
        "v_sin_f32 v187, v222\n"
        "v_mul_f32 v223, -v205, v222\n"
        "v_mul_f32 v222, 0x41800000, v222\n"
        "v_cos_f32 v188, v214\n"
        "v_sin_f32 v189, v214\n"
        "v_mul_f32 v215, -v205, v214\n"
        "v_mul_f32 v214, 0x41800000, v214\n"
        "v_cos_f32 v196, v222\n"
        "v_sin_f32 v197, v222\n"
        "v_cos_f32 v198, v214\n"
        "v_sin_f32 v199, v214\n"
        "v_add_f32 v222, 0x42400000, v204\n"
        "v_add_f32 v214, 0x42800000, v204\n"
        "v_fract_f32 v223, v223\n"
        "v_fract_f32 v215, v215\n"
        "v_mul_f32 v222, 0x3acccccd, v222\n"
        "v_mul_f32 v214, 0x3acccccd, v214\n"
        "v_cos_f32 v152, v223\n"
        "v_sin_f32 v153, v223\n"
        "v_cos_f32 v160, v215\n"
        "v_sin_f32 v161, v215\n"
        "v_mul_f32 v223, -v205, v222\n"
        "v_mul_f32 v215, -v205, v214\n"
        "v_fract_f32 v223, v223\n"
        "v_fract_f32 v215, v215\n"
        "v_cos_f32 v190, v222\n"
        "v_sin_f32 v191, v222\n"
        "v_cos_f32 v168, v223\n"
        "v_sin_f32 v169, v223\n"
        "v_pk_mul_f32 v[216:217], v[144:145], v[184:185] op_sel_hi:[1,0]\n"
        "v_cos_f32 v192, v214\n"
        "v_sin_f32 v193, v214\n"
        "v_cos_f32 v176, v215\n"
        "v_sin_f32 v177, v215\n"
        "v_pk_fma_f32 v[146:147], v[144:145], v[184:185], v[216:217] op_sel:[1,1,0] op_sel_hi:[0,1,1] neg_hi:[0,1,0]\n"
        "v_pk_mul_f32 v[224:225], v[152:153], v[186:187] op_sel_hi:[1,0]\n"
        "v_pk_mul_f32 v[216:217], v[160:161], v[188:189] op_sel_hi:[1,0]\n"
        "v_pk_mul_f32 v[218:219], v[146:147], v[184:185] op_sel_hi:[1,0]\n"
        "v_pk_fma_f32 v[154:155], v[152:153], v[186:187], v[224:225] op_sel:[1,1,0] op_sel_hi:[0,1,1] neg_hi:[0,1,0]\n"
        "v_pk_fma_f32 v[162:163], v[160:161], v[188:189], v[216:217] op_sel:[1,1,0] op_sel_hi:[0,1,1] neg_hi:[0,1,0]\n"
        "v_pk_mul_f32 v[224:225], v[168:169], v[190:191] op_sel_hi:[1,0]\n"
        "v_pk_mul_f32 v[216:217], v[176:177], v[192:193] op_sel_hi:[1,0]\n"
        "v_pk_fma_f32 v[148:149], v[146:147], v[184:185], v[218:219] op_sel:[1,1,0] op_sel_hi:[0,1,1] neg_hi:[0,1,0]\n"
        "v_pk_mul_f32 v[226:227], v[154:155], v[186:187] op_sel_hi:[1,0]\n"
        "v_pk_mul_f32 v[218:219], v[162:163], v[188:189] op_sel_hi:[1,0]\n"
        "v_pk_fma_f32 v[170:171], v[168:169], v[190:191], v[224:225] op_sel:[1,1,0] op_sel_hi:[0,1,1] neg_hi:[0,1,0]\n"
        "v_pk_fma_f32 v[178:179], v[176:177], v[192:193], v[216:217] op_sel:[1,1,0] op_sel_hi:[0,1,1] neg_hi:[0,1,0]\n"
        "v_pk_mul_f32 v[220:221], v[148:149], v[184:185] op_sel_hi:[1,0]\n"
        "v_pk_fma_f32 v[156:157], v[154:155], v[186:187], v[226:227] op_sel:[1,1,0] op_sel_hi:[0,1,1] neg_hi:[0,1,0]\n"
        "v_pk_fma_f32 v[164:165], v[162:163], v[188:189], v[218:219] op_sel:[1,1,0] op_sel_hi:[0,1,1] neg_hi:[0,1,0]\n"
        "v_pk_mul_f32 v[226:227], v[170:171], v[190:191] op_sel_hi:[1,0]\n"
        "v_pk_mul_f32 v[218:219], v[178:179], v[192:193] op_sel_hi:[1,0]\n"
        "v_pk_fma_f32 v[150:151], v[148:149], v[184:185], v[220:221] op_sel:[1,1,0] op_sel_hi:[0,1,1] neg_hi:[0,1,0]\n"
        "v_pk_mul_f32 v[212:213], v[156:157], v[186:187] op_sel_hi:[1,0]\n"
        "v_pk_mul_f32 v[220:221], v[164:165], v[188:189] op_sel_hi:[1,0]\n"
        "v_pk_fma_f32 v[172:173], v[170:171], v[190:191], v[226:227] op_sel:[1,1,0] op_sel_hi:[0,1,1] neg_hi:[0,1,0]\n"
        "v_mul_f32 v222, 0x41800000, v222\n"
        "v_pk_fma_f32 v[180:181], v[178:179], v[192:193], v[218:219] op_sel:[1,1,0] op_sel_hi:[0,1,1] neg_hi:[0,1,0]\n"
        "v_pk_fma_f32 v[158:159], v[156:157], v[186:187], v[212:213] op_sel:[1,1,0] op_sel_hi:[0,1,1] neg_hi:[0,1,0]\n"
        "v_pk_fma_f32 v[166:167], v[164:165], v[188:189], v[220:221] op_sel:[1,1,0] op_sel_hi:[0,1,1] neg_hi:[0,1,0]\n"
        "v_pk_mul_f32 v[212:213], v[172:173], v[190:191] op_sel_hi:[1,0]\n"
        "v_cos_f32 v200, v222\n"
        "v_sin_f32 v201, v222\n"
        "v_mul_f32 v214, 0x41800000, v214\n"
        "v_pk_mul_f32 v[220:221], v[180:181], v[192:193] op_sel_hi:[1,0]\n"
        "v_pk_fma_f32 v[174:175], v[172:173], v[190:191], v[212:213] op_sel:[1,1,0] op_sel_hi:[0,1,1] neg_hi:[0,1,0]\n"
        "v_cos_f32 v202, v214\n"
        "v_sin_f32 v203, v214\n"
        "v_pk_fma_f32 v[182:183], v[180:181], v[192:193], v[220:221] op_sel:[1,1,0] op_sel_hi:[0,1,1] neg_hi:[0,1,0]\n"
        "v_pk_mul_f32 v[222:223], v[144:145], v[194:195] op_sel_hi:[1,0]\n"
        "v_pk_mul_f32 v[224:225], v[146:147], v[194:195] op_sel_hi:[1,0]\n"
        "v_pk_mul_f32 v[226:227], v[148:149], v[194:195] op_sel_hi:[1,0]\n"
        "v_pk_mul_f32 v[212:213], v[150:151], v[194:195] op_sel_hi:[1,0]\n"
        "v_pk_mul_f32 v[214:215], v[152:153], v[196:197] op_sel_hi:[1,0]\n"
        "v_pk_mul_f32 v[216:217], v[154:155], v[196:197] op_sel_hi:[1,0]\n"
        "v_pk_mul_f32 v[218:219], v[156:157], v[196:197] op_sel_hi:[1,0]\n"
        "v_pk_mul_f32 v[220:221], v[158:159], v[196:197] op_sel_hi:[1,0]\n"
        "v_cvt_pk_bf16_f32 v228, v144, v145\n"
        "v_cvt_pk_bf16_f32 v229, v146, v147\n"
        "v_cvt_pk_bf16_f32 v230, v148, v149\n"
        "v_cvt_pk_bf16_f32 v231, v150, v151\n"
        "v_cvt_pk_bf16_f32 v232, v152, v153\n"
        "v_cvt_pk_bf16_f32 v233, v154, v155\n"
        "v_cvt_pk_bf16_f32 v234, v156, v157\n"
        "v_cvt_pk_bf16_f32 v235, v158, v159\n"
        "v_pk_fma_f32 v[144:145], v[144:145], v[194:195], v[222:223] op_sel:[1,1,0] op_sel_hi:[0,1,1] neg_hi:[0,1,0]\n"
        "v_pk_fma_f32 v[146:147], v[146:147], v[194:195], v[224:225] op_sel:[1,1,0] op_sel_hi:[0,1,1] neg_hi:[0,1,0]\n"
        "v_pk_fma_f32 v[148:149], v[148:149], v[194:195], v[226:227] op_sel:[1,1,0] op_sel_hi:[0,1,1] neg_hi:[0,1,0]\n"
        "v_pk_fma_f32 v[150:151], v[150:151], v[194:195], v[212:213] op_sel:[1,1,0] op_sel_hi:[0,1,1] neg_hi:[0,1,0]\n"
        "v_pk_fma_f32 v[152:153], v[152:153], v[196:197], v[214:215] op_sel:[1,1,0] op_sel_hi:[0,1,1] neg_hi:[0,1,0]\n"
        "v_pk_fma_f32 v[154:155], v[154:155], v[196:197], v[216:217] op_sel:[1,1,0] op_sel_hi:[0,1,1] neg_hi:[0,1,0]\n"
        "v_pk_fma_f32 v[156:157], v[156:157], v[196:197], v[218:219] op_sel:[1,1,0] op_sel_hi:[0,1,1] neg_hi:[0,1,0]\n"
        "v_pk_fma_f32 v[158:159], v[158:159], v[196:197], v[220:221] op_sel:[1,1,0] op_sel_hi:[0,1,1] neg_hi:[0,1,0]\n"
        "v_pk_mul_f32 v[222:223], v[160:161], v[198:199] op_sel_hi:[1,0]\n"
        "v_pk_mul_f32 v[224:225], v[162:163], v[198:199] op_sel_hi:[1,0]\n"
        "v_pk_mul_f32 v[226:227], v[164:165], v[198:199] op_sel_hi:[1,0]\n"
        "v_pk_mul_f32 v[212:213], v[166:167], v[198:199] op_sel_hi:[1,0]\n"
        "v_pk_mul_f32 v[214:215], v[168:169], v[200:201] op_sel_hi:[1,0]\n"
        "v_pk_mul_f32 v[216:217], v[170:171], v[200:201] op_sel_hi:[1,0]\n"
        "v_pk_mul_f32 v[218:219], v[172:173], v[200:201] op_sel_hi:[1,0]\n"
        "v_pk_mul_f32 v[220:221], v[174:175], v[200:201] op_sel_hi:[1,0]\n"
        "s_mul_i32 s72, s3, 10240\n"
        "v_accvgpr_write_b32 a176, v228\n"
        "v_accvgpr_write_b32 a177, v229\n"
        "v_accvgpr_write_b32 a178, v230\n"
        "v_accvgpr_write_b32 a179, v231\n"
        "v_cvt_pk_bf16_f32 v236, v160, v161\n"
        "v_cvt_pk_bf16_f32 v237, v162, v163\n"
        "v_cvt_pk_bf16_f32 v238, v164, v165\n"
        "v_cvt_pk_bf16_f32 v239, v166, v167\n"
        "v_cvt_pk_bf16_f32 v228, v168, v169\n"
        "v_cvt_pk_bf16_f32 v229, v170, v171\n"
        "buffer_load_dwordx2 v[64:65], v240, s[40:43], 0 offen sc1\n"
        "v_cvt_pk_bf16_f32 v230, v172, v173\n"
        "v_cvt_pk_bf16_f32 v231, v174, v175\n"
        "v_pk_fma_f32 v[160:161], v[160:161], v[198:199], v[222:223] op_sel:[1,1,0] op_sel_hi:[0,1,1] neg_hi:[0,1,0]\n"
        "v_pk_fma_f32 v[162:163], v[162:163], v[198:199], v[224:225] op_sel:[1,1,0] op_sel_hi:[0,1,1] neg_hi:[0,1,0]\n"
        "v_pk_fma_f32 v[164:165], v[164:165], v[198:199], v[226:227] op_sel:[1,1,0] op_sel_hi:[0,1,1] neg_hi:[0,1,0]\n"
        "v_pk_fma_f32 v[166:167], v[166:167], v[198:199], v[212:213] op_sel:[1,1,0] op_sel_hi:[0,1,1] neg_hi:[0,1,0]\n"
        "v_pk_fma_f32 v[168:169], v[168:169], v[200:201], v[214:215] op_sel:[1,1,0] op_sel_hi:[0,1,1] neg_hi:[0,1,0]\n"
        "v_pk_fma_f32 v[170:171], v[170:171], v[200:201], v[216:217] op_sel:[1,1,0] op_sel_hi:[0,1,1] neg_hi:[0,1,0]\n"
        "v_pk_fma_f32 v[172:173], v[172:173], v[200:201], v[218:219] op_sel:[1,1,0] op_sel_hi:[0,1,1] neg_hi:[0,1,0]\n"
        "v_pk_fma_f32 v[174:175], v[174:175], v[200:201], v[220:221] op_sel:[1,1,0] op_sel_hi:[0,1,1] neg_hi:[0,1,0]\n"
        "v_pk_mul_f32 v[222:223], v[176:177], v[202:203] op_sel_hi:[1,0]\n"
        "v_pk_mul_f32 v[224:225], v[178:179], v[202:203] op_sel_hi:[1,0]\n"
        "v_pk_mul_f32 v[226:227], v[180:181], v[202:203] op_sel_hi:[1,0]\n"
        "v_pk_mul_f32 v[212:213], v[182:183], v[202:203] op_sel_hi:[1,0]\n"
        "s_add_u32 s72, s72, 114688\n"
        "v_pk_mul_f32 v[214:215], v[144:145], v[194:195] op_sel_hi:[1,0]\n"
        "v_pk_mul_f32 v[216:217], v[146:147], v[194:195] op_sel_hi:[1,0]\n"
        "v_pk_mul_f32 v[218:219], v[148:149], v[194:195] op_sel_hi:[1,0]\n"
        "v_pk_mul_f32 v[220:221], v[150:151], v[194:195] op_sel_hi:[1,0]\n"
        "v_accvgpr_write_b32 a180, v232\n"
        "v_accvgpr_write_b32 a181, v233\n"
        "v_accvgpr_write_b32 a182, v234\n"
        "v_accvgpr_write_b32 a183, v235\n"
        "v_accvgpr_write_b32 a184, v236\n"
        "v_accvgpr_write_b32 a185, v237\n"
        "v_accvgpr_write_b32 a186, v238\n"
        "v_accvgpr_write_b32 a187, v239\n"
        "v_cvt_pk_bf16_f32 v232, v176, v177\n"
        "v_cvt_pk_bf16_f32 v233, v178, v179\n"
        "v_cvt_pk_bf16_f32 v234, v180, v181\n"
        "v_cvt_pk_bf16_f32 v235, v182, v183\n"
        "v_add_u32 v206, s72, v3\n"
        "v_cvt_pk_bf16_f32 v236, v144, v145\n"
        "v_cvt_pk_bf16_f32 v237, v146, v147\n"
        "v_cvt_pk_bf16_f32 v238, v148, v149\n"
        "v_cvt_pk_bf16_f32 v239, v150, v151\n"
        "v_pk_fma_f32 v[176:177], v[176:177], v[202:203], v[222:223] op_sel:[1,1,0] op_sel_hi:[0,1,1] neg_hi:[0,1,0]\n"
        "v_pk_fma_f32 v[178:179], v[178:179], v[202:203], v[224:225] op_sel:[1,1,0] op_sel_hi:[0,1,1] neg_hi:[0,1,0]\n"
        "v_pk_fma_f32 v[180:181], v[180:181], v[202:203], v[226:227] op_sel:[1,1,0] op_sel_hi:[0,1,1] neg_hi:[0,1,0]\n"
        "v_pk_fma_f32 v[182:183], v[182:183], v[202:203], v[212:213] op_sel:[1,1,0] op_sel_hi:[0,1,1] neg_hi:[0,1,0]\n"
        "v_pk_fma_f32 v[144:145], v[144:145], v[194:195], v[214:215] op_sel:[1,1,0] op_sel_hi:[0,1,1] neg_hi:[0,1,0]\n"
        "v_pk_fma_f32 v[146:147], v[146:147], v[194:195], v[216:217] op_sel:[1,1,0] op_sel_hi:[0,1,1] neg_hi:[0,1,0]\n"
        "v_pk_fma_f32 v[148:149], v[148:149], v[194:195], v[218:219] op_sel:[1,1,0] op_sel_hi:[0,1,1] neg_hi:[0,1,0]\n"
        "v_pk_fma_f32 v[150:151], v[150:151], v[194:195], v[220:221] op_sel:[1,1,0] op_sel_hi:[0,1,1] neg_hi:[0,1,0]\n"
        "v_pk_mul_f32 v[222:223], v[152:153], v[196:197] op_sel_hi:[1,0]\n"
        "v_pk_mul_f32 v[224:225], v[154:155], v[196:197] op_sel_hi:[1,0]\n"
        "v_pk_mul_f32 v[226:227], v[156:157], v[196:197] op_sel_hi:[1,0]\n"
        "v_pk_mul_f32 v[212:213], v[158:159], v[196:197] op_sel_hi:[1,0]\n"
        "v_pk_mul_f32 v[214:215], v[160:161], v[198:199] op_sel_hi:[1,0]\n"
        "v_pk_mul_f32 v[216:217], v[162:163], v[198:199] op_sel_hi:[1,0]\n"
        "v_pk_mul_f32 v[218:219], v[164:165], v[198:199] op_sel_hi:[1,0]\n"
        "v_pk_mul_f32 v[220:221], v[166:167], v[198:199] op_sel_hi:[1,0]\n"
        "v_accvgpr_write_b32 a188, v228\n"
        "v_accvgpr_write_b32 a189, v229\n"
        "v_accvgpr_write_b32 a190, v230\n"
        "v_accvgpr_write_b32 a191, v231\n"
        "v_accvgpr_write_b32 a192, v232\n"
        "v_accvgpr_write_b32 a193, v233\n"
        "v_accvgpr_write_b32 a194, v234\n"
        "v_accvgpr_write_b32 a195, v235\n"
        "v_cvt_pk_bf16_f32 v228, v152, v153\n"
        "v_cvt_pk_bf16_f32 v229, v154, v155\n"
        "v_cvt_pk_bf16_f32 v230, v156, v157\n"
        "v_cvt_pk_bf16_f32 v231, v158, v159\n"
        "ds_write_b128 v206, v[236:239] offset:0\n"
        "v_cvt_pk_bf16_f32 v232, v160, v161\n"
        "v_cvt_pk_bf16_f32 v233, v162, v163\n"
        "v_cvt_pk_bf16_f32 v234, v164, v165\n"
        "v_cvt_pk_bf16_f32 v235, v166, v167\n"
        "v_cvt_pk_bf16_f32 v236, v168, v169\n"
        "v_cvt_pk_bf16_f32 v237, v170, v171\n"
        "v_cvt_pk_bf16_f32 v238, v172, v173\n"
        "v_cvt_pk_bf16_f32 v239, v174, v175\n"
        "v_pk_fma_f32 v[152:153], v[152:153], v[196:197], v[222:223] op_sel:[1,1,0] op_sel_hi:[0,1,1] neg_hi:[0,1,0]\n"
        "v_pk_fma_f32 v[154:155], v[154:155], v[196:197], v[224:225] op_sel:[1,1,0] op_sel_hi:[0,1,1] neg_hi:[0,1,0]\n"
        "v_pk_fma_f32 v[156:157], v[156:157], v[196:197], v[226:227] op_sel:[1,1,0] op_sel_hi:[0,1,1] neg_hi:[0,1,0]\n"
        "v_pk_fma_f32 v[158:159], v[158:159], v[196:197], v[212:213] op_sel:[1,1,0] op_sel_hi:[0,1,1] neg_hi:[0,1,0]\n"
        "v_pk_fma_f32 v[160:161], v[160:161], v[198:199], v[214:215] op_sel:[1,1,0] op_sel_hi:[0,1,1] neg_hi:[0,1,0]\n"
        "v_pk_fma_f32 v[162:163], v[162:163], v[198:199], v[216:217] op_sel:[1,1,0] op_sel_hi:[0,1,1] neg_hi:[0,1,0]\n"
        "v_pk_fma_f32 v[164:165], v[164:165], v[198:199], v[218:219] op_sel:[1,1,0] op_sel_hi:[0,1,1] neg_hi:[0,1,0]\n"
        "v_pk_fma_f32 v[166:167], v[166:167], v[198:199], v[220:221] op_sel:[1,1,0] op_sel_hi:[0,1,1] neg_hi:[0,1,0]\n"
        "v_pk_mul_f32 v[222:223], v[168:169], v[200:201] op_sel_hi:[1,0]\n"
        "v_pk_mul_f32 v[224:225], v[170:171], v[200:201] op_sel_hi:[1,0]\n"
        "v_pk_mul_f32 v[226:227], v[172:173], v[200:201] op_sel_hi:[1,0]\n"
        "v_pk_mul_f32 v[212:213], v[174:175], v[200:201] op_sel_hi:[1,0]\n"
        "v_pk_mul_f32 v[214:215], v[176:177], v[202:203] op_sel_hi:[1,0]\n"
        "v_pk_mul_f32 v[216:217], v[178:179], v[202:203] op_sel_hi:[1,0]\n"
        "v_pk_mul_f32 v[218:219], v[180:181], v[202:203] op_sel_hi:[1,0]\n"
        "v_pk_mul_f32 v[220:221], v[182:183], v[202:203] op_sel_hi:[1,0]\n"
        "ds_write_b128 v206, v[228:231] offset:1024\n"
        "ds_write_b128 v206, v[232:235] offset:2048\n"
        "v_cvt_pk_bf16_f32 v228, v176, v177\n"
        "v_cvt_pk_bf16_f32 v229, v178, v179\n"
        "v_cvt_pk_bf16_f32 v230, v180, v181\n"
        "v_cvt_pk_bf16_f32 v231, v182, v183\n"
        "ds_write_b128 v206, v[236:239] offset:3072\n"
        "v_cvt_pk_bf16_f32 v232, v144, v145\n"
        "v_cvt_pk_bf16_f32 v233, v146, v147\n"
        "v_cvt_pk_bf16_f32 v234, v148, v149\n"
        "v_cvt_pk_bf16_f32 v235, v150, v151\n"
        "v_cvt_pk_bf16_f32 v236, v152, v153\n"
        "v_cvt_pk_bf16_f32 v237, v154, v155\n"
        "v_cvt_pk_bf16_f32 v238, v156, v157\n"
        "v_cvt_pk_bf16_f32 v239, v158, v159\n"
        "v_pk_fma_f32 v[168:169], v[168:169], v[200:201], v[222:223] op_sel:[1,1,0] op_sel_hi:[0,1,1] neg_hi:[0,1,0]\n"
        "v_pk_fma_f32 v[170:171], v[170:171], v[200:201], v[224:225] op_sel:[1,1,0] op_sel_hi:[0,1,1] neg_hi:[0,1,0]\n"
        "v_pk_fma_f32 v[172:173], v[172:173], v[200:201], v[226:227] op_sel:[1,1,0] op_sel_hi:[0,1,1] neg_hi:[0,1,0]\n"
        "v_pk_fma_f32 v[174:175], v[174:175], v[200:201], v[212:213] op_sel:[1,1,0] op_sel_hi:[0,1,1] neg_hi:[0,1,0]\n"
        "v_pk_fma_f32 v[176:177], v[176:177], v[202:203], v[214:215] op_sel:[1,1,0] op_sel_hi:[0,1,1] neg_hi:[0,1,0]\n"
        "v_pk_fma_f32 v[178:179], v[178:179], v[202:203], v[216:217] op_sel:[1,1,0] op_sel_hi:[0,1,1] neg_hi:[0,1,0]\n"
        "v_pk_fma_f32 v[180:181], v[180:181], v[202:203], v[218:219] op_sel:[1,1,0] op_sel_hi:[0,1,1] neg_hi:[0,1,0]\n"
        "v_pk_fma_f32 v[182:183], v[182:183], v[202:203], v[220:221] op_sel:[1,1,0] op_sel_hi:[0,1,1] neg_hi:[0,1,0]\n"
        "ds_write_b128 v206, v[228:231] offset:4096\n"
        "ds_write_b128 v206, v[232:235] offset:5120\n"
        "v_cvt_pk_bf16_f32 v228, v160, v161\n"
        "v_cvt_pk_bf16_f32 v229, v162, v163\n"
        "v_cvt_pk_bf16_f32 v230, v164, v165\n"
        "v_cvt_pk_bf16_f32 v231, v166, v167\n"
        "ds_write_b128 v206, v[236:239] offset:6144\n"
        "v_cvt_pk_bf16_f32 v232, v168, v169\n"
        "v_cvt_pk_bf16_f32 v233, v170, v171\n"
        "v_cvt_pk_bf16_f32 v234, v172, v173\n"
        "v_cvt_pk_bf16_f32 v235, v174, v175\n"
        "v_cvt_pk_bf16_f32 v236, v176, v177\n"
        "v_cvt_pk_bf16_f32 v237, v178, v179\n"
        "v_cvt_pk_bf16_f32 v238, v180, v181\n"
        "v_cvt_pk_bf16_f32 v239, v182, v183\n"
        "ds_write_b128 v206, v[228:231] offset:7168\n"
        "ds_write_b128 v206, v[232:235] offset:8192\n"
        "ds_write_b128 v206, v[236:239] offset:9216\n"
        "s_waitcnt vmcnt(0)\n"
        "v_cmp_ne_u64 vcc, s[64:65], v[64:65]\n"
        "s_cbranch_vccz L18_%=\n"
        "s_nop 7\n"
        "s_nop 7\n"
        "s_nop 7\n"
        "s_waitcnt vmcnt(0) lgkmcnt(0)\n"
        "s_mov_b32 s6, 65536\n"
        "L17_%=:\n"
        "s_sleep 4\n"
        "buffer_load_dwordx2 v[64:65], v240, s[40:43], 0 offen sc1\n"
        "s_waitcnt vmcnt(0)\n"
        "v_cmp_ne_u64 vcc, s[64:65], v[64:65]\n"
        "s_cbranch_vccz L18_%=\n"
        "s_sub_u32 s6, s6, 1\n"
        "s_cmp_lg_u32 s6, 0\n"
        "s_cbranch_scc1 L17_%=\n"
        "L18_%=:\n"
        "s_barrier\n"
        "s_lshl_b32 s90, s3, 13\n"
        "s_add_u32 m0, s90, 0\n"
        "s_mov_b32 s84, 0x40000\n"
        "buffer_load_dwordx4 v251, s[40:43], s84 offen offset:0 sc1 lds\n"
        "s_mov_b32 s85, 0x91c00\n"
        "s_mov_b32 s84, 0xe3800\n"
        "v_lshrrev_b32 v241, 1, v1\n"
        "v_lshlrev_b32 v243, 1, v2\n"
        "v_xor_b32 v242, s3, v241\n"
        "v_lshrrev_b32 v241, 2, v1\n"
        "v_lshlrev_b32 v242, 4, v242\n"
        "v_and_b32 v244, 3, v1\n"
        "v_lshl_add_u32 v4, v0, 7, v242\n"
        "v_and_b32 v242, 7, v1\n"
        "buffer_load_dwordx4 v252, s[40:43], s85 offen offset:1024 sc1 lds\n"
        "s_mov_b32 s85, 0x135400\n"
        "s_mul_i32 s70, s3, 8320\n"
        "v_xor_b32 v242, v242, v243\n"
        "v_lshl_add_u32 v244, v241, 3, v244\n"
        "v_lshlrev_b32 v242, 4, v242\n"
        "v_and_b32 v245, 1, v1\n"
        "s_mul_i32 s86, s3, 688128\n"
        "s_mul_i32 s71, s3, 10240\n"
        "v_add_u32 v88, 0, v0\n"
        "v_add_u32 v89, 64, v0\n"
        "buffer_load_dwordx4 v251, s[40:43], s84 offen offset:2048 sc1 lds\n"
        "s_mov_b32 s84, 0x188000\n"
        "v_mul_u32_u24 v91, 205, v88\n"
        "v_add_u32 v90, 128, v0\n"
        "v_lshrrev_b32 v91, 11, v91\n"
        "v_pk_mul_f32 v[222:223], v[144:145], v[194:195] op_sel_hi:[1,0]\n"
        "v_mul_u32_u24 v91, 1120, v91\n"
        "v_pk_mul_f32 v[224:225], v[146:147], v[194:195] op_sel_hi:[1,0]\n"
        "v_lshl_add_u32 v88, v88, 4, v91\n"
        "v_mul_u32_u24 v91, 205, v89\n"
        "v_pk_mul_f32 v[226:227], v[148:149], v[194:195] op_sel_hi:[1,0]\n"
        "buffer_load_dwordx4 v252, s[40:43], s85 offen offset:3072 sc1 lds\n"
        "s_add_u32 m0, s90, 4096\n"
        "s_mov_b32 s85, 0x1d9c00\n"
        "v_lshrrev_b32 v91, 11, v91\n"
        "v_pk_mul_f32 v[212:213], v[150:151], v[194:195] op_sel_hi:[1,0]\n"
        "v_mul_u32_u24 v91, 1120, v91\n"
        "v_pk_fma_f32 v[144:145], v[144:145], v[194:195], v[222:223] op_sel:[1,1,0] op_sel_hi:[0,1,1] neg_hi:[0,1,0]\n"
        "v_lshl_add_u32 v89, v89, 4, v91\n"
        "v_mul_u32_u24 v91, 205, v90\n"
        "v_pk_fma_f32 v[146:147], v[146:147], v[194:195], v[224:225] op_sel:[1,1,0] op_sel_hi:[0,1,1] neg_hi:[0,1,0]\n"
        "v_pk_fma_f32 v[148:149], v[148:149], v[194:195], v[226:227] op_sel:[1,1,0] op_sel_hi:[0,1,1] neg_hi:[0,1,0]\n"
        "buffer_load_dwordx4 v251, s[40:43], s84 offen offset:0 sc1 lds\n"
        "s_mov_b32 s84, 0x22b800\n"
        "v_pk_fma_f32 v[150:151], v[150:151], v[194:195], v[212:213] op_sel:[1,1,0] op_sel_hi:[0,1,1] neg_hi:[0,1,0]\n"
        "v_pk_mul_f32 v[214:215], v[152:153], v[196:197] op_sel_hi:[1,0]\n"
        "v_pk_mul_f32 v[216:217], v[154:155], v[196:197] op_sel_hi:[1,0]\n"
        "v_pk_mul_f32 v[218:219], v[156:157], v[196:197] op_sel_hi:[1,0]\n"
        "v_pk_mul_f32 v[220:221], v[158:159], v[196:197] op_sel_hi:[1,0]\n"
        "v_pk_mul_f32 v[222:223], v[160:161], v[198:199] op_sel_hi:[1,0]\n"
        "v_pk_mul_f32 v[224:225], v[162:163], v[198:199] op_sel_hi:[1,0]\n"
        "v_pk_mul_f32 v[226:227], v[164:165], v[198:199] op_sel_hi:[1,0]\n"
        "v_pk_mul_f32 v[212:213], v[166:167], v[198:199] op_sel_hi:[1,0]\n"
        "buffer_load_dwordx4 v252, s[40:43], s85 offen offset:1024 sc1 lds\n"
        "s_mov_b32 s85, 0x27d400\n"
        "v_lshrrev_b32 v91, 11, v91\n"
        "v_pk_fma_f32 v[152:153], v[152:153], v[196:197], v[214:215] op_sel:[1,1,0] op_sel_hi:[0,1,1] neg_hi:[0,1,0]\n"
        "v_pk_fma_f32 v[154:155], v[154:155], v[196:197], v[216:217] op_sel:[1,1,0] op_sel_hi:[0,1,1] neg_hi:[0,1,0]\n"
        "v_pk_fma_f32 v[156:157], v[156:157], v[196:197], v[218:219] op_sel:[1,1,0] op_sel_hi:[0,1,1] neg_hi:[0,1,0]\n"
        "v_pk_fma_f32 v[158:159], v[158:159], v[196:197], v[220:221] op_sel:[1,1,0] op_sel_hi:[0,1,1] neg_hi:[0,1,0]\n"
        "v_pk_fma_f32 v[160:161], v[160:161], v[198:199], v[222:223] op_sel:[1,1,0] op_sel_hi:[0,1,1] neg_hi:[0,1,0]\n"
        "v_pk_fma_f32 v[162:163], v[162:163], v[198:199], v[224:225] op_sel:[1,1,0] op_sel_hi:[0,1,1] neg_hi:[0,1,0]\n"
        "v_pk_fma_f32 v[164:165], v[164:165], v[198:199], v[226:227] op_sel:[1,1,0] op_sel_hi:[0,1,1] neg_hi:[0,1,0]\n"
        "v_pk_fma_f32 v[166:167], v[166:167], v[198:199], v[212:213] op_sel:[1,1,0] op_sel_hi:[0,1,1] neg_hi:[0,1,0]\n"
        "buffer_load_dwordx4 v251, s[40:43], s84 offen offset:2048 sc1 lds\n"
        "s_mov_b32 s84, 0xe00000\n"
        "v_pk_mul_f32 v[214:215], v[168:169], v[200:201] op_sel_hi:[1,0]\n"
        "v_pk_mul_f32 v[216:217], v[170:171], v[200:201] op_sel_hi:[1,0]\n"
        "v_pk_mul_f32 v[218:219], v[172:173], v[200:201] op_sel_hi:[1,0]\n"
        "v_pk_mul_f32 v[220:221], v[174:175], v[200:201] op_sel_hi:[1,0]\n"
        "v_pk_mul_f32 v[222:223], v[176:177], v[202:203] op_sel_hi:[1,0]\n"
        "v_pk_mul_f32 v[224:225], v[178:179], v[202:203] op_sel_hi:[1,0]\n"
        "v_pk_mul_f32 v[226:227], v[180:181], v[202:203] op_sel_hi:[1,0]\n"
        "v_pk_mul_f32 v[212:213], v[182:183], v[202:203] op_sel_hi:[1,0]\n"
        "v_mul_u32_u24 v91, 1120, v91\n"
        "buffer_load_dwordx4 v252, s[40:43], s85 offen offset:3072 sc1 lds\n"
        "s_add_u32 m0, s90, 81920\n"
        "s_mov_b32 s85, 0xf47c00\n"
        "v_cvt_pk_bf16_f32 v228, v144, v145\n"
        "v_cvt_pk_bf16_f32 v229, v146, v147\n"
        "v_cvt_pk_bf16_f32 v230, v148, v149\n"
        "v_cvt_pk_bf16_f32 v231, v150, v151\n"
        "v_cvt_pk_bf16_f32 v232, v152, v153\n"
        "v_cvt_pk_bf16_f32 v233, v154, v155\n"
        "v_cvt_pk_bf16_f32 v234, v156, v157\n"
        "v_cvt_pk_bf16_f32 v235, v158, v159\n"
        "buffer_load_dwordx4 v250, s[40:43], s84 offen offset:0 sc1 lds\n"
        "s_mov_b32 s84, 0xe04a00\n"
        "v_pk_fma_f32 v[168:169], v[168:169], v[200:201], v[214:215] op_sel:[1,1,0] op_sel_hi:[0,1,1] neg_hi:[0,1,0]\n"
        "v_pk_fma_f32 v[170:171], v[170:171], v[200:201], v[216:217] op_sel:[1,1,0] op_sel_hi:[0,1,1] neg_hi:[0,1,0]\n"
        "v_pk_fma_f32 v[172:173], v[172:173], v[200:201], v[218:219] op_sel:[1,1,0] op_sel_hi:[0,1,1] neg_hi:[0,1,0]\n"
        "v_pk_fma_f32 v[174:175], v[174:175], v[200:201], v[220:221] op_sel:[1,1,0] op_sel_hi:[0,1,1] neg_hi:[0,1,0]\n"
        "v_pk_fma_f32 v[176:177], v[176:177], v[202:203], v[222:223] op_sel:[1,1,0] op_sel_hi:[0,1,1] neg_hi:[0,1,0]\n"
        "v_pk_fma_f32 v[178:179], v[178:179], v[202:203], v[224:225] op_sel:[1,1,0] op_sel_hi:[0,1,1] neg_hi:[0,1,0]\n"
        "v_pk_fma_f32 v[180:181], v[180:181], v[202:203], v[226:227] op_sel:[1,1,0] op_sel_hi:[0,1,1] neg_hi:[0,1,0]\n"
        "v_pk_fma_f32 v[182:183], v[182:183], v[202:203], v[212:213] op_sel:[1,1,0] op_sel_hi:[0,1,1] neg_hi:[0,1,0]\n"
        "v_lshl_add_u32 v90, v90, 4, v91\n"
        "buffer_load_dwordx4 v253, s[40:43], s85 offen offset:1024 sc1 lds\n"
        "s_mov_b32 s85, 0xf4c600\n"
        "v_accvgpr_write_b32 a224, v228\n"
        "v_accvgpr_write_b32 a225, v229\n"
        "v_accvgpr_write_b32 a226, v230\n"
        "v_accvgpr_write_b32 a227, v231\n"
        "v_accvgpr_write_b32 a228, v232\n"
        "v_accvgpr_write_b32 a229, v233\n"
        "v_accvgpr_write_b32 a230, v234\n"
        "v_accvgpr_write_b32 a231, v235\n"
        "v_add_u32 v16, 81920, v4\n"
        "buffer_load_dwordx4 v250, s[40:43], s84 offen offset:2048 sc1 lds\n"
        "s_mov_b32 s84, 0xe0a400\n"
        "v_cvt_pk_bf16_f32 v236, v160, v161\n"
        "v_cvt_pk_bf16_f32 v237, v162, v163\n"
        "v_cvt_pk_bf16_f32 v238, v164, v165\n"
        "v_cvt_pk_bf16_f32 v239, v166, v167\n"
        "v_cvt_pk_bf16_f32 v228, v168, v169\n"
        "v_cvt_pk_bf16_f32 v229, v170, v171\n"
        "v_cvt_pk_bf16_f32 v230, v172, v173\n"
        "v_cvt_pk_bf16_f32 v231, v174, v175\n"
        "v_cvt_pk_bf16_f32 v232, v176, v177\n"
        "buffer_load_dwordx4 v253, s[40:43], s85 offen offset:3072 sc1 lds\n"
        "s_add_u32 m0, s90, 86016\n"
        "s_add_u32 s70, s70, 40960\n"
        "v_lshl_add_u32 v244, v244, 7, s70\n"
        "s_and_b32 s70, s3, 1\n"
        "v_add_u32 v5, v244, v242\n"
        "v_xor_b32 v242, 16, v242\n"
        "s_lshl_b32 s70, s70, 2\n"
        "v_add_u32 v6, v244, v242\n"
        "v_bfe_u32 v242, v1, 1, 1\n"
        "v_mul_u32_u24 v244, 4160, v241\n"
        "buffer_load_dwordx4 v250, s[40:43], s84 offen offset:0 sc1 lds\n"
        "v_or_b32 v242, v242, v243\n"
        "v_lshl_add_u32 v244, v245, 3, v244\n"
        "v_xor_b32 v242, s70, v242\n"
        "s_lshl_b32 s70, s3, 10\n"
        "s_add_u32 s70, s70, 40960\n"
        "s_mov_b32 s85, 0xf52000\n"
        "v_add_u32 v244, s70, v244\n"
        "s_lshl_b32 s70, s91, 8\n"
        "s_add_u32 s86, s86, s70\n"
        "s_add_u32 s86, s86, 0x300000\n"
        "buffer_load_dwordx4 v253, s[40:43], s85 offen offset:1024 sc1 lds\n"
        "s_lshl_b32 s68, s3, 7\n"
        "s_add_u32 s68, s68, s91\n"
        "s_lshl_b32 s68, s68, 7\n"
        "s_add_u32 s68, s68, 11665408\n"
        "s_add_u32 s12, s3, 1\n"
        "s_and_b32 s12, s12, 3\n"
        "s_add_u32 s13, s3, 2\n"
        "s_and_b32 s13, s13, 3\n"
        "s_add_u32 s14, s3, 3\n"
        "s_and_b32 s14, s14, 3\n"
        "s_add_u32 s15, s3, 4\n"
        "s_mul_i32 s70, s66, 163840\n"
        "s_and_b32 s15, s15, 3\n"
        "s_add_u32 s70, s70, s71\n"
        "s_add_u32 s70, s70, 0x600000\n"
        "s_mul_i32 s16, s12, 40960\n"
        "s_mul_i32 s17, s13, 40960\n"
        "s_mul_i32 s18, s14, 40960\n"
        "s_mul_i32 s19, s15, 40960\n"
        "s_add_u32 s16, s16, s70\n"
        "s_add_u32 s17, s17, s70\n"
        "s_add_u32 s18, s18, s70\n"
        "s_add_u32 s19, s19, s70\n"
        "s_mul_i32 s71, s3, 640\n"
        "s_lshl_b32 s48, s66, 12\n"
        "s_mov_b32 s84, 0xe0ee00\n"
        "s_add_u32 s48, s48, s71\n"
        "buffer_load_dwordx4 v250, s[40:43], s84 offen offset:2048 sc1 lds\n"
        "s_mul_i32 s70, s66, 86016\n"
        "s_mul_i32 s71, s3, 5120\n"
        "s_add_u32 s48, s48, 11796480\n"
        "s_add_u32 s70, s70, s71\n"
        "s_add_u32 s70, s70, 0x300000\n"
        "s_lshl_b32 s71, s66, 10\n"
        "s_mul_i32 s20, s12, 21504\n"
        "s_add_u32 s71, s71, 32768\n"
        "s_add_u32 s20, s20, s70\n"
        "s_lshl_b32 s24, s12, 8\n"
        "s_mul_i32 s28, s12, 21504\n"
        "s_mul_i32 s21, s13, 21504\n"
        "s_add_u32 s24, s24, s71\n"
        "s_add_u32 s28, s28, 84992\n"
        "s_add_u32 s21, s21, s70\n"
        "s_lshl_b32 s25, s13, 8\n"
        "s_mul_i32 s29, s13, 21504\n"
        "s_mul_i32 s22, s14, 21504\n"
        "s_add_u32 s25, s25, s71\n"
        "s_add_u32 s29, s29, 79616\n"
        "s_add_u32 s22, s22, s70\n"
        "s_lshl_b32 s26, s14, 8\n"
        "s_mul_i32 s30, s14, 21504\n"
        "s_mul_i32 s23, s15, 21504\n"
        "s_add_u32 s26, s26, s71\n"
        "v_lshlrev_b32 v242, 4, v242\n"
        "s_add_u32 s30, s30, 74240\n"
        "s_add_u32 s23, s23, s70\n"
        "s_lshl_b32 s27, s15, 8\n"
        "s_mul_i32 s31, s15, 21504\n"
        "v_xor_b32 v245, 0, v242\n"
        "s_add_u32 s27, s27, s71\n"
        "s_add_u32 s31, s31, 90368\n"
        "s_lshl_b32 s70, s66, 2\n"
        "v_add_u32 v7, v244, v245\n"
        "v_xor_b32 v245, 16, v242\n"
        "s_add_u32 s70, s70, s3\n"
        "v_add_u32 v8, v244, v245\n"
        "s_mul_i32 s44, s70, 20480\n"
        "s_lshl_b32 s45, s69, 1\n"
        "v_xor_b32 v245, 32, v242\n"
        "s_mov_b32 s85, 0xf56a00\n"
        "s_add_u32 s44, s44, s45\n"
        "s_mul_i32 s47, s70, 21504\n"
        "buffer_load_dwordx4 v253, s[40:43], s85 offen offset:3072 sc1 lds\n"
        "v_add_u32 v9, v244, v245\n"
        "v_xor_b32 v245, 48, v242\n"
        "s_lshl_b32 s46, s44, 1\n"
        "s_add_u32 s47, s47, 0x305000\n"
        "s_lshr_b32 s49, s66, 3\n"
        "v_add_u32 v10, v244, v245\n"
        "v_mul_u32_u24 v244, 21504, v2\n"
        "v_cvt_pk_bf16_f32 v233, v178, v179\n"
        "v_cvt_pk_bf16_f32 v234, v180, v181\n"
        "v_cvt_pk_bf16_f32 v235, v182, v183\n"
        "v_subrev_u32 v90, 2048, v90\n"
        "v_cmp_gt_u32 vcc, 32, v0\n"
        "v_mov_b32 v91, 0x40000000\n"
        "s_lshl_b32 s49, s49, 14\n"
        "v_xor_b32 v18, 64, v4\n"
        "v_lshl_add_u32 v11, v1, 4, v244\n"
        "v_xor_b32 v17, 64, v16\n"
        "v_accvgpr_write_b32 a232, v236\n"
        "v_accvgpr_write_b32 a233, v237\n"
        "v_accvgpr_write_b32 a234, v238\n"
        "v_accvgpr_write_b32 a235, v239\n"
        "v_accvgpr_write_b32 a236, v228\n"
        "v_accvgpr_write_b32 a237, v229\n"
        "v_accvgpr_write_b32 a238, v230\n"
        "v_accvgpr_write_b32 a239, v231\n"
        "v_accvgpr_write_b32 a240, v232\n"
        "v_accvgpr_write_b32 a241, v233\n"
        "v_accvgpr_write_b32 a242, v234\n"
        "v_accvgpr_write_b32 a243, v235\n"
        "s_add_u32 s49, s49, 11665408\n"
        "v_subrev_u32 v89, 1024, v89\n"
        "v_cndmask_b32 v90, v91, v90, vcc\n"
        "s_waitcnt vmcnt(8)\n"
        "s_barrier\n"
        "ds_read_b128 v[208:211], v4 offset:0\n"
        "ds_read_b128 v[212:215], v18 offset:0\n"
        "ds_read_b128 v[216:219], v4 offset:8192\n"
        "ds_read_b128 v[220:223], v18 offset:8192\n"
        "ds_read_b128 v[224:227], v4 offset:16384\n"
        "ds_read_b128 v[228:231], v18 offset:16384\n"
        "ds_read_b128 v[232:235], v4 offset:24576\n"
        "ds_read_b128 v[236:239], v18 offset:24576\n"
        "s_waitcnt lgkmcnt(7)\n"
        "v_mfma_f32_16x16x32_bf16 v[144:147], v[208:211], a[200:203], 0\n"
        "s_waitcnt lgkmcnt(6)\n"
        "v_mfma_f32_16x16x32_bf16 v[148:151], v[212:215], a[200:203], 0\n"
        "s_waitcnt lgkmcnt(5)\n"
        "v_mfma_f32_16x16x32_bf16 v[160:163], v[216:219], a[200:203], 0\n"
        "s_waitcnt lgkmcnt(4)\n"
        "v_mfma_f32_16x16x32_bf16 v[164:167], v[220:223], a[200:203], 0\n"
        "v_mfma_f32_16x16x32_bf16 v[152:155], v[208:211], a[204:207], 0\n"
        "v_mfma_f32_16x16x32_bf16 v[156:159], v[212:215], a[204:207], 0\n"
        "v_mfma_f32_16x16x32_bf16 v[168:171], v[216:219], a[204:207], 0\n"
        "v_mfma_f32_16x16x32_bf16 v[172:175], v[220:223], a[204:207], 0\n"
        "s_waitcnt lgkmcnt(3)\n"
        "v_mfma_f32_16x16x32_bf16 v[176:179], v[224:227], a[200:203], 0\n"
        "s_waitcnt lgkmcnt(2)\n"
        "v_mfma_f32_16x16x32_bf16 v[180:183], v[228:231], a[200:203], 0\n"
        "s_waitcnt lgkmcnt(1)\n"
        "v_mfma_f32_16x16x32_bf16 v[192:195], v[232:235], a[200:203], 0\n"
        "s_waitcnt lgkmcnt(0)\n"
        "v_mfma_f32_16x16x32_bf16 v[196:199], v[236:239], a[200:203], 0\n"
        "v_mfma_f32_16x16x32_bf16 v[184:187], v[224:227], a[204:207], 0\n"
        "v_mfma_f32_16x16x32_bf16 v[188:191], v[228:231], a[204:207], 0\n"
        "v_mfma_f32_16x16x32_bf16 v[200:203], v[232:235], a[204:207], 0\n"
        "v_mfma_f32_16x16x32_bf16 v[204:207], v[236:239], a[204:207], 0\n"
        "v_mfma_f32_16x16x32_bf16 v[144:147], v[212:215], a[208:211], v[144:147]\n"
        "v_mfma_f32_16x16x32_bf16 v[148:151], v[208:211], a[216:219], v[148:151]\n"
        "v_mfma_f32_16x16x32_bf16 v[160:163], v[220:223], a[208:211], v[160:163]\n"
        "v_mfma_f32_16x16x32_bf16 v[164:167], v[216:219], a[216:219], v[164:167]\n"
        "v_mfma_f32_16x16x32_bf16 v[152:155], v[212:215], a[212:215], v[152:155]\n"
        "v_mfma_f32_16x16x32_bf16 v[156:159], v[208:211], a[220:223], v[156:159]\n"
        "v_mfma_f32_16x16x32_bf16 v[168:171], v[220:223], a[212:215], v[168:171]\n"
        "v_mfma_f32_16x16x32_bf16 v[172:175], v[216:219], a[220:223], v[172:175]\n"
        "v_mfma_f32_16x16x32_bf16 v[176:179], v[228:231], a[208:211], v[176:179]\n"
        "v_mfma_f32_16x16x32_bf16 v[180:183], v[224:227], a[216:219], v[180:183]\n"
        "v_mfma_f32_16x16x32_bf16 v[192:195], v[236:239], a[208:211], v[192:195]\n"
        "v_mfma_f32_16x16x32_bf16 v[196:199], v[232:235], a[216:219], v[196:199]\n"
        "v_mfma_f32_16x16x32_bf16 v[184:187], v[228:231], a[212:215], v[184:187]\n"
        "v_mfma_f32_16x16x32_bf16 v[188:191], v[224:227], a[220:223], v[188:191]\n"
        "v_mfma_f32_16x16x32_bf16 v[200:203], v[236:239], a[212:215], v[200:203]\n"
        "v_mfma_f32_16x16x32_bf16 v[204:207], v[232:235], a[220:223], v[204:207]\n"
        "v_cmp_ne_u64 vcc, s[64:65], v[248:249]\n"
        "s_cbranch_vccz L20_%=\n"
        "s_nop 7\n"
        "s_nop 7\n"
        "s_nop 7\n"
        "s_waitcnt vmcnt(0) lgkmcnt(0)\n"
        "s_mov_b32 s6, 65536\n"
        "L19_%=:\n"
        "s_sleep 4\n"
        "buffer_load_dwordx2 v[248:249], v246, s[40:43], 0 offen sc1\n"
        "s_waitcnt vmcnt(0)\n"
        "v_cmp_ne_u64 vcc, s[64:65], v[248:249]\n"
        "s_cbranch_vccz L20_%=\n"
        "s_sub_u32 s6, s6, 1\n"
        "s_cmp_lg_u32 s6, 0\n"
        "s_cbranch_scc1 L19_%=\n"
        "L20_%=:\n"
        "s_waitcnt vmcnt(0)\n"
        "s_barrier\n"
        "s_mul_i32 s73, s3, 3072\n"
        "s_add_u32 m0, s73, 0\n"
        "s_nop 0\n"
        "buffer_load_dwordx4 v88, s[8:11], s44 offen offset:0 lds\n"
        "s_add_u32 s84, s67, 0\n"
        "s_add_u32 s85, s67, 10240\n"
        "ds_read_b128 v[24:27], v16 offset:0\n"
        "ds_read_b128 v[28:31], v17 offset:0\n"
        "ds_read_b128 v[48:51], v16 offset:8192\n"
        "ds_read_b128 v[52:55], v17 offset:8192\n"
        "ds_read_b128 v[72:75], v16 offset:16384\n"
        "ds_read_b128 v[76:79], v17 offset:16384\n"
        "ds_read_b128 v[96:99], v16 offset:24576\n"
        "ds_read_b128 v[100:103], v17 offset:24576\n"
        "v_pk_mul_f32 v[242:243], v[144:145], v[112:113]\n"
        "v_pk_mul_f32 v[244:245], v[148:149], v[112:113]\n"
        "v_pk_mul_f32 v[246:247], v[146:147], v[114:115]\n"
        "v_pk_mul_f32 v[248:249], v[150:151], v[114:115]\n"
        "buffer_load_dwordx4 v89, s[8:11], s44 offen offset:1024 lds\n"
        "v_pk_mul_f32 v[250:251], v[160:161], v[128:129]\n"
        "v_pk_mul_f32 v[252:253], v[164:165], v[128:129]\n"
        "v_pk_mul_f32 v[254:255], v[162:163], v[130:131]\n"
        "v_pk_mul_f32 v[240:241], v[166:167], v[130:131]\n"
        "v_pk_fma_f32 v[242:243], v[148:149], v[116:117], v[242:243]\n"
        "v_pk_fma_f32 v[244:245], v[144:145], v[116:117], v[244:245] neg_lo:[1,0,0] neg_hi:[1,0,0]\n"
        "v_pk_fma_f32 v[246:247], v[150:151], v[118:119], v[246:247]\n"
        "v_pk_fma_f32 v[248:249], v[146:147], v[118:119], v[248:249] neg_lo:[1,0,0] neg_hi:[1,0,0]\n"
        "v_pk_fma_f32 v[250:251], v[164:165], v[132:133], v[250:251]\n"
        "v_pk_fma_f32 v[252:253], v[160:161], v[132:133], v[252:253] neg_lo:[1,0,0] neg_hi:[1,0,0]\n"
        "v_pk_fma_f32 v[254:255], v[166:167], v[134:135], v[254:255]\n"
        "v_pk_fma_f32 v[240:241], v[162:163], v[134:135], v[240:241] neg_lo:[1,0,0] neg_hi:[1,0,0]\n"
        "v_cvt_pk_bf16_f32 v216, v242, v243\n"
        "v_cvt_pk_bf16_f32 v220, v244, v245\n"
        "buffer_load_dwordx4 v90, s[8:11], s44 offen offset:2048 lds\n"
        "v_cvt_pk_bf16_f32 v217, v246, v247\n"
        "v_cvt_pk_bf16_f32 v221, v248, v249\n"
        "v_cvt_pk_bf16_f32 v218, v250, v251\n"
        "v_cvt_pk_bf16_f32 v222, v252, v253\n"
        "v_cvt_pk_bf16_f32 v219, v254, v255\n"
        "v_cvt_pk_bf16_f32 v223, v240, v241\n"
        "v_pk_mul_f32 v[242:243], v[176:177], v[112:113]\n"
        "v_pk_mul_f32 v[244:245], v[180:181], v[112:113]\n"
        "v_mfma_f32_16x16x32_bf16 v[144:147], a[200:203], v[216:219], 0\n"
        "s_waitcnt lgkmcnt(5)\n"
        "v_lshlrev_b32 v40, 16, v48\n"
        "v_and_b32 v41, 0xffff0000, v48\n"
        "v_mfma_f32_16x16x32_bf16 v[148:151], a[200:203], v[220:223], 0\n"
        "buffer_load_dwordx4 a[0:3], v12, s[40:43], s84 offen offset:0 sc1\n"
        "v_mfma_f32_16x16x32_bf16 v[144:147], a[208:211], v[220:223], v[144:147]\n"
        "v_lshlrev_b32 v42, 16, v49\n"
        "v_and_b32 v43, 0xffff0000, v49\n"
        "v_mfma_f32_16x16x32_bf16 v[148:151], a[216:219], v[216:219], v[148:151]\n"
        "v_lshlrev_b32 v44, 16, v50\n"
        "v_and_b32 v45, 0xffff0000, v50\n"
        "v_mfma_f32_16x16x32_bf16 v[160:163], a[204:207], v[216:219], 0\n"
        "v_lshlrev_b32 v46, 16, v51\n"
        "v_and_b32 v47, 0xffff0000, v51\n"
        "v_mfma_f32_16x16x32_bf16 v[164:167], a[204:207], v[220:223], 0\n"
        "buffer_load_dwordx4 a[4:7], v12, s[40:43], s84 offen offset:1024 sc1\n"
        "v_mfma_f32_16x16x32_bf16 v[160:163], a[212:215], v[220:223], v[160:163]\n"
        "s_waitcnt lgkmcnt(1)\n"
        "v_lshlrev_b32 v88, 16, v96\n"
        "v_and_b32 v89, 0xffff0000, v96\n"
        "v_mfma_f32_16x16x32_bf16 v[164:167], a[220:223], v[216:219], v[164:167]\n"
        "v_lshlrev_b32 v90, 16, v97\n"
        "v_and_b32 v91, 0xffff0000, v97\n"
        "v_pk_mul_f32 v[246:247], v[178:179], v[114:115]\n"
        "v_pk_mul_f32 v[248:249], v[182:183], v[114:115]\n"
        "v_pk_mul_f32 v[250:251], v[192:193], v[128:129]\n"
        "v_pk_mul_f32 v[252:253], v[196:197], v[128:129]\n"
        "v_pk_mul_f32 v[254:255], v[194:195], v[130:131]\n"
        "v_pk_mul_f32 v[240:241], v[198:199], v[130:131]\n"
        "buffer_load_dwordx4 a[8:11], v12, s[40:43], s84 offen offset:2048 sc1\n"
        "v_pk_fma_f32 v[242:243], v[180:181], v[116:117], v[242:243]\n"
        "v_pk_fma_f32 v[244:245], v[176:177], v[116:117], v[244:245] neg_lo:[1,0,0] neg_hi:[1,0,0]\n"
        "v_pk_fma_f32 v[246:247], v[182:183], v[118:119], v[246:247]\n"
        "v_pk_fma_f32 v[248:249], v[178:179], v[118:119], v[248:249] neg_lo:[1,0,0] neg_hi:[1,0,0]\n"
        "v_pk_fma_f32 v[250:251], v[196:197], v[132:133], v[250:251]\n"
        "v_pk_fma_f32 v[252:253], v[192:193], v[132:133], v[252:253] neg_lo:[1,0,0] neg_hi:[1,0,0]\n"
        "v_pk_fma_f32 v[254:255], v[198:199], v[134:135], v[254:255]\n"
        "v_pk_fma_f32 v[240:241], v[194:195], v[134:135], v[240:241] neg_lo:[1,0,0] neg_hi:[1,0,0]\n"
        "v_cvt_pk_bf16_f32 v224, v242, v243\n"
        "v_cvt_pk_bf16_f32 v228, v244, v245\n"
        "v_cvt_pk_bf16_f32 v225, v246, v247\n"
        "v_cvt_pk_bf16_f32 v229, v248, v249\n"
        "v_cvt_pk_bf16_f32 v226, v250, v251\n"
        "v_cvt_pk_bf16_f32 v230, v252, v253\n"
        "buffer_load_dwordx4 a[12:15], v12, s[40:43], s84 offen offset:3072 sc1\n"
        "v_cvt_pk_bf16_f32 v227, v254, v255\n"
        "v_cvt_pk_bf16_f32 v231, v240, v241\n"
        "v_pk_mul_f32 v[244:245], v[156:157], v[120:121]\n"
        "v_pk_mul_f32 v[248:249], v[158:159], v[122:123]\n"
        "v_mfma_f32_16x16x32_bf16 v[176:179], a[200:203], v[224:227], 0\n"
        "v_lshlrev_b32 v92, 16, v98\n"
        "v_and_b32 v93, 0xffff0000, v98\n"
        "v_mfma_f32_16x16x32_bf16 v[180:183], a[200:203], v[228:231], 0\n"
        "v_lshlrev_b32 v94, 16, v99\n"
        "v_and_b32 v95, 0xffff0000, v99\n"
        "v_mfma_f32_16x16x32_bf16 v[176:179], a[208:211], v[228:231], v[176:179]\n"
        "buffer_load_dwordx4 a[16:19], v13, s[40:43], s84 offen offset:0 sc1\n"
        "v_mfma_f32_16x16x32_bf16 v[180:183], a[216:219], v[224:227], v[180:183]\n"
        "v_lshlrev_b32 v16, 16, v24\n"
        "v_and_b32 v17, 0xffff0000, v24\n"
        "v_mfma_f32_16x16x32_bf16 v[192:195], a[204:207], v[224:227], 0\n"
        "v_lshlrev_b32 v18, 16, v25\n"
        "v_and_b32 v19, 0xffff0000, v25\n"
        "v_mfma_f32_16x16x32_bf16 v[196:199], a[204:207], v[228:231], 0\n"
        "s_mul_i32 s72, s3, 10240\n"
        "v_lshlrev_b32 v20, 16, v26\n"
        "v_mfma_f32_16x16x32_bf16 v[192:195], a[212:215], v[228:231], v[192:195]\n"
        "buffer_load_dwordx4 a[20:23], v13, s[40:43], s84 offen offset:1024 sc1\n"
        "v_mfma_f32_16x16x32_bf16 v[196:199], a[220:223], v[224:227], v[196:199]\n"
        "v_and_b32 v21, 0xffff0000, v26\n"
        "v_lshlrev_b32 v22, 16, v27\n"
        "v_pk_mul_f32 v[252:253], v[172:173], v[136:137]\n"
        "v_pk_mul_f32 v[240:241], v[174:175], v[138:139]\n"
        "v_pk_fma_f32 v[244:245], v[152:153], v[124:125], v[244:245] neg_lo:[1,0,0] neg_hi:[1,0,0]\n"
        "v_pk_fma_f32 v[248:249], v[154:155], v[126:127], v[248:249] neg_lo:[1,0,0] neg_hi:[1,0,0]\n"
        "v_pk_fma_f32 v[252:253], v[168:169], v[140:141], v[252:253] neg_lo:[1,0,0] neg_hi:[1,0,0]\n"
        "v_pk_fma_f32 v[240:241], v[170:171], v[142:143], v[240:241] neg_lo:[1,0,0] neg_hi:[1,0,0]\n"
        "v_pk_mul_f32 v[242:243], v[152:153], v[120:121]\n"
        "v_pk_mul_f32 v[246:247], v[154:155], v[122:123]\n"
        "v_cvt_pk_bf16_f32 v236, v244, v245\n"
        "v_cvt_pk_bf16_f32 v237, v248, v249\n"
        "buffer_load_dwordx4 a[24:27], v13, s[40:43], s84 offen offset:2048 sc1\n"
        "v_cvt_pk_bf16_f32 v238, v252, v253\n"
        "v_cvt_pk_bf16_f32 v239, v240, v241\n"
        "v_pk_fma_f32 v[242:243], v[156:157], v[124:125], v[242:243]\n"
        "v_pk_fma_f32 v[246:247], v[158:159], v[126:127], v[246:247]\n"
        "v_pk_mul_f32 v[250:251], v[168:169], v[136:137]\n"
        "v_mfma_f32_16x16x32_bf16 v[156:159], a[200:203], v[236:239], 0\n"
        "v_cvt_pk_bf16_f32 v232, v242, v243\n"
        "v_cvt_pk_bf16_f32 v233, v246, v247\n"
        "v_pk_mul_f32 v[254:255], v[170:171], v[138:139]\n"
        "v_pk_mul_f32 v[244:245], v[188:189], v[120:121]\n"
        "v_pk_mul_f32 v[248:249], v[190:191], v[122:123]\n"
        "v_pk_mul_f32 v[252:253], v[204:205], v[136:137]\n"
        "v_pk_mul_f32 v[240:241], v[206:207], v[138:139]\n"
        "buffer_load_dwordx4 a[28:31], v13, s[40:43], s84 offen offset:3072 sc1\n"
        "v_pk_fma_f32 v[250:251], v[172:173], v[140:141], v[250:251]\n"
        "v_pk_fma_f32 v[254:255], v[174:175], v[142:143], v[254:255]\n"
        "v_mfma_f32_16x16x32_bf16 v[172:175], a[204:207], v[236:239], 0\n"
        "v_cvt_pk_bf16_f32 v234, v250, v251\n"
        "v_cvt_pk_bf16_f32 v235, v254, v255\n"
        "v_pk_fma_f32 v[244:245], v[184:185], v[124:125], v[244:245] neg_lo:[1,0,0] neg_hi:[1,0,0]\n"
        "v_pk_fma_f32 v[248:249], v[186:187], v[126:127], v[248:249] neg_lo:[1,0,0] neg_hi:[1,0,0]\n"
        "v_pk_fma_f32 v[252:253], v[200:201], v[140:141], v[252:253] neg_lo:[1,0,0] neg_hi:[1,0,0]\n"
        "v_mfma_f32_16x16x32_bf16 v[152:155], a[200:203], v[232:235], 0\n"
        "v_cvt_pk_bf16_f32 v212, v244, v245\n"
        "v_cvt_pk_bf16_f32 v213, v248, v249\n"
        "v_mfma_f32_16x16x32_bf16 v[168:171], a[204:207], v[232:235], 0\n"
        "buffer_load_dwordx4 a[32:35], v14, s[40:43], s84 offen offset:0 sc1\n"
        "v_mfma_f32_16x16x32_bf16 v[152:155], a[208:211], v[236:239], v[152:155]\n"
        "v_cvt_pk_bf16_f32 v214, v252, v253\n"
        "v_lshlrev_b32 v244, 16, v54\n"
        "v_mfma_f32_16x16x32_bf16 v[156:159], a[216:219], v[232:235], v[156:159]\n"
        "v_and_b32 v245, 0xffff0000, v54\n"
        "v_and_b32 v23, 0xffff0000, v27\n"
        "v_mfma_f32_16x16x32_bf16 v[168:171], a[212:215], v[236:239], v[168:171]\n"
        "v_lshlrev_b32 v64, 16, v72\n"
        "v_and_b32 v65, 0xffff0000, v72\n"
        "v_mfma_f32_16x16x32_bf16 v[172:175], a[220:223], v[232:235], v[172:175]\n"
        "buffer_load_dwordx4 a[36:39], v14, s[40:43], s84 offen offset:1024 sc1\n"
        "s_add_u32 s84, s67, 20480\n"
        "v_pk_fma_f32 v[240:241], v[202:203], v[142:143], v[240:241] neg_lo:[1,0,0] neg_hi:[1,0,0]\n"
        "v_pk_mul_f32 v[242:243], v[184:185], v[120:121]\n"
        "v_pk_mul_f32 v[246:247], v[186:187], v[122:123]\n"
        "v_cvt_pk_bf16_f32 v215, v240, v241\n"
        "v_pk_fma_f32 v[242:243], v[188:189], v[124:125], v[242:243]\n"
        "v_pk_fma_f32 v[246:247], v[190:191], v[126:127], v[246:247]\n"
        "v_pk_mul_f32 v[250:251], v[200:201], v[136:137]\n"
        "v_mfma_f32_16x16x32_bf16 v[188:191], a[200:203], v[212:215], 0\n"
        "v_cvt_pk_bf16_f32 v208, v242, v243\n"
        "v_cvt_pk_bf16_f32 v209, v246, v247\n"
        "v_pk_mul_f32 v[254:255], v[202:203], v[138:139]\n"
        "v_pk_fma_f32 v[250:251], v[204:205], v[140:141], v[250:251]\n"
        "buffer_load_dwordx4 a[40:43], v12, s[40:43], s85 offen offset:0 sc1\n"
        "v_pk_fma_f32 v[254:255], v[206:207], v[142:143], v[254:255]\n"
        "v_mfma_f32_16x16x32_bf16 v[204:207], a[204:207], v[212:215], 0\n"
        "v_lshlrev_b32 v240, 16, v52\n"
        "v_and_b32 v241, 0xffff0000, v52\n"
        "v_lshlrev_b32 v242, 16, v53\n"
        "v_and_b32 v243, 0xffff0000, v53\n"
        "v_lshlrev_b32 v246, 16, v55\n"
        "v_and_b32 v247, 0xffff0000, v55\n"
        "v_cvt_pk_bf16_f32 v210, v250, v251\n"
        "v_cvt_pk_bf16_f32 v211, v254, v255\n"
        "v_pk_add_f32 v[248:249], v[40:41], v[240:241] neg_lo:[0,1] neg_hi:[0,1]\n"
        "v_pk_add_f32 v[250:251], v[44:45], v[244:245] neg_lo:[0,1] neg_hi:[0,1]\n"
        "v_pk_add_f32 v[252:253], v[42:43], v[242:243] neg_lo:[0,1] neg_hi:[0,1]\n"
        "v_mfma_f32_16x16x32_bf16 v[184:187], a[200:203], v[208:211], 0\n"
        "buffer_load_dwordx4 a[44:47], v12, s[40:43], s85 offen offset:1024 sc1\n"
        "v_mfma_f32_16x16x32_bf16 v[188:191], a[216:219], v[208:211], v[188:191]\n"
        "v_lshlrev_b32 v66, 16, v73\n"
        "v_and_b32 v67, 0xffff0000, v73\n"
        "v_mfma_f32_16x16x32_bf16 v[200:203], a[204:207], v[208:211], 0\n"
        "v_lshlrev_b32 v224, 16, v76\n"
        "v_and_b32 v225, 0xffff0000, v76\n"
        "v_mfma_f32_16x16x32_bf16 v[204:207], a[220:223], v[208:211], v[204:207]\n"
        "v_lshlrev_b32 v208, 16, v28\n"
        "v_and_b32 v209, 0xffff0000, v28\n"
        "v_mfma_f32_16x16x32_bf16 v[184:187], a[208:211], v[212:215], v[184:187]\n"
        "buffer_load_dwordx4 a[48:51], v12, s[40:43], s85 offen offset:2048 sc1\n"
        "v_mfma_f32_16x16x32_bf16 v[200:203], a[212:215], v[212:215], v[200:203]\n"
        "v_lshlrev_b32 v210, 16, v29\n"
        "v_and_b32 v211, 0xffff0000, v29\n"
        "v_pk_add_f32 v[254:255], v[46:47], v[246:247] neg_lo:[0,1] neg_hi:[0,1]\n"
        "v_pk_add_f32 v[40:41], v[40:41], v[240:241]\n"
        "v_pk_add_f32 v[44:45], v[44:45], v[244:245]\n"
        "v_pk_add_f32 v[42:43], v[42:43], v[242:243]\n"
        "v_pk_add_f32 v[46:47], v[46:47], v[246:247]\n"
        "v_pk_mul_f32 v[240:241], v[248:249], v[56:57]\n"
        "v_pk_mul_f32 v[244:245], v[250:251], v[56:57]\n"
        "v_pk_mul_f32 v[242:243], v[252:253], v[58:59]\n"
        "v_pk_mul_f32 v[246:247], v[254:255], v[58:59]\n"
        "v_pk_fma_f32 v[48:49], v[250:251], v[60:61], v[240:241]\n"
        "buffer_load_dwordx4 a[52:55], v12, s[40:43], s85 offen offset:3072 sc1\n"
        "v_pk_fma_f32 v[52:53], v[248:249], v[60:61], v[244:245] neg_lo:[1,0,0] neg_hi:[1,0,0]\n"
        "v_pk_fma_f32 v[56:57], v[250:251], v[60:61], v[240:241] neg_lo:[1,0,0] neg_hi:[1,0,0]\n"
        "v_pk_fma_f32 v[50:51], v[254:255], v[62:63], v[242:243]\n"
        "v_pk_fma_f32 v[54:55], v[252:253], v[62:63], v[246:247] neg_lo:[1,0,0] neg_hi:[1,0,0]\n"
        "v_pk_fma_f32 v[58:59], v[254:255], v[62:63], v[242:243] neg_lo:[1,0,0] neg_hi:[1,0,0]\n"
        "v_pk_fma_f32 v[60:61], v[248:249], v[60:61], v[244:245]\n"
        "v_pk_fma_f32 v[62:63], v[252:253], v[62:63], v[246:247]\n"
        "s_waitcnt lgkmcnt(0)\n"
        "v_lshlrev_b32 v240, 16, v100\n"
        "v_and_b32 v241, 0xffff0000, v100\n"
        "v_lshlrev_b32 v242, 16, v101\n"
        "v_and_b32 v243, 0xffff0000, v101\n"
        "v_lshlrev_b32 v244, 16, v102\n"
        "v_and_b32 v245, 0xffff0000, v102\n"
        "v_lshlrev_b32 v246, 16, v103\n"
        "buffer_load_dwordx4 a[56:59], v13, s[40:43], s85 offen offset:0 sc1\n"
        "v_and_b32 v247, 0xffff0000, v103\n"
        "v_pk_add_f32 v[248:249], v[88:89], v[240:241] neg_lo:[0,1] neg_hi:[0,1]\n"
        "v_pk_add_f32 v[250:251], v[92:93], v[244:245] neg_lo:[0,1] neg_hi:[0,1]\n"
        "v_pk_add_f32 v[252:253], v[90:91], v[242:243] neg_lo:[0,1] neg_hi:[0,1]\n"
        "v_pk_add_f32 v[254:255], v[94:95], v[246:247] neg_lo:[0,1] neg_hi:[0,1]\n"
        "v_pk_add_f32 v[88:89], v[88:89], v[240:241]\n"
        "v_pk_add_f32 v[92:93], v[92:93], v[244:245]\n"
        "v_pk_add_f32 v[90:91], v[90:91], v[242:243]\n"
        "v_pk_add_f32 v[94:95], v[94:95], v[246:247]\n"
        "v_pk_mul_f32 v[240:241], v[248:249], v[104:105]\n"
        "v_pk_mul_f32 v[244:245], v[250:251], v[104:105]\n"
        "v_pk_mul_f32 v[242:243], v[252:253], v[106:107]\n"
        "v_pk_mul_f32 v[246:247], v[254:255], v[106:107]\n"
        "v_pk_add_f32 v[216:217], v[16:17], v[208:209] neg_lo:[0,1] neg_hi:[0,1]\n"
        "buffer_load_dwordx4 a[60:63], v13, s[40:43], s85 offen offset:1024 sc1\n"
        "v_pk_add_f32 v[220:221], v[18:19], v[210:211] neg_lo:[0,1] neg_hi:[0,1]\n"
        "v_pk_add_f32 v[16:17], v[16:17], v[208:209]\n"
        "v_pk_add_f32 v[18:19], v[18:19], v[210:211]\n"
        "v_pk_fma_f32 v[96:97], v[250:251], v[108:109], v[240:241]\n"
        "v_pk_fma_f32 v[100:101], v[248:249], v[108:109], v[244:245] neg_lo:[1,0,0] neg_hi:[1,0,0]\n"
        "v_pk_fma_f32 v[104:105], v[250:251], v[108:109], v[240:241] neg_lo:[1,0,0] neg_hi:[1,0,0]\n"
        "v_pk_fma_f32 v[98:99], v[254:255], v[110:111], v[242:243]\n"
        "v_pk_fma_f32 v[102:103], v[252:253], v[110:111], v[246:247] neg_lo:[1,0,0] neg_hi:[1,0,0]\n"
        "v_pk_fma_f32 v[106:107], v[254:255], v[110:111], v[242:243] neg_lo:[1,0,0] neg_hi:[1,0,0]\n"
        "v_pk_fma_f32 v[108:109], v[248:249], v[108:109], v[244:245]\n"
        "v_pk_fma_f32 v[110:111], v[252:253], v[110:111], v[246:247]\n"
        "v_pk_mul_f32 v[242:243], v[16:17], v[144:145]\n"
        "v_pk_mul_f32 v[244:245], v[16:17], v[148:149]\n"
        "v_pk_mul_f32 v[246:247], v[16:17], v[176:177]\n"
        "buffer_load_dwordx4 a[64:67], v13, s[40:43], s85 offen offset:2048 sc1\n"
        "v_pk_mul_f32 v[248:249], v[16:17], v[180:181]\n"
        "v_pk_mul_f32 v[250:251], v[18:19], v[146:147]\n"
        "v_pk_mul_f32 v[252:253], v[18:19], v[150:151]\n"
        "v_pk_mul_f32 v[254:255], v[18:19], v[178:179]\n"
        "v_pk_mul_f32 v[240:241], v[18:19], v[182:183]\n"
        "v_lshlrev_b32 v212, 16, v30\n"
        "v_and_b32 v213, 0xffff0000, v30\n"
        "v_lshlrev_b32 v214, 16, v31\n"
        "v_and_b32 v215, 0xffff0000, v31\n"
        "v_pk_add_f32 v[218:219], v[20:21], v[212:213] neg_lo:[0,1] neg_hi:[0,1]\n"
        "v_pk_add_f32 v[222:223], v[22:23], v[214:215] neg_lo:[0,1] neg_hi:[0,1]\n"
        "v_pk_add_f32 v[20:21], v[20:21], v[212:213]\n"
        "v_pk_add_f32 v[22:23], v[22:23], v[214:215]\n"
        "v_pk_fma_f32 v[242:243], v[20:21], v[148:149], v[242:243] neg_lo:[1,0,0] neg_hi:[1,0,0]\n"
        "buffer_load_dwordx4 a[68:71], v13, s[40:43], s85 offen offset:3072 sc1\n"
        "v_pk_fma_f32 v[244:245], v[20:21], v[144:145], v[244:245]\n"
        "v_pk_fma_f32 v[246:247], v[20:21], v[180:181], v[246:247] neg_lo:[1,0,0] neg_hi:[1,0,0]\n"
        "v_pk_fma_f32 v[248:249], v[20:21], v[176:177], v[248:249]\n"
        "v_pk_fma_f32 v[250:251], v[22:23], v[150:151], v[250:251] neg_lo:[1,0,0] neg_hi:[1,0,0]\n"
        "v_pk_fma_f32 v[252:253], v[22:23], v[146:147], v[252:253]\n"
        "v_pk_fma_f32 v[254:255], v[22:23], v[182:183], v[254:255] neg_lo:[1,0,0] neg_hi:[1,0,0]\n"
        "v_pk_fma_f32 v[240:241], v[22:23], v[178:179], v[240:241]\n"
        "v_pk_mul_f32 v[208:209], v[216:217], v[32:33]\n"
        "v_pk_mul_f32 v[210:211], v[220:221], v[34:35]\n"
        "v_pk_mul_f32 v[212:213], v[218:219], v[32:33]\n"
        "v_pk_fma_f32 v[24:25], v[218:219], v[36:37], v[208:209]\n"
        "v_pk_mul_f32 v[214:215], v[222:223], v[34:35]\n"
        "v_pk_fma_f32 v[26:27], v[222:223], v[38:39], v[210:211]\n"
        "v_pk_fma_f32 v[28:29], v[216:217], v[36:37], v[212:213] neg_lo:[1,0,0] neg_hi:[1,0,0]\n"
        "buffer_load_dwordx4 a[72:75], v14, s[40:43], s85 offen offset:0 sc1\n"
        "v_pk_fma_f32 v[30:31], v[220:221], v[38:39], v[214:215] neg_lo:[1,0,0] neg_hi:[1,0,0]\n"
        "v_lshlrev_b32 v226, 16, v77\n"
        "v_and_b32 v227, 0xffff0000, v77\n"
        "v_pk_fma_f32 v[242:243], v[24:25], v[176:177], v[242:243]\n"
        "v_pk_fma_f32 v[250:251], v[26:27], v[178:179], v[250:251]\n"
        "v_pk_fma_f32 v[244:245], v[24:25], v[180:181], v[244:245]\n"
        "v_pk_fma_f32 v[252:253], v[26:27], v[182:183], v[252:253]\n"
        "v_lshlrev_b32 v68, 16, v74\n"
        "v_and_b32 v69, 0xffff0000, v74\n"
        "v_lshlrev_b32 v70, 16, v75\n"
        "v_and_b32 v71, 0xffff0000, v75\n"
        "v_lshlrev_b32 v228, 16, v78\n"
        "v_and_b32 v229, 0xffff0000, v78\n"
        "v_lshlrev_b32 v230, 16, v79\n"
        "buffer_load_dwordx4 a[76:79], v14, s[40:43], s85 offen offset:1024 sc1\n"
        "s_add_u32 s85, s67, 30720\n"
        "v_and_b32 v231, 0xffff0000, v79\n"
        "v_pk_add_f32 v[232:233], v[64:65], v[224:225] neg_lo:[0,1] neg_hi:[0,1]\n"
        "v_pk_add_f32 v[236:237], v[66:67], v[226:227] neg_lo:[0,1] neg_hi:[0,1]\n"
        "v_pk_fma_f32 v[242:243], v[28:29], v[180:181], v[242:243] neg_lo:[1,0,0] neg_hi:[1,0,0]\n"
        "v_pk_fma_f32 v[250:251], v[30:31], v[182:183], v[250:251] neg_lo:[1,0,0] neg_hi:[1,0,0]\n"
        "v_pk_fma_f32 v[32:33], v[218:219], v[36:37], v[208:209] neg_lo:[1,0,0] neg_hi:[1,0,0]\n"
        "v_pk_fma_f32 v[34:35], v[222:223], v[38:39], v[210:211] neg_lo:[1,0,0] neg_hi:[1,0,0]\n"
        "v_pk_add_f32 v[64:65], v[64:65], v[224:225]\n"
        "v_pk_add_f32 v[66:67], v[66:67], v[226:227]\n"
        "v_pk_fma_f32 v[244:245], v[28:29], v[176:177], v[244:245]\n"
        "v_pk_fma_f32 v[252:253], v[30:31], v[178:179], v[252:253]\n"
        "v_pk_fma_f32 v[36:37], v[216:217], v[36:37], v[212:213]\n"
        "v_pk_fma_f32 v[38:39], v[220:221], v[38:39], v[214:215]\n"
        "buffer_load_dwordx4 a[80:83], v12, s[40:43], s84 offen offset:0 sc1\n"
        "v_pk_add_f32 v[234:235], v[68:69], v[228:229] neg_lo:[0,1] neg_hi:[0,1]\n"
        "v_pk_add_f32 v[238:239], v[70:71], v[230:231] neg_lo:[0,1] neg_hi:[0,1]\n"
        "v_cvt_pk_bf16_f32 v216, v242, v243\n"
        "v_cvt_pk_bf16_f32 v217, v250, v251\n"
        "v_pk_add_f32 v[68:69], v[68:69], v[228:229]\n"
        "v_pk_mul_f32 v[224:225], v[232:233], v[80:81]\n"
        "v_pk_add_f32 v[70:71], v[70:71], v[230:231]\n"
        "v_pk_mul_f32 v[226:227], v[236:237], v[82:83]\n"
        "v_cvt_pk_bf16_f32 v220, v244, v245\n"
        "v_pk_fma_f32 v[246:247], v[32:33], v[144:145], v[246:247]\n"
        "v_cvt_pk_bf16_f32 v221, v252, v253\n"
        "v_pk_fma_f32 v[254:255], v[34:35], v[146:147], v[254:255]\n"
        "v_pk_mul_f32 v[242:243], v[64:65], v[160:161]\n"
        "v_pk_mul_f32 v[250:251], v[66:67], v[162:163]\n"
        "buffer_load_dwordx4 a[84:87], v12, s[40:43], s84 offen offset:1024 sc1\n"
        "v_pk_fma_f32 v[248:249], v[32:33], v[148:149], v[248:249]\n"
        "v_pk_fma_f32 v[240:241], v[34:35], v[150:151], v[240:241]\n"
        "v_pk_mul_f32 v[244:245], v[64:65], v[164:165]\n"
        "v_pk_mul_f32 v[252:253], v[66:67], v[166:167]\n"
        "v_pk_mul_f32 v[228:229], v[234:235], v[80:81]\n"
        "v_pk_fma_f32 v[72:73], v[234:235], v[84:85], v[224:225]\n"
        "v_pk_mul_f32 v[230:231], v[238:239], v[82:83]\n"
        "v_pk_fma_f32 v[74:75], v[238:239], v[86:87], v[226:227]\n"
        "v_pk_fma_f32 v[246:247], v[36:37], v[148:149], v[246:247] neg_lo:[1,0,0] neg_hi:[1,0,0]\n"
        "v_pk_fma_f32 v[254:255], v[38:39], v[150:151], v[254:255] neg_lo:[1,0,0] neg_hi:[1,0,0]\n"
        "v_pk_fma_f32 v[242:243], v[68:69], v[164:165], v[242:243] neg_lo:[1,0,0] neg_hi:[1,0,0]\n"
        "v_pk_fma_f32 v[250:251], v[70:71], v[166:167], v[250:251] neg_lo:[1,0,0] neg_hi:[1,0,0]\n"
        "v_pk_fma_f32 v[248:249], v[36:37], v[144:145], v[248:249]\n"
        "v_pk_fma_f32 v[240:241], v[38:39], v[146:147], v[240:241]\n"
        "buffer_load_dwordx4 a[88:91], v12, s[40:43], s84 offen offset:2048 sc1\n"
        "v_pk_fma_f32 v[244:245], v[68:69], v[160:161], v[244:245]\n"
        "v_pk_fma_f32 v[252:253], v[70:71], v[162:163], v[252:253]\n"
        "v_pk_fma_f32 v[76:77], v[232:233], v[84:85], v[228:229] neg_lo:[1,0,0] neg_hi:[1,0,0]\n"
        "v_pk_fma_f32 v[80:81], v[234:235], v[84:85], v[224:225] neg_lo:[1,0,0] neg_hi:[1,0,0]\n"
        "v_pk_fma_f32 v[84:85], v[232:233], v[84:85], v[228:229]\n"
        "v_pk_fma_f32 v[78:79], v[236:237], v[86:87], v[230:231] neg_lo:[1,0,0] neg_hi:[1,0,0]\n"
        "v_cvt_pk_bf16_f32 v224, v246, v247\n"
        "v_cvt_pk_bf16_f32 v225, v254, v255\n"
        "v_pk_fma_f32 v[242:243], v[72:73], v[192:193], v[242:243]\n"
        "v_pk_fma_f32 v[250:251], v[74:75], v[194:195], v[250:251]\n"
        "v_cvt_pk_bf16_f32 v228, v248, v249\n"
        "v_cvt_pk_bf16_f32 v229, v240, v241\n"
        "v_pk_fma_f32 v[244:245], v[72:73], v[196:197], v[244:245]\n"
        "v_pk_mul_f32 v[246:247], v[64:65], v[192:193]\n"
        "buffer_load_dwordx4 a[92:95], v12, s[40:43], s84 offen offset:3072 sc1\n"
        "v_pk_fma_f32 v[252:253], v[74:75], v[198:199], v[252:253]\n"
        "v_pk_mul_f32 v[254:255], v[66:67], v[194:195]\n"
        "v_pk_mul_f32 v[248:249], v[64:65], v[196:197]\n"
        "v_pk_mul_f32 v[240:241], v[66:67], v[198:199]\n"
        "v_pk_fma_f32 v[242:243], v[76:77], v[196:197], v[242:243] neg_lo:[1,0,0] neg_hi:[1,0,0]\n"
        "v_pk_fma_f32 v[250:251], v[78:79], v[198:199], v[250:251] neg_lo:[1,0,0] neg_hi:[1,0,0]\n"
        "v_pk_fma_f32 v[82:83], v[238:239], v[86:87], v[226:227] neg_lo:[1,0,0] neg_hi:[1,0,0]\n"
        "v_pk_fma_f32 v[244:245], v[76:77], v[192:193], v[244:245]\n"
        "v_pk_fma_f32 v[246:247], v[68:69], v[196:197], v[246:247] neg_lo:[1,0,0] neg_hi:[1,0,0]\n"
        "v_pk_fma_f32 v[252:253], v[78:79], v[194:195], v[252:253]\n"
        "v_pk_fma_f32 v[254:255], v[70:71], v[198:199], v[254:255] neg_lo:[1,0,0] neg_hi:[1,0,0]\n"
        "v_pk_fma_f32 v[248:249], v[68:69], v[192:193], v[248:249]\n"
        "v_pk_fma_f32 v[240:241], v[70:71], v[194:195], v[240:241]\n"
        "v_cvt_pk_bf16_f32 v218, v242, v243\n"
        "buffer_load_dwordx4 a[96:99], v13, s[40:43], s84 offen offset:0 sc1\n"
        "v_cvt_pk_bf16_f32 v219, v250, v251\n"
        "v_pk_fma_f32 v[86:87], v[236:237], v[86:87], v[230:231]\n"
        "v_cvt_pk_bf16_f32 v222, v244, v245\n"
        "v_pk_fma_f32 v[246:247], v[80:81], v[160:161], v[246:247]\n"
        "v_mfma_f32_16x16x32_bf16 v[144:147], v[216:219], a[200:203], 0\n"
        "v_cvt_pk_bf16_f32 v223, v252, v253\n"
        "v_pk_fma_f32 v[254:255], v[82:83], v[162:163], v[254:255]\n"
        "v_pk_mul_f32 v[242:243], v[40:41], v[152:153]\n"
        "v_mfma_f32_16x16x32_bf16 v[148:151], v[220:223], a[200:203], 0\n"
        "v_mfma_f32_16x16x32_bf16 v[144:147], v[220:223], a[216:219], v[144:147]\n"
        "buffer_load_dwordx4 a[100:103], v13, s[40:43], s84 offen offset:1024 sc1\n"
        "v_mfma_f32_16x16x32_bf16 v[148:151], v[216:219], a[208:211], v[148:151]\n"
        "v_pk_mul_f32 v[250:251], v[42:43], v[154:155]\n"
        "v_pk_fma_f32 v[248:249], v[80:81], v[164:165], v[248:249]\n"
        "v_pk_fma_f32 v[240:241], v[82:83], v[166:167], v[240:241]\n"
        "v_pk_mul_f32 v[244:245], v[40:41], v[156:157]\n"
        "v_pk_mul_f32 v[252:253], v[42:43], v[158:159]\n"
        "v_pk_fma_f32 v[246:247], v[84:85], v[164:165], v[246:247] neg_lo:[1,0,0] neg_hi:[1,0,0]\n"
        "v_pk_fma_f32 v[254:255], v[86:87], v[166:167], v[254:255] neg_lo:[1,0,0] neg_hi:[1,0,0]\n"
        "v_mfma_f32_16x16x32_bf16 v[164:167], v[220:223], a[204:207], 0\n"
        "v_cvt_pk_bf16_f32 v226, v246, v247\n"
        "buffer_load_dwordx4 a[104:107], v13, s[40:43], s84 offen offset:2048 sc1\n"
        "v_mfma_f32_16x16x32_bf16 v[164:167], v[216:219], a[212:215], v[164:167]\n"
        "v_cvt_pk_bf16_f32 v227, v254, v255\n"
        "v_pk_fma_f32 v[242:243], v[44:45], v[156:157], v[242:243] neg_lo:[1,0,0] neg_hi:[1,0,0]\n"
        "v_pk_fma_f32 v[250:251], v[46:47], v[158:159], v[250:251] neg_lo:[1,0,0] neg_hi:[1,0,0]\n"
        "v_mfma_f32_16x16x32_bf16 v[176:179], v[224:227], a[200:203], 0\n"
        "v_mfma_f32_16x16x32_bf16 v[192:195], v[224:227], a[204:207], 0\n"
        "buffer_load_dwordx4 a[108:111], v13, s[40:43], s84 offen offset:3072 sc1\n"
        "v_pk_fma_f32 v[248:249], v[84:85], v[160:161], v[248:249]\n"
        "v_pk_fma_f32 v[240:241], v[86:87], v[162:163], v[240:241]\n"
        "v_mfma_f32_16x16x32_bf16 v[160:163], v[216:219], a[204:207], 0\n"
        "v_cvt_pk_bf16_f32 v230, v248, v249\n"
        "v_cvt_pk_bf16_f32 v231, v240, v241\n"
        "v_mfma_f32_16x16x32_bf16 v[160:163], v[220:223], a[220:223], v[160:163]\n"
        "s_nop 0\n"
        "v_mfma_f32_16x16x32_bf16 v[180:183], v[228:231], a[200:203], 0\n"
        "v_mfma_f32_16x16x32_bf16 v[176:179], v[228:231], a[216:219], v[176:179]\n"
        "buffer_load_dwordx4 a[112:115], v14, s[40:43], s84 offen offset:0 sc1\n"
        "v_mfma_f32_16x16x32_bf16 v[180:183], v[224:227], a[208:211], v[180:183]\n"
        "v_mfma_f32_16x16x32_bf16 v[196:199], v[228:231], a[204:207], 0\n"
        "v_mfma_f32_16x16x32_bf16 v[192:195], v[228:231], a[220:223], v[192:195]\n"
        "v_mfma_f32_16x16x32_bf16 v[196:199], v[224:227], a[212:215], v[196:199]\n"
        "buffer_load_dwordx4 a[116:119], v14, s[40:43], s84 offen offset:1024 sc1\n"
        "s_add_u32 s84, s67, 40960\n"
        "s_add_u32 s72, s72, 114688\n"
        "v_pk_fma_f32 v[244:245], v[44:45], v[152:153], v[244:245]\n"
        "v_pk_fma_f32 v[252:253], v[46:47], v[154:155], v[252:253]\n"
        "v_pk_fma_f32 v[242:243], v[48:49], v[184:185], v[242:243]\n"
        "v_pk_fma_f32 v[250:251], v[50:51], v[186:187], v[250:251]\n"
        "v_pk_fma_f32 v[244:245], v[48:49], v[188:189], v[244:245]\n"
        "v_pk_mul_f32 v[246:247], v[40:41], v[184:185]\n"
        "v_pk_fma_f32 v[252:253], v[50:51], v[190:191], v[252:253]\n"
        "v_pk_mul_f32 v[254:255], v[42:43], v[186:187]\n"
        "v_pk_mul_f32 v[248:249], v[40:41], v[188:189]\n"
        "v_pk_mul_f32 v[240:241], v[42:43], v[190:191]\n"
        "v_pk_fma_f32 v[242:243], v[52:53], v[188:189], v[242:243] neg_lo:[1,0,0] neg_hi:[1,0,0]\n"
        "v_pk_fma_f32 v[250:251], v[54:55], v[190:191], v[250:251] neg_lo:[1,0,0] neg_hi:[1,0,0]\n"
        "buffer_load_dwordx4 a[120:123], v12, s[40:43], s85 offen offset:0 sc1\n"
        "v_pk_fma_f32 v[244:245], v[52:53], v[184:185], v[244:245]\n"
        "v_pk_fma_f32 v[246:247], v[44:45], v[188:189], v[246:247] neg_lo:[1,0,0] neg_hi:[1,0,0]\n"
        "v_pk_fma_f32 v[252:253], v[54:55], v[186:187], v[252:253]\n"
        "v_pk_fma_f32 v[254:255], v[46:47], v[190:191], v[254:255] neg_lo:[1,0,0] neg_hi:[1,0,0]\n"
        "v_pk_fma_f32 v[248:249], v[44:45], v[184:185], v[248:249]\n"
        "v_pk_fma_f32 v[240:241], v[46:47], v[186:187], v[240:241]\n"
        "v_cvt_pk_bf16_f32 v232, v242, v243\n"
        "v_cvt_pk_bf16_f32 v233, v250, v251\n"
        "v_cvt_pk_bf16_f32 v236, v244, v245\n"
        "v_pk_fma_f32 v[246:247], v[56:57], v[152:153], v[246:247]\n"
        "v_cvt_pk_bf16_f32 v237, v252, v253\n"
        "v_pk_fma_f32 v[254:255], v[58:59], v[154:155], v[254:255]\n"
        "v_pk_mul_f32 v[242:243], v[88:89], v[168:169]\n"
        "v_pk_mul_f32 v[250:251], v[90:91], v[170:171]\n"
        "buffer_load_dwordx4 a[124:127], v12, s[40:43], s85 offen offset:1024 sc1\n"
        "v_pk_fma_f32 v[248:249], v[56:57], v[156:157], v[248:249]\n"
        "v_pk_fma_f32 v[240:241], v[58:59], v[158:159], v[240:241]\n"
        "v_pk_mul_f32 v[244:245], v[88:89], v[172:173]\n"
        "v_pk_mul_f32 v[252:253], v[90:91], v[174:175]\n"
        "v_pk_fma_f32 v[246:247], v[60:61], v[156:157], v[246:247] neg_lo:[1,0,0] neg_hi:[1,0,0]\n"
        "v_pk_fma_f32 v[254:255], v[62:63], v[158:159], v[254:255] neg_lo:[1,0,0] neg_hi:[1,0,0]\n"
        "v_pk_fma_f32 v[242:243], v[92:93], v[172:173], v[242:243] neg_lo:[1,0,0] neg_hi:[1,0,0]\n"
        "v_pk_fma_f32 v[250:251], v[94:95], v[174:175], v[250:251] neg_lo:[1,0,0] neg_hi:[1,0,0]\n"
        "v_pk_fma_f32 v[248:249], v[60:61], v[152:153], v[248:249]\n"
        "v_pk_fma_f32 v[240:241], v[62:63], v[154:155], v[240:241]\n"
        "v_pk_fma_f32 v[244:245], v[92:93], v[168:169], v[244:245]\n"
        "v_pk_fma_f32 v[252:253], v[94:95], v[170:171], v[252:253]\n"
        "v_cvt_pk_bf16_f32 v208, v246, v247\n"
        "v_cvt_pk_bf16_f32 v209, v254, v255\n"
        "buffer_load_dwordx4 a[128:131], v12, s[40:43], s85 offen offset:2048 sc1\n"
        "v_pk_fma_f32 v[242:243], v[96:97], v[200:201], v[242:243]\n"
        "v_pk_fma_f32 v[250:251], v[98:99], v[202:203], v[250:251]\n"
        "v_cvt_pk_bf16_f32 v212, v248, v249\n"
        "v_cvt_pk_bf16_f32 v213, v240, v241\n"
        "v_pk_fma_f32 v[244:245], v[96:97], v[204:205], v[244:245]\n"
        "v_pk_mul_f32 v[246:247], v[88:89], v[200:201]\n"
        "v_pk_fma_f32 v[252:253], v[98:99], v[206:207], v[252:253]\n"
        "v_pk_mul_f32 v[254:255], v[90:91], v[202:203]\n"
        "v_pk_mul_f32 v[248:249], v[88:89], v[204:205]\n"
        "v_pk_mul_f32 v[240:241], v[90:91], v[206:207]\n"
        "v_pk_fma_f32 v[242:243], v[100:101], v[204:205], v[242:243] neg_lo:[1,0,0] neg_hi:[1,0,0]\n"
        "v_pk_fma_f32 v[250:251], v[102:103], v[206:207], v[250:251] neg_lo:[1,0,0] neg_hi:[1,0,0]\n"
        "v_pk_fma_f32 v[244:245], v[100:101], v[200:201], v[244:245]\n"
        "v_pk_fma_f32 v[246:247], v[92:93], v[204:205], v[246:247] neg_lo:[1,0,0] neg_hi:[1,0,0]\n"
        "buffer_load_dwordx4 a[132:135], v12, s[40:43], s85 offen offset:3072 sc1\n"
        "v_pk_fma_f32 v[252:253], v[102:103], v[202:203], v[252:253]\n"
        "v_pk_fma_f32 v[254:255], v[94:95], v[206:207], v[254:255] neg_lo:[1,0,0] neg_hi:[1,0,0]\n"
        "v_pk_fma_f32 v[248:249], v[92:93], v[200:201], v[248:249]\n"
        "v_pk_fma_f32 v[240:241], v[94:95], v[202:203], v[240:241]\n"
        "v_cvt_pk_bf16_f32 v234, v242, v243\n"
        "v_cvt_pk_bf16_f32 v235, v250, v251\n"
        "v_cvt_pk_bf16_f32 v238, v244, v245\n"
        "v_pk_fma_f32 v[246:247], v[104:105], v[168:169], v[246:247]\n"
        "v_cvt_pk_bf16_f32 v239, v252, v253\n"
        "v_mfma_f32_16x16x32_bf16 v[152:155], v[232:235], a[200:203], 0\n"
        "v_min_u32 v60, 4, v0\n"
        "v_lshl_add_u32 v60, v60, 7, s48\n"
        "v_mfma_f32_16x16x32_bf16 v[156:159], v[236:239], a[200:203], 0\n"
        "buffer_load_dwordx4 a[136:139], v13, s[40:43], s85 offen offset:0 sc1\n"
        "v_mfma_f32_16x16x32_bf16 v[152:155], v[236:239], a[216:219], v[152:155]\n"
        "v_mfma_f32_16x16x32_bf16 v[156:159], v[232:235], a[208:211], v[156:159]\n"
        "v_pk_fma_f32 v[254:255], v[106:107], v[170:171], v[254:255]\n"
        "v_pk_fma_f32 v[248:249], v[104:105], v[172:173], v[248:249]\n"
        "v_pk_fma_f32 v[240:241], v[106:107], v[174:175], v[240:241]\n"
        "v_pk_fma_f32 v[246:247], v[108:109], v[172:173], v[246:247] neg_lo:[1,0,0] neg_hi:[1,0,0]\n"
        "v_pk_fma_f32 v[254:255], v[110:111], v[174:175], v[254:255] neg_lo:[1,0,0] neg_hi:[1,0,0]\n"
        "v_mfma_f32_16x16x32_bf16 v[172:175], v[236:239], a[204:207], 0\n"
        "buffer_load_dwordx4 a[140:143], v13, s[40:43], s85 offen offset:1024 sc1\n"
        "v_mfma_f32_16x16x32_bf16 v[172:175], v[232:235], a[212:215], v[172:175]\n"
        "v_cvt_pk_bf16_f32 v210, v246, v247\n"
        "v_cvt_pk_bf16_f32 v211, v254, v255\n"
        "v_pk_fma_f32 v[248:249], v[108:109], v[168:169], v[248:249]\n"
        "v_pk_fma_f32 v[240:241], v[110:111], v[170:171], v[240:241]\n"
        "v_mfma_f32_16x16x32_bf16 v[168:171], v[232:235], a[204:207], 0\n"
        "v_cvt_pk_bf16_f32 v214, v248, v249\n"
        "v_cvt_pk_bf16_f32 v215, v240, v241\n"
        "v_mfma_f32_16x16x32_bf16 v[184:187], v[208:211], a[200:203], 0\n"
        "v_add_u32 v104, s72, v3\n"
        "ds_read_b128 v[64:67], v104 offset:0\n"
        "v_mfma_f32_16x16x32_bf16 v[188:191], v[212:215], a[200:203], 0\n"
        "buffer_load_dwordx4 a[144:147], v13, s[40:43], s85 offen offset:2048 sc1\n"
        "v_mfma_f32_16x16x32_bf16 v[184:187], v[212:215], a[216:219], v[184:187]\n"
        "ds_read_b128 v[68:71], v104 offset:1024\n"
        "ds_read_b128 v[72:75], v104 offset:2048\n"
        "v_mfma_f32_16x16x32_bf16 v[188:191], v[208:211], a[208:211], v[188:191]\n"
        "ds_read_b128 v[76:79], v104 offset:3072\n"
        "ds_read_b128 v[80:83], v104 offset:4096\n"
        "v_mfma_f32_16x16x32_bf16 v[200:203], v[208:211], a[204:207], 0\n"
        "ds_read_b128 v[84:87], v104 offset:5120\n"
        "ds_read_b128 v[88:91], v104 offset:6144\n"
        "v_mfma_f32_16x16x32_bf16 v[204:207], v[212:215], a[204:207], 0\n"
        "buffer_load_dwordx4 a[148:151], v13, s[40:43], s85 offen offset:3072 sc1\n"
        "v_mfma_f32_16x16x32_bf16 v[168:171], v[236:239], a[220:223], v[168:171]\n"
        "ds_read_b128 v[92:95], v104 offset:7168\n"
        "ds_read_b128 v[96:99], v104 offset:8192\n"
        "v_mfma_f32_16x16x32_bf16 v[200:203], v[212:215], a[220:223], v[200:203]\n"
        "ds_read_b128 v[100:103], v104 offset:9216\n"
        "v_mfma_f32_16x16x32_bf16 v[204:207], v[208:211], a[212:215], v[204:207]\n"
        "v_pk_mul_f32 v[242:243], v[144:145], v[112:113]\n"
        "v_pk_mul_f32 v[246:247], v[146:147], v[114:115]\n"
        "buffer_load_dwordx4 a[152:155], v14, s[40:43], s85 offen offset:0 sc1\n"
        "v_pk_mul_f32 v[250:251], v[152:153], v[128:129]\n"
        "v_pk_mul_f32 v[254:255], v[154:155], v[130:131]\n"
        "v_pk_mul_f32 v[244:245], v[148:149], v[112:113]\n"
        "v_pk_mul_f32 v[248:249], v[150:151], v[114:115]\n"
        "v_pk_mul_f32 v[252:253], v[156:157], v[128:129]\n"
        "v_pk_mul_f32 v[240:241], v[158:159], v[130:131]\n"
        "v_pk_fma_f32 v[242:243], v[148:149], v[116:117], v[242:243] neg_lo:[1,0,0] neg_hi:[1,0,0]\n"
        "v_pk_fma_f32 v[246:247], v[150:151], v[118:119], v[246:247] neg_lo:[1,0,0] neg_hi:[1,0,0]\n"
        "v_pk_fma_f32 v[250:251], v[156:157], v[132:133], v[250:251] neg_lo:[1,0,0] neg_hi:[1,0,0]\n"
        "v_pk_fma_f32 v[254:255], v[158:159], v[134:135], v[254:255] neg_lo:[1,0,0] neg_hi:[1,0,0]\n"
        "v_pk_fma_f32 v[244:245], v[144:145], v[116:117], v[244:245]\n"
        "v_pk_fma_f32 v[248:249], v[146:147], v[118:119], v[248:249]\n"
        "v_pk_fma_f32 v[252:253], v[152:153], v[132:133], v[252:253]\n"
        "v_pk_fma_f32 v[240:241], v[154:155], v[134:135], v[240:241]\n"
        "buffer_load_dwordx4 a[156:159], v14, s[40:43], s85 offen offset:1024 sc1\n"
        "v_cvt_pk_bf16_f32 v216, v242, v243\n"
        "v_cvt_pk_bf16_f32 v217, v246, v247\n"
        "v_cvt_pk_bf16_f32 v218, v250, v251\n"
        "v_cvt_pk_bf16_f32 v219, v254, v255\n"
        "v_cvt_pk_bf16_f32 v220, v244, v245\n"
        "v_cvt_pk_bf16_f32 v221, v248, v249\n"
        "v_cvt_pk_bf16_f32 v222, v252, v253\n"
        "v_mfma_f32_16x16x32_bf16 v[144:147], v[216:219], a[200:203], 0\n"
        "v_cvt_pk_bf16_f32 v223, v240, v241\n"
        "v_mfma_f32_16x16x32_bf16 v[152:155], v[216:219], a[204:207], 0\n"
        "buffer_load_dwordx4 a[160:163], v12, s[40:43], s84 offen offset:0 sc1\n"
        "v_mfma_f32_16x16x32_bf16 v[148:151], v[220:223], a[200:203], 0\n"
        "v_mfma_f32_16x16x32_bf16 v[156:159], v[220:223], a[204:207], 0\n"
        "v_mfma_f32_16x16x32_bf16 v[144:147], v[220:223], a[216:219], v[144:147]\n"
        "v_mfma_f32_16x16x32_bf16 v[148:151], v[216:219], a[208:211], v[148:151]\n"
        "buffer_load_dwordx4 a[164:167], v12, s[40:43], s84 offen offset:1024 sc1\n"
        "v_mfma_f32_16x16x32_bf16 v[152:155], v[220:223], a[220:223], v[152:155]\n"
        "v_mfma_f32_16x16x32_bf16 v[156:159], v[216:219], a[212:215], v[156:159]\n"
        "v_pk_mul_f32 v[242:243], v[176:177], v[112:113]\n"
        "v_pk_mul_f32 v[246:247], v[178:179], v[114:115]\n"
        "v_pk_mul_f32 v[250:251], v[184:185], v[128:129]\n"
        "v_pk_mul_f32 v[254:255], v[186:187], v[130:131]\n"
        "v_pk_mul_f32 v[244:245], v[180:181], v[112:113]\n"
        "v_pk_mul_f32 v[248:249], v[182:183], v[114:115]\n"
        "buffer_load_dwordx4 a[168:171], v12, s[40:43], s84 offen offset:2048 sc1\n"
        "v_pk_mul_f32 v[252:253], v[188:189], v[128:129]\n"
        "v_pk_mul_f32 v[240:241], v[190:191], v[130:131]\n"
        "v_pk_fma_f32 v[242:243], v[180:181], v[116:117], v[242:243] neg_lo:[1,0,0] neg_hi:[1,0,0]\n"
        "v_pk_fma_f32 v[246:247], v[182:183], v[118:119], v[246:247] neg_lo:[1,0,0] neg_hi:[1,0,0]\n"
        "v_pk_fma_f32 v[250:251], v[188:189], v[132:133], v[250:251] neg_lo:[1,0,0] neg_hi:[1,0,0]\n"
        "v_pk_fma_f32 v[254:255], v[190:191], v[134:135], v[254:255] neg_lo:[1,0,0] neg_hi:[1,0,0]\n"
        "v_pk_fma_f32 v[244:245], v[176:177], v[116:117], v[244:245]\n"
        "v_pk_fma_f32 v[248:249], v[178:179], v[118:119], v[248:249]\n"
        "v_pk_fma_f32 v[252:253], v[184:185], v[132:133], v[252:253]\n"
        "v_pk_fma_f32 v[240:241], v[186:187], v[134:135], v[240:241]\n"
        "v_cvt_pk_bf16_f32 v224, v242, v243\n"
        "v_cvt_pk_bf16_f32 v225, v246, v247\n"
        "v_cvt_pk_bf16_f32 v226, v250, v251\n"
        "v_cvt_pk_bf16_f32 v227, v254, v255\n"
        "buffer_load_dwordx4 a[172:175], v12, s[40:43], s84 offen offset:3072 sc1\n"
        "s_nop 0\n"
        "v_mfma_f32_16x16x32_bf16 v[176:179], v[224:227], a[200:203], 0\n"
        "s_add_u32 s84, s67, 40960\n"
        "v_cvt_pk_bf16_f32 v228, v244, v245\n"
        "v_mfma_f32_16x16x32_bf16 v[184:187], v[224:227], a[204:207], 0\n"
        "v_cvt_pk_bf16_f32 v229, v248, v249\n"
        "v_cvt_pk_bf16_f32 v230, v252, v253\n"
        "v_cvt_pk_bf16_f32 v231, v240, v241\n"
        "v_pk_mul_f32 v[242:243], v[160:161], v[120:121]\n"
        "v_pk_mul_f32 v[246:247], v[162:163], v[122:123]\n"
        "v_pk_mul_f32 v[250:251], v[168:169], v[136:137]\n"
        "v_mfma_f32_16x16x32_bf16 v[180:183], v[228:231], a[200:203], 0\n"
        "buffer_load_dwordx4 v[16:19], v13, s[40:43], s84 offen offset:0 sc1\n"
        "v_mfma_f32_16x16x32_bf16 v[188:191], v[228:231], a[204:207], 0\n"
        "v_mfma_f32_16x16x32_bf16 v[176:179], v[228:231], a[216:219], v[176:179]\n"
        "v_mfma_f32_16x16x32_bf16 v[180:183], v[224:227], a[208:211], v[180:183]\n"
        "v_mfma_f32_16x16x32_bf16 v[184:187], v[228:231], a[220:223], v[184:187]\n"
        "buffer_load_dwordx4 v[20:23], v13, s[40:43], s84 offen offset:1024 sc1\n"
        "v_mfma_f32_16x16x32_bf16 v[188:191], v[224:227], a[212:215], v[188:191]\n"
        "s_nop 3\n"
        "v_cvt_pk_bf16_f32 v48, v144, v176\n"
        "v_cvt_pk_bf16_f32 v49, v145, v177\n"
        "v_pk_mul_f32 v[254:255], v[170:171], v[138:139]\n"
        "v_pk_mul_f32 v[244:245], v[164:165], v[120:121]\n"
        "v_pk_mul_f32 v[248:249], v[166:167], v[122:123]\n"
        "v_pk_mul_f32 v[252:253], v[172:173], v[136:137]\n"
        "v_pk_mul_f32 v[240:241], v[174:175], v[138:139]\n"
        "v_pk_fma_f32 v[242:243], v[164:165], v[124:125], v[242:243] neg_lo:[1,0,0] neg_hi:[1,0,0]\n"
        "v_pk_fma_f32 v[246:247], v[166:167], v[126:127], v[246:247] neg_lo:[1,0,0] neg_hi:[1,0,0]\n"
        "v_pk_fma_f32 v[250:251], v[172:173], v[140:141], v[250:251] neg_lo:[1,0,0] neg_hi:[1,0,0]\n"
        "v_pk_fma_f32 v[254:255], v[174:175], v[142:143], v[254:255] neg_lo:[1,0,0] neg_hi:[1,0,0]\n"
        "v_pk_fma_f32 v[244:245], v[160:161], v[124:125], v[244:245]\n"
        "buffer_load_dwordx4 v[24:27], v13, s[40:43], s84 offen offset:2048 sc1\n"
        "v_pk_fma_f32 v[248:249], v[162:163], v[126:127], v[248:249]\n"
        "v_pk_fma_f32 v[252:253], v[168:169], v[140:141], v[252:253]\n"
        "v_pk_fma_f32 v[240:241], v[170:171], v[142:143], v[240:241]\n"
        "v_cvt_pk_bf16_f32 v232, v242, v243\n"
        "v_cvt_pk_bf16_f32 v233, v246, v247\n"
        "v_cvt_pk_bf16_f32 v234, v250, v251\n"
        "v_cvt_pk_bf16_f32 v235, v254, v255\n"
        "v_cvt_pk_bf16_f32 v236, v244, v245\n"
        "v_cvt_pk_bf16_f32 v237, v248, v249\n"
        "v_cvt_pk_bf16_f32 v238, v252, v253\n"
        "v_mfma_f32_16x16x32_bf16 v[160:163], v[232:235], a[200:203], 0\n"
        "v_cvt_pk_bf16_f32 v239, v240, v241\n"
        "v_cvt_pk_bf16_f32 v50, v146, v178\n"
        "v_mfma_f32_16x16x32_bf16 v[168:171], v[232:235], a[204:207], 0\n"
        "buffer_load_dwordx4 v[28:31], v13, s[40:43], s84 offen offset:3072 sc1\n"
        "v_mfma_f32_16x16x32_bf16 v[164:167], v[236:239], a[200:203], 0\n"
        "v_cvt_pk_bf16_f32 v51, v147, v179\n"
        "v_cvt_pk_bf16_f32 v52, v148, v180\n"
        "v_mfma_f32_16x16x32_bf16 v[172:175], v[236:239], a[204:207], 0\n"
        "v_cvt_pk_bf16_f32 v53, v149, v181\n"
        "v_cvt_pk_bf16_f32 v54, v150, v182\n"
        "v_mfma_f32_16x16x32_bf16 v[160:163], v[236:239], a[216:219], v[160:163]\n"
        "v_cvt_pk_bf16_f32 v55, v151, v183\n"
        "v_cvt_pk_bf16_f32 v104, v152, v184\n"
        "v_mfma_f32_16x16x32_bf16 v[164:167], v[232:235], a[208:211], v[164:167]\n"
        "buffer_load_dwordx4 v[32:35], v14, s[40:43], s84 offen offset:0 sc1\n"
        "v_mfma_f32_16x16x32_bf16 v[168:171], v[236:239], a[220:223], v[168:171]\n"
        "v_cvt_pk_bf16_f32 v105, v153, v185\n"
        "v_cvt_pk_bf16_f32 v106, v154, v186\n"
        "v_mfma_f32_16x16x32_bf16 v[172:175], v[232:235], a[212:215], v[172:175]\n"
        "v_cvt_pk_bf16_f32 v107, v155, v187\n"
        "v_cvt_pk_bf16_f32 v108, v156, v188\n"
        "v_pk_mul_f32 v[242:243], v[192:193], v[120:121]\n"
        "v_pk_mul_f32 v[246:247], v[194:195], v[122:123]\n"
        "v_pk_mul_f32 v[250:251], v[200:201], v[136:137]\n"
        "v_pk_mul_f32 v[254:255], v[202:203], v[138:139]\n"
        "v_pk_mul_f32 v[244:245], v[196:197], v[120:121]\n"
        "v_pk_mul_f32 v[248:249], v[198:199], v[122:123]\n"
        "v_pk_mul_f32 v[252:253], v[204:205], v[136:137]\n"
        "v_pk_mul_f32 v[240:241], v[206:207], v[138:139]\n"
        "v_pk_fma_f32 v[242:243], v[196:197], v[124:125], v[242:243] neg_lo:[1,0,0] neg_hi:[1,0,0]\n"
        "v_pk_fma_f32 v[246:247], v[198:199], v[126:127], v[246:247] neg_lo:[1,0,0] neg_hi:[1,0,0]\n"
        "v_pk_fma_f32 v[250:251], v[204:205], v[140:141], v[250:251] neg_lo:[1,0,0] neg_hi:[1,0,0]\n"
        "v_pk_fma_f32 v[254:255], v[206:207], v[142:143], v[254:255] neg_lo:[1,0,0] neg_hi:[1,0,0]\n"
        "v_pk_fma_f32 v[244:245], v[192:193], v[124:125], v[244:245]\n"
        "v_pk_fma_f32 v[248:249], v[194:195], v[126:127], v[248:249]\n"
        "v_pk_fma_f32 v[252:253], v[200:201], v[140:141], v[252:253]\n"
        "v_pk_fma_f32 v[240:241], v[202:203], v[142:143], v[240:241]\n"
        "v_cvt_pk_bf16_f32 v208, v242, v243\n"
        "v_cvt_pk_bf16_f32 v209, v246, v247\n"
        "v_cvt_pk_bf16_f32 v210, v250, v251\n"
        "v_cvt_pk_bf16_f32 v211, v254, v255\n"
        "v_cvt_pk_bf16_f32 v212, v244, v245\n"
        "v_cvt_pk_bf16_f32 v213, v248, v249\n"
        "v_cvt_pk_bf16_f32 v214, v252, v253\n"
        "v_mfma_f32_16x16x32_bf16 v[192:195], v[208:211], a[200:203], 0\n"
        "v_cvt_pk_bf16_f32 v215, v240, v241\n"
        "v_cvt_pk_bf16_f32 v109, v157, v189\n"
        "v_mfma_f32_16x16x32_bf16 v[200:203], v[208:211], a[204:207], 0\n"
        "v_cvt_pk_bf16_f32 v110, v158, v190\n"
        "v_cvt_pk_bf16_f32 v111, v159, v191\n"
        "v_mfma_f32_16x16x32_bf16 v[196:199], v[212:215], a[200:203], 0\n"
        "ds_write_b128 v5, v[48:51] offset:0\n"
        "v_mfma_f32_16x16x32_bf16 v[204:207], v[212:215], a[204:207], 0\n"
        "buffer_load_dwordx4 a[196:199], v14, s[40:43], s84 offen offset:1024 sc1\n"
        "v_mfma_f32_16x16x32_bf16 v[192:195], v[212:215], a[216:219], v[192:195]\n"
        "ds_write_b128 v5, v[52:55] offset:4160\n"
        "v_mfma_f32_16x16x32_bf16 v[196:199], v[208:211], a[208:211], v[196:199]\n"
        "ds_write_b128 v5, v[104:107] offset:512\n"
        "v_mfma_f32_16x16x32_bf16 v[200:203], v[212:215], a[220:223], v[200:203]\n"
        "s_nop 3\n"
        "v_cvt_pk_bf16_f32 v48, v160, v192\n"
        "v_cvt_pk_bf16_f32 v49, v161, v193\n"
        "v_mfma_f32_16x16x32_bf16 v[204:207], v[208:211], a[212:215], v[204:207]\n"
        "v_cvt_pk_bf16_f32 v50, v162, v194\n"
        "v_cvt_pk_bf16_f32 v51, v163, v195\n"
        "v_cvt_pk_bf16_f32 v52, v164, v196\n"
        "v_cvt_pk_bf16_f32 v53, v165, v197\n"
        "v_cvt_pk_bf16_f32 v54, v166, v198\n"
        "v_cvt_pk_bf16_f32 v55, v167, v199\n"
        "ds_write_b128 v5, v[108:111] offset:4672\n"
        "v_cvt_pk_bf16_f32 v104, v168, v200\n"
        "v_cvt_pk_bf16_f32 v105, v169, v201\n"
        "v_cvt_pk_bf16_f32 v106, v170, v202\n"
        "v_cvt_pk_bf16_f32 v107, v171, v203\n"
        "v_cvt_pk_bf16_f32 v108, v172, v204\n"
        "v_cvt_pk_bf16_f32 v109, v173, v205\n"
        "v_cvt_pk_bf16_f32 v110, v174, v206\n"
        "v_cvt_pk_bf16_f32 v111, v175, v207\n"
        "ds_write_b128 v6, v[48:51] offset:0\n"
        "ds_write_b128 v6, v[52:55] offset:4160\n"
        "ds_write_b128 v6, v[104:107] offset:512\n"
        "ds_write_b128 v6, v[108:111] offset:4672\n"
        "buffer_load_dwordx2 v[56:57], v60, s[40:43], 0 offen sc1\n"
        "s_waitcnt lgkmcnt(0)\n"
        "s_barrier\n"
        "ds_read_b64_tr_b16 v[208:209], v7 offset:0\n"
        "ds_read_b64_tr_b16 v[210:211], v7 offset:16640\n"
        "ds_read_b64_tr_b16 v[212:213], v8 offset:128\n"
        "ds_read_b64_tr_b16 v[214:215], v8 offset:16768\n"
        "ds_read_b64_tr_b16 v[216:217], v9 offset:256\n"
        "ds_read_b64_tr_b16 v[218:219], v9 offset:16896\n"
        "ds_read_b64_tr_b16 v[220:221], v10 offset:384\n"
        "ds_read_b64_tr_b16 v[222:223], v10 offset:17024\n"
        "ds_read_b64_tr_b16 v[224:225], v7 offset:512\n"
        "ds_read_b64_tr_b16 v[226:227], v7 offset:17152\n"
        "ds_read_b64_tr_b16 v[228:229], v8 offset:640\n"
        "ds_read_b64_tr_b16 v[230:231], v8 offset:17280\n"
        "ds_read_b64_tr_b16 v[232:233], v9 offset:768\n"
        "ds_read_b64_tr_b16 v[234:235], v9 offset:17408\n"
        "ds_read_b64_tr_b16 v[236:237], v10 offset:896\n"
        "ds_read_b64_tr_b16 v[238:239], v10 offset:17536\n"
        "s_waitcnt lgkmcnt(14)\n"
        "buffer_store_dwordx4 v[208:211], v11, s[40:43], s86 offen sc1\n"
        "s_add_u32 s86, s86, 86016\n"
        "s_waitcnt lgkmcnt(12)\n"
        "buffer_store_dwordx4 v[212:215], v11, s[40:43], s86 offen sc1\n"
        "s_add_u32 s86, s86, 86016\n"
        "s_waitcnt lgkmcnt(10)\n"
        "buffer_store_dwordx4 v[216:219], v11, s[40:43], s86 offen sc1\n"
        "s_add_u32 s86, s86, 86016\n"
        "s_waitcnt lgkmcnt(8)\n"
        "buffer_store_dwordx4 v[220:223], v11, s[40:43], s86 offen sc1\n"
        "s_add_u32 s86, s86, 86016\n"
        "s_waitcnt lgkmcnt(6)\n"
        "buffer_store_dwordx4 v[224:227], v11, s[40:43], s86 offen sc1\n"
        "s_add_u32 s86, s86, 86016\n"
        "s_waitcnt lgkmcnt(4)\n"
        "buffer_store_dwordx4 v[228:231], v11, s[40:43], s86 offen sc1\n"
        "s_add_u32 s86, s86, 86016\n"
        "s_waitcnt lgkmcnt(2)\n"
        "buffer_store_dwordx4 v[232:235], v11, s[40:43], s86 offen sc1\n"
        "s_add_u32 s86, s86, 86016\n"
        "s_waitcnt lgkmcnt(0)\n"
        "buffer_store_dwordx4 v[236:239], v11, s[40:43], s86 offen sc1\n"
        "v_add_u32 v10, s69, v1\n"
        "s_mul_i32 s70, s3, 80\n"
        "v_cvt_f32_u32 v10, v10\n"
        "s_add_u32 s70, s70, 64\n"
        "v_lshl_add_u32 v11, v2, 2, s70\n"
        "v_add_f32 v178, 0x00000000, v10\n"
        "v_cvt_f32_u32 v11, v11\n"
        "v_mul_f32 v178, 0x3acccccd, v178\n"
        "v_add_f32 v186, 0x41800000, v10\n"
        "v_cos_f32 v156, v178\n"
        "v_sin_f32 v157, v178\n"
        "v_mul_f32 v179, -v11, v178\n"
        "v_add_f32 v178, 0x42000000, v10\n"
        "v_fract_f32 v179, v179\n"
        "v_mul_f32 v186, 0x3acccccd, v186\n"
        "v_mul_f32 v178, 0x3acccccd, v178\n"
        "v_cos_f32 v116, v179\n"
        "v_sin_f32 v117, v179\n"
        "v_cos_f32 v158, v186\n"
        "v_sin_f32 v159, v186\n"
        "v_mul_f32 v187, -v11, v186\n"
        "v_cos_f32 v160, v178\n"
        "v_sin_f32 v161, v178\n"
        "v_mul_f32 v179, -v11, v178\n"
        "v_add_f32 v186, 0x42400000, v10\n"
        "v_add_f32 v178, 0x42800000, v10\n"
        "v_fract_f32 v187, v187\n"
        "v_fract_f32 v179, v179\n"
        "v_mul_f32 v186, 0x3acccccd, v186\n"
        "v_mul_f32 v178, 0x3acccccd, v178\n"
        "v_cos_f32 v124, v187\n"
        "v_sin_f32 v125, v187\n"
        "v_cos_f32 v132, v179\n"
        "v_sin_f32 v133, v179\n"
        "v_mul_f32 v187, -v11, v186\n"
        "v_mul_f32 v179, -v11, v178\n"
        "v_fract_f32 v187, v187\n"
        "v_fract_f32 v179, v179\n"
        "v_pk_mul_f32 v[180:181], v[116:117], v[156:157] op_sel_hi:[1,0]\n"
        "v_cos_f32 v162, v186\n"
        "v_sin_f32 v163, v186\n"
        "v_cos_f32 v140, v187\n"
        "v_sin_f32 v141, v187\n"
        "v_cos_f32 v164, v178\n"
        "v_sin_f32 v165, v178\n"
        "v_cos_f32 v148, v179\n"
        "v_sin_f32 v149, v179\n"
        "v_pk_fma_f32 v[118:119], v[116:117], v[156:157], v[180:181] op_sel:[1,1,0] op_sel_hi:[0,1,1] neg_hi:[0,1,0]\n"
        "v_pk_mul_f32 v[188:189], v[124:125], v[158:159] op_sel_hi:[1,0]\n"
        "v_pk_mul_f32 v[180:181], v[132:133], v[160:161] op_sel_hi:[1,0]\n"
        "v_pk_mul_f32 v[182:183], v[118:119], v[156:157] op_sel_hi:[1,0]\n"
        "v_pk_fma_f32 v[126:127], v[124:125], v[158:159], v[188:189] op_sel:[1,1,0] op_sel_hi:[0,1,1] neg_hi:[0,1,0]\n"
        "v_pk_fma_f32 v[134:135], v[132:133], v[160:161], v[180:181] op_sel:[1,1,0] op_sel_hi:[0,1,1] neg_hi:[0,1,0]\n"
        "v_pk_mul_f32 v[188:189], v[140:141], v[162:163] op_sel_hi:[1,0]\n"
        "v_pk_mul_f32 v[180:181], v[148:149], v[164:165] op_sel_hi:[1,0]\n"
        "v_pk_fma_f32 v[120:121], v[118:119], v[156:157], v[182:183] op_sel:[1,1,0] op_sel_hi:[0,1,1] neg_hi:[0,1,0]\n"
        "v_pk_mul_f32 v[190:191], v[126:127], v[158:159] op_sel_hi:[1,0]\n"
        "v_pk_mul_f32 v[182:183], v[134:135], v[160:161] op_sel_hi:[1,0]\n"
        "v_pk_fma_f32 v[142:143], v[140:141], v[162:163], v[188:189] op_sel:[1,1,0] op_sel_hi:[0,1,1] neg_hi:[0,1,0]\n"
        "v_pk_fma_f32 v[150:151], v[148:149], v[164:165], v[180:181] op_sel:[1,1,0] op_sel_hi:[0,1,1] neg_hi:[0,1,0]\n"
        "v_pk_mul_f32 v[184:185], v[120:121], v[156:157] op_sel_hi:[1,0]\n"
        "v_pk_fma_f32 v[128:129], v[126:127], v[158:159], v[190:191] op_sel:[1,1,0] op_sel_hi:[0,1,1] neg_hi:[0,1,0]\n"
        "v_pk_fma_f32 v[136:137], v[134:135], v[160:161], v[182:183] op_sel:[1,1,0] op_sel_hi:[0,1,1] neg_hi:[0,1,0]\n"
        "v_pk_mul_f32 v[190:191], v[142:143], v[162:163] op_sel_hi:[1,0]\n"
        "v_pk_mul_f32 v[182:183], v[150:151], v[164:165] op_sel_hi:[1,0]\n"
        "v_pk_fma_f32 v[122:123], v[120:121], v[156:157], v[184:185] op_sel:[1,1,0] op_sel_hi:[0,1,1] neg_hi:[0,1,0]\n"
        "v_pk_mul_f32 v[176:177], v[128:129], v[158:159] op_sel_hi:[1,0]\n"
        "v_pk_mul_f32 v[184:185], v[136:137], v[160:161] op_sel_hi:[1,0]\n"
        "v_pk_fma_f32 v[144:145], v[142:143], v[162:163], v[190:191] op_sel:[1,1,0] op_sel_hi:[0,1,1] neg_hi:[0,1,0]\n"
        "v_pk_fma_f32 v[152:153], v[150:151], v[164:165], v[182:183] op_sel:[1,1,0] op_sel_hi:[0,1,1] neg_hi:[0,1,0]\n"
        "v_pk_fma_f32 v[130:131], v[128:129], v[158:159], v[176:177] op_sel:[1,1,0] op_sel_hi:[0,1,1] neg_hi:[0,1,0]\n"
        "v_pk_fma_f32 v[138:139], v[136:137], v[160:161], v[184:185] op_sel:[1,1,0] op_sel_hi:[0,1,1] neg_hi:[0,1,0]\n"
        "v_pk_mul_f32 v[176:177], v[144:145], v[162:163] op_sel_hi:[1,0]\n"
        "v_pk_mul_f32 v[184:185], v[152:153], v[164:165] op_sel_hi:[1,0]\n"
        "v_pk_fma_f32 v[146:147], v[144:145], v[162:163], v[176:177] op_sel:[1,1,0] op_sel_hi:[0,1,1] neg_hi:[0,1,0]\n"
        "v_pk_fma_f32 v[154:155], v[152:153], v[164:165], v[184:185] op_sel:[1,1,0] op_sel_hi:[0,1,1] neg_hi:[0,1,0]\n"
        "s_waitcnt vmcnt(8)\n"
        "v_cmp_ne_u64 vcc, s[64:65], v[56:57]\n"
        "s_cmp_eq_u64 vcc, 0\n"
        "s_cselect_b32 s7, 0, 0x40000000\n"
        "v_add_u32 v240, s7, v12\n"
        "v_add_u32 v244, 0, v0\n"
        "buffer_load_dwordx4 a[200:203], v240, s[40:43], s16 offen offset:0 sc1\n"
        "v_mul_u32_u24 v236, 205, v244\n"
        "s_movk_i32 s70, 2560\n"
        "v_lshrrev_b32 v236, 12, v236\n"
        "s_movk_i32 s71, 336\n"
        "v_mul_u32_u24 v237, 20, v236\n"
        "buffer_load_dwordx4 a[204:207], v240, s[40:43], s16 offen offset:1024 sc1\n"
        "v_sub_u32 v244, v244, v237\n"
        "v_add_u32 v245, 64, v0\n"
        "v_lshlrev_b32 v244, 4, v244\n"
        "v_add_u32 v246, 128, v0\n"
        "v_mad_u32_u24 v249, v236, s70, v244\n"
        "buffer_load_dwordx4 a[208:211], v240, s[40:43], s16 offen offset:2048 sc1\n"
        "v_mad_u32_u24 v244, v236, s71, v244\n"
        "v_mul_u32_u24 v238, 205, v245\n"
        "v_mul_u32_u24 v236, 205, v246\n"
        "v_lshrrev_b32 v238, 12, v238\n"
        "v_lshrrev_b32 v236, 12, v236\n"
        "v_mul_u32_u24 v239, 20, v238\n"
        "v_mul_u32_u24 v237, 20, v236\n"
        "v_sub_u32 v245, v245, v239\n"
        "v_sub_u32 v246, v246, v237\n"
        "buffer_load_dwordx4 a[212:215], v240, s[40:43], s16 offen offset:3072 sc1\n"
        "v_lshlrev_b32 v245, 4, v245\n"
        "v_lshlrev_b32 v246, 4, v246\n"
        "v_add_u32 v247, 192, v0\n"
        "v_add_u32 v248, 256, v0\n"
        "v_mad_u32_u24 v250, v238, s70, v245\n"
        "v_mad_u32_u24 v251, v236, s70, v246\n"
        "v_mad_u32_u24 v245, v238, s71, v245\n"
        "v_mad_u32_u24 v246, v236, s71, v246\n"
        "v_mul_u32_u24 v238, 205, v247\n"
        "v_mul_u32_u24 v236, 205, v248\n"
        "v_lshrrev_b32 v238, 12, v238\n"
        "v_lshrrev_b32 v236, 12, v236\n"
        "v_mul_u32_u24 v239, 20, v238\n"
        "v_mul_u32_u24 v237, 20, v236\n"
        "v_sub_u32 v247, v247, v239\n"
        "v_sub_u32 v248, v248, v237\n"
        "v_min_u32 v6, 4, v0\n"
        "v_lshlrev_b32 v247, 4, v247\n"
        "v_lshlrev_b32 v248, 4, v248\n"
        "v_lshl_add_u32 v6, v6, 7, s48\n"
        "v_mad_u32_u24 v252, v238, s70, v247\n"
        "v_mad_u32_u24 v253, v236, s70, v248\n"
        "v_min_u32 v7, 20, v0\n"
        "s_mul_i32 s70, s3, 20\n"
        "buffer_load_dwordx2 v[4:5], v6, s[40:43], 0 offen sc1\n"
        "v_mad_u32_u24 v248, v236, s71, v248\n"
        "v_add_u32 v236, s70, v7\n"
        "v_cmp_eq_u32 vcc, 20, v7\n"
        "v_mov_b32 v237, 80\n"
        "s_mul_i32 s73, s3, 21504\n"
        "v_mad_u32_u24 v247, v238, s71, v247\n"
        "v_cndmask_b32 v7, v236, v237, vcc\n"
        "s_add_u32 s73, s73, 74240\n"
        "v_add_u32 v241, s7, v13\n"
        "v_add_u32 v242, s7, v14\n"
        "v_cvt_pk_bf16_f32 v56, v116, v117\n"
        "v_cvt_pk_bf16_f32 v57, v118, v119\n"
        "v_cvt_pk_bf16_f32 v58, v120, v121\n"
        "v_cvt_pk_bf16_f32 v59, v122, v123\n"
        "v_cvt_pk_bf16_f32 v60, v124, v125\n"
        "v_cvt_pk_bf16_f32 v61, v126, v127\n"
        "v_cvt_pk_bf16_f32 v62, v128, v129\n"
        "v_cvt_pk_bf16_f32 v63, v130, v131\n"
        "v_cvt_pk_bf16_f32 v104, v132, v133\n"
        "v_cvt_pk_bf16_f32 v105, v134, v135\n"
        "v_cvt_pk_bf16_f32 v106, v136, v137\n"
        "v_cvt_pk_bf16_f32 v107, v138, v139\n"
        "v_cvt_pk_bf16_f32 v108, v140, v141\n"
        "v_cvt_pk_bf16_f32 v109, v142, v143\n"
        "v_cvt_pk_bf16_f32 v110, v144, v145\n"
        "v_cvt_pk_bf16_f32 v111, v146, v147\n"
        "v_cvt_pk_bf16_f32 v112, v148, v149\n"
        "v_cvt_pk_bf16_f32 v113, v150, v151\n"
        "v_cvt_pk_bf16_f32 v114, v152, v153\n"
        "v_cvt_pk_bf16_f32 v115, v154, v155\n"
        "v_add_u32 v244, s73, v244\n"
        "v_add_u32 v245, s73, v245\n"
        "v_add_u32 v246, s73, v246\n"
        "v_add_u32 v247, s73, v247\n"
        "v_add_u32 v248, s73, v248\n"
        "v_lshl_add_u32 v7, v7, 7, s49\n"
        "s_waitcnt vmcnt(5)\n"
        "v_mov_b32 v192, s64\n"
        "v_mov_b32 v193, s65\n"
        "v_cmp_eq_u32 vcc, 0, v0\n"
        "v_mov_b32 v194, 0x40000000\n"
        "s_nop 0\n"
        "v_cndmask_b32 v194, v194, v0, vcc\n"
        "buffer_store_dwordx2 v[192:193], v194, s[40:43], s68 offen sc1\n"
        "buffer_load_dwordx4 a[216:219], v241, s[40:43], s16 offen offset:0 sc1\n"
        "s_mul_i32 s72, s3, 3072\n"
        "v_lshlrev_b32 v9, 3, v2\n"
        "v_mul_u32_u24 v8, 160, v1\n"
        "s_add_u32 s72, s72, 0\n"
        "v_add3_u32 v255, v8, v9, s72\n"
        "v_lshlrev_b32 v254, 4, v1\n"
        "v_mov_b32 v243, v9\n"
        "buffer_load_dwordx4 a[220:223], v241, s[40:43], s16 offen offset:1024 sc1\n"
        "buffer_load_dwordx4 v[36:39], v241, s[40:43], s16 offen offset:2048 sc1\n"
        "buffer_load_dwordx4 v[40:43], v241, s[40:43], s16 offen offset:3072 sc1\n"
        "buffer_load_dwordx4 v[44:47], v242, s[40:43], s16 offen offset:0 sc1\n"
        "buffer_load_dwordx4 v[48:51], v242, s[40:43], s16 offen offset:1024 sc1\n"
        "buffer_load_dwordx4 v[52:55], v240, s[40:43], s17 offen offset:0 sc1\n"
        "buffer_load_dwordx4 a[244:247], v240, s[40:43], s17 offen offset:1024 sc1\n"
        "s_cmp_eq_u32 s7, 0\n"
        "s_cbranch_scc1 L21_%=\n"
        "s_waitcnt vmcnt(9)\n"
        "v_cmp_ne_u64 vcc, s[64:65], v[4:5]\n"
        "s_cbranch_vccz L23_%=\n"
        "s_nop 7\n"
        "s_nop 7\n"
        "s_nop 7\n"
        "s_waitcnt vmcnt(0) lgkmcnt(0)\n"
        "s_mov_b32 s6, 65536\n"
        "L22_%=:\n"
        "s_sleep 4\n"
        "buffer_load_dwordx2 v[4:5], v6, s[40:43], 0 offen sc1\n"
        "s_waitcnt vmcnt(0)\n"
        "v_cmp_ne_u64 vcc, s[64:65], v[4:5]\n"
        "s_cbranch_vccz L23_%=\n"
        "s_sub_u32 s6, s6, 1\n"
        "s_cmp_lg_u32 s6, 0\n"
        "s_cbranch_scc1 L22_%=\n"
        "L23_%=:\n"
        "buffer_load_dwordx4 a[200:203], v12, s[40:43], s16 offen offset:0 sc1\n"
        "buffer_load_dwordx4 a[204:207], v12, s[40:43], s16 offen offset:1024 sc1\n"
        "buffer_load_dwordx4 a[208:211], v12, s[40:43], s16 offen offset:2048 sc1\n"
        "buffer_load_dwordx4 a[212:215], v12, s[40:43], s16 offen offset:3072 sc1\n"
        "s_waitcnt vmcnt(11)\n"
        "buffer_load_dwordx4 a[216:219], v13, s[40:43], s16 offen offset:0 sc1\n"
        "s_waitcnt vmcnt(11)\n"
        "buffer_load_dwordx4 a[220:223], v13, s[40:43], s16 offen offset:1024 sc1\n"
        "s_waitcnt vmcnt(11)\n"
        "buffer_load_dwordx4 v[36:39], v13, s[40:43], s16 offen offset:2048 sc1\n"
        "s_waitcnt vmcnt(11)\n"
        "buffer_load_dwordx4 v[40:43], v13, s[40:43], s16 offen offset:3072 sc1\n"
        "s_waitcnt vmcnt(11)\n"
        "buffer_load_dwordx4 v[44:47], v14, s[40:43], s16 offen offset:0 sc1\n"
        "s_waitcnt vmcnt(11)\n"
        "buffer_load_dwordx4 v[48:51], v14, s[40:43], s16 offen offset:1024 sc1\n"
        "s_waitcnt vmcnt(11)\n"
        "buffer_load_dwordx4 v[52:55], v12, s[40:43], s17 offen offset:0 sc1\n"
        "s_waitcnt vmcnt(11)\n"
        "buffer_load_dwordx4 a[244:247], v12, s[40:43], s17 offen offset:1024 sc1\n"
        "L21_%=:\n"
        "JOIN_%=:\n"
        "s_waitcnt vmcnt(11)\n"
        "v_mfma_f32_16x16x32_bf16 v[116:119], a[0:3], a[200:203], 0\n"
        "v_mfma_f32_16x16x32_bf16 v[120:123], a[4:7], a[200:203], 0\n"
        "v_mfma_f32_16x16x32_bf16 v[124:127], a[8:11], a[200:203], 0\n"
        "v_mfma_f32_16x16x32_bf16 v[128:131], a[12:15], a[200:203], 0\n"
        "v_mfma_f32_16x16x32_bf16 v[132:135], a[16:19], a[200:203], 0\n"
        "buffer_load_dwordx4 a[200:203], v12, s[40:43], s17 offen offset:2048 sc1\n"
        "s_waitcnt vmcnt(11)\n"
        "v_mfma_f32_16x16x32_bf16 v[116:119], a[20:23], a[204:207], v[116:119]\n"
        "v_mfma_f32_16x16x32_bf16 v[120:123], a[24:27], a[204:207], v[120:123]\n"
        "buffer_load_dwordx2 v[222:223], v243, s[56:59], s45 offen offset:0\n"
        "v_mfma_f32_16x16x32_bf16 v[124:127], a[28:31], a[204:207], v[124:127]\n"
        "v_mfma_f32_16x16x32_bf16 v[128:131], a[32:35], a[204:207], v[128:131]\n"
        "v_mfma_f32_16x16x32_bf16 v[132:135], a[36:39], a[204:207], v[132:135]\n"
        "buffer_load_dwordx4 a[204:207], v12, s[40:43], s17 offen offset:3072 sc1\n"
        "s_waitcnt vmcnt(12)\n"
        "v_mfma_f32_16x16x32_bf16 v[116:119], a[40:43], a[208:211], v[116:119]\n"
        "v_mfma_f32_16x16x32_bf16 v[120:123], a[44:47], a[208:211], v[120:123]\n"
        "buffer_load_dwordx2 v[224:225], v243, s[56:59], s45 offen offset:32\n"
        "v_mfma_f32_16x16x32_bf16 v[124:127], a[48:51], a[208:211], v[124:127]\n"
        "v_mfma_f32_16x16x32_bf16 v[128:131], a[52:55], a[208:211], v[128:131]\n"
        "v_mfma_f32_16x16x32_bf16 v[132:135], a[56:59], a[208:211], v[132:135]\n"
        "buffer_load_dwordx4 a[208:211], v13, s[40:43], s17 offen offset:0 sc1\n"
        "s_waitcnt vmcnt(13)\n"
        "v_mfma_f32_16x16x32_bf16 v[116:119], a[60:63], a[212:215], v[116:119]\n"
        "v_mfma_f32_16x16x32_bf16 v[120:123], a[64:67], a[212:215], v[120:123]\n"
        "buffer_load_dwordx2 v[226:227], v243, s[56:59], s45 offen offset:64\n"
        "v_mfma_f32_16x16x32_bf16 v[124:127], a[68:71], a[212:215], v[124:127]\n"
        "v_mfma_f32_16x16x32_bf16 v[128:131], a[72:75], a[212:215], v[128:131]\n"
        "v_mfma_f32_16x16x32_bf16 v[132:135], a[76:79], a[212:215], v[132:135]\n"
        "buffer_load_dwordx4 a[212:215], v13, s[40:43], s17 offen offset:1024 sc1\n"
        "s_waitcnt vmcnt(14)\n"
        "v_mfma_f32_16x16x32_bf16 v[116:119], a[80:83], a[216:219], v[116:119]\n"
        "v_mfma_f32_16x16x32_bf16 v[120:123], a[84:87], a[216:219], v[120:123]\n"
        "buffer_load_dwordx2 v[228:229], v243, s[56:59], s45 offen offset:96\n"
        "v_mfma_f32_16x16x32_bf16 v[124:127], a[88:91], a[216:219], v[124:127]\n"
        "v_mfma_f32_16x16x32_bf16 v[128:131], a[92:95], a[216:219], v[128:131]\n"
        "v_mfma_f32_16x16x32_bf16 v[132:135], a[96:99], a[216:219], v[132:135]\n"
        "buffer_load_dwordx4 a[216:219], v13, s[40:43], s17 offen offset:2048 sc1\n"
        "s_waitcnt vmcnt(15)\n"
        "v_mfma_f32_16x16x32_bf16 v[116:119], a[100:103], a[220:223], v[116:119]\n"
        "v_mfma_f32_16x16x32_bf16 v[120:123], a[104:107], a[220:223], v[120:123]\n"
        "buffer_load_dwordx2 v[230:231], v243, s[56:59], s45 offen offset:128\n"
        "v_mfma_f32_16x16x32_bf16 v[124:127], a[108:111], a[220:223], v[124:127]\n"
        "v_mfma_f32_16x16x32_bf16 v[128:131], a[112:115], a[220:223], v[128:131]\n"
        "v_mfma_f32_16x16x32_bf16 v[132:135], a[116:119], a[220:223], v[132:135]\n"
        "buffer_load_dwordx4 a[220:223], v13, s[40:43], s17 offen offset:3072 sc1\n"
        "s_waitcnt vmcnt(16)\n"
        "v_mfma_f32_16x16x32_bf16 v[116:119], a[120:123], v[36:39], v[116:119]\n"
        "v_mfma_f32_16x16x32_bf16 v[120:123], a[124:127], v[36:39], v[120:123]\n"
        "buffer_load_dwordx4 v[196:199], v254, s[40:43], s24 offen sc1\n"
        "v_mfma_f32_16x16x32_bf16 v[124:127], a[128:131], v[36:39], v[124:127]\n"
        "v_mfma_f32_16x16x32_bf16 v[128:131], a[132:135], v[36:39], v[128:131]\n"
        "v_mfma_f32_16x16x32_bf16 v[132:135], a[136:139], v[36:39], v[132:135]\n"
        "buffer_load_dwordx4 v[36:39], v14, s[40:43], s17 offen offset:0 sc1\n"
        "s_waitcnt vmcnt(17)\n"
        "v_mfma_f32_16x16x32_bf16 v[116:119], a[140:143], v[40:43], v[116:119]\n"
        "v_mfma_f32_16x16x32_bf16 v[120:123], a[144:147], v[40:43], v[120:123]\n"
        "buffer_load_dwordx4 v[200:203], v254, s[40:43], s25 offen sc1\n"
        "v_mfma_f32_16x16x32_bf16 v[124:127], a[148:151], v[40:43], v[124:127]\n"
        "v_mfma_f32_16x16x32_bf16 v[128:131], a[152:155], v[40:43], v[128:131]\n"
        "v_mfma_f32_16x16x32_bf16 v[132:135], a[156:159], v[40:43], v[132:135]\n"
        "buffer_load_dwordx4 v[40:43], v14, s[40:43], s17 offen offset:1024 sc1\n"
        "s_waitcnt vmcnt(18)\n"
        "v_mfma_f32_16x16x32_bf16 v[116:119], a[160:163], v[44:47], v[116:119]\n"
        "v_mfma_f32_16x16x32_bf16 v[120:123], a[164:167], v[44:47], v[120:123]\n"
        "buffer_load_dwordx4 v[204:207], v254, s[40:43], s26 offen sc1\n"
        "v_mfma_f32_16x16x32_bf16 v[124:127], a[168:171], v[44:47], v[124:127]\n"
        "v_mfma_f32_16x16x32_bf16 v[128:131], a[172:175], v[44:47], v[128:131]\n"
        "v_mfma_f32_16x16x32_bf16 v[132:135], v[16:19], v[44:47], v[132:135]\n"
        "buffer_load_dwordx4 v[44:47], v12, s[40:43], s18 offen offset:0 sc1\n"
        "s_waitcnt vmcnt(19)\n"
        "v_mfma_f32_16x16x32_bf16 v[116:119], v[20:23], v[48:51], v[116:119]\n"
        "v_mfma_f32_16x16x32_bf16 v[120:123], v[24:27], v[48:51], v[120:123]\n"
        "buffer_load_dwordx4 v[208:211], v254, s[40:43], s27 offen sc1\n"
        "v_mfma_f32_16x16x32_bf16 v[124:127], v[28:31], v[48:51], v[124:127]\n"
        "v_mfma_f32_16x16x32_bf16 v[128:131], v[32:35], v[48:51], v[128:131]\n"
        "v_mfma_f32_16x16x32_bf16 v[132:135], a[196:199], v[48:51], v[132:135]\n"
        "buffer_load_dwordx4 v[48:51], v12, s[40:43], s18 offen offset:1024 sc1\n"
        "s_waitcnt vmcnt(20)\n"
        "v_mfma_f32_16x16x32_bf16 v[136:139], a[0:3], v[52:55], 0\n"
        "ds_read_b64 v[212:213], v255 offset:0\n"
        "ds_read_b64 v[214:215], v255 offset:32\n"
        "v_mfma_f32_16x16x32_bf16 v[140:143], a[4:7], v[52:55], 0\n"
        "ds_read_b64 v[216:217], v255 offset:64\n"
        "ds_read_b64 v[218:219], v255 offset:96\n"
        "v_mfma_f32_16x16x32_bf16 v[144:147], a[8:11], v[52:55], 0\n"
        "ds_read_b64 v[220:221], v255 offset:128\n"
        "s_waitcnt vmcnt(17)\n"
        "v_lshlrev_b32 v232, 16, v222\n"
        "v_mfma_f32_16x16x32_bf16 v[148:151], a[12:15], v[52:55], 0\n"
        "v_and_b32 v233, 0xffff0000, v222\n"
        "v_lshlrev_b32 v234, 16, v223\n"
        "v_mfma_f32_16x16x32_bf16 v[152:155], a[16:19], v[52:55], 0\n"
        "buffer_load_dwordx4 v[52:55], v12, s[40:43], s18 offen offset:2048 sc1\n"
        "v_mfma_f32_16x16x32_bf16 v[136:139], a[20:23], a[244:247], v[136:139]\n"
        "v_and_b32 v235, 0xffff0000, v223\n"
        "v_mfma_f32_16x16x32_bf16 v[140:143], a[24:27], a[244:247], v[140:143]\n"
        "s_waitcnt lgkmcnt(4)\n"
        "v_lshlrev_b32 v176, 16, v212\n"
        "v_and_b32 v177, 0xffff0000, v212\n"
        "v_mfma_f32_16x16x32_bf16 v[144:147], a[28:31], a[244:247], v[144:147]\n"
        "v_lshlrev_b32 v178, 16, v213\n"
        "v_and_b32 v179, 0xffff0000, v213\n"
        "v_mfma_f32_16x16x32_bf16 v[148:151], a[32:35], a[244:247], v[148:151]\n"
        "v_mul_f32 v176, v176, v232\n"
        "v_mul_f32 v177, v177, v233\n"
        "v_mfma_f32_16x16x32_bf16 v[152:155], a[36:39], a[244:247], v[152:155]\n"
        "buffer_load_dwordx4 a[244:247], v12, s[40:43], s18 offen offset:3072 sc1\n"
        "v_mfma_f32_16x16x32_bf16 v[136:139], a[40:43], a[200:203], v[136:139]\n"
        "v_mul_f32 v178, v178, v234\n"
        "v_mul_f32 v179, v179, v235\n"
        "v_mfma_f32_16x16x32_bf16 v[140:143], a[44:47], a[200:203], v[140:143]\n"
        "v_cvt_pk_bf16_f32 v232, v176, v177\n"
        "v_cvt_pk_bf16_f32 v233, v178, v179\n"
        "v_mfma_f32_16x16x32_bf16 v[144:147], a[48:51], a[200:203], v[144:147]\n"
        "v_lshlrev_b32 v176, 16, v232\n"
        "v_and_b32 v177, 0xffff0000, v232\n"
        "v_mfma_f32_16x16x32_bf16 v[148:151], a[52:55], a[200:203], v[148:151]\n"
        "v_lshlrev_b32 v178, 16, v233\n"
        "v_and_b32 v179, 0xffff0000, v233\n"
        "v_mfma_f32_16x16x32_bf16 v[152:155], a[56:59], a[200:203], v[152:155]\n"
        "buffer_load_dwordx4 a[200:203], v13, s[40:43], s18 offen offset:0 sc1\n"
        "s_waitcnt vmcnt(19)\n"
        "v_mfma_f32_16x16x32_bf16 v[136:139], a[60:63], a[204:207], v[136:139]\n"
        "s_waitcnt lgkmcnt(3)\n"
        "v_lshlrev_b32 v180, 16, v214\n"
        "v_and_b32 v181, 0xffff0000, v214\n"
        "v_mfma_f32_16x16x32_bf16 v[140:143], a[64:67], a[204:207], v[140:143]\n"
        "s_waitcnt vmcnt(18)\n"
        "v_lshlrev_b32 v232, 16, v224\n"
        "v_and_b32 v233, 0xffff0000, v224\n"
        "v_mfma_f32_16x16x32_bf16 v[144:147], a[68:71], a[204:207], v[144:147]\n"
        "v_lshlrev_b32 v182, 16, v215\n"
        "v_and_b32 v183, 0xffff0000, v215\n"
        "v_mfma_f32_16x16x32_bf16 v[148:151], a[72:75], a[204:207], v[148:151]\n"
        "v_lshlrev_b32 v234, 16, v225\n"
        "v_and_b32 v235, 0xffff0000, v225\n"
        "v_mfma_f32_16x16x32_bf16 v[152:155], a[76:79], a[204:207], v[152:155]\n"
        "buffer_load_dwordx4 a[204:207], v13, s[40:43], s18 offen offset:1024 sc1\n"
        "s_waitcnt vmcnt(18)\n"
        "v_mfma_f32_16x16x32_bf16 v[136:139], a[80:83], a[208:211], v[136:139]\n"
        "v_mul_f32 v180, v180, v232\n"
        "v_mul_f32 v181, v181, v233\n"
        "v_mfma_f32_16x16x32_bf16 v[140:143], a[84:87], a[208:211], v[140:143]\n"
        "v_mul_f32 v182, v182, v234\n"
        "v_mul_f32 v183, v183, v235\n"
        "v_mfma_f32_16x16x32_bf16 v[144:147], a[88:91], a[208:211], v[144:147]\n"
        "v_cvt_pk_bf16_f32 v232, v180, v181\n"
        "v_cvt_pk_bf16_f32 v233, v182, v183\n"
        "v_mfma_f32_16x16x32_bf16 v[148:151], a[92:95], a[208:211], v[148:151]\n"
        "v_lshlrev_b32 v180, 16, v232\n"
        "v_and_b32 v181, 0xffff0000, v232\n"
        "v_mfma_f32_16x16x32_bf16 v[152:155], a[96:99], a[208:211], v[152:155]\n"
        "buffer_load_dwordx4 a[208:211], v13, s[40:43], s18 offen offset:2048 sc1\n"
        "s_waitcnt vmcnt(17)\n"
        "v_mfma_f32_16x16x32_bf16 v[136:139], a[100:103], a[212:215], v[136:139]\n"
        "v_lshlrev_b32 v182, 16, v233\n"
        "v_and_b32 v183, 0xffff0000, v233\n"
        "v_mfma_f32_16x16x32_bf16 v[140:143], a[104:107], a[212:215], v[140:143]\n"
        "s_waitcnt lgkmcnt(2)\n"
        "v_lshlrev_b32 v184, 16, v216\n"
        "v_and_b32 v185, 0xffff0000, v216\n"
        "v_mfma_f32_16x16x32_bf16 v[144:147], a[108:111], a[212:215], v[144:147]\n"
        "v_lshlrev_b32 v232, 16, v226\n"
        "v_and_b32 v233, 0xffff0000, v226\n"
        "v_mfma_f32_16x16x32_bf16 v[148:151], a[112:115], a[212:215], v[148:151]\n"
        "v_lshlrev_b32 v186, 16, v217\n"
        "v_and_b32 v187, 0xffff0000, v217\n"
        "v_mfma_f32_16x16x32_bf16 v[152:155], a[116:119], a[212:215], v[152:155]\n"
        "buffer_load_dwordx4 a[212:215], v13, s[40:43], s18 offen offset:3072 sc1\n"
        "s_waitcnt vmcnt(16)\n"
        "v_mfma_f32_16x16x32_bf16 v[136:139], a[120:123], a[216:219], v[136:139]\n"
        "v_lshlrev_b32 v234, 16, v227\n"
        "v_and_b32 v235, 0xffff0000, v227\n"
        "v_mfma_f32_16x16x32_bf16 v[140:143], a[124:127], a[216:219], v[140:143]\n"
        "v_mul_f32 v184, v184, v232\n"
        "v_mul_f32 v185, v185, v233\n"
        "v_mfma_f32_16x16x32_bf16 v[144:147], a[128:131], a[216:219], v[144:147]\n"
        "v_mul_f32 v186, v186, v234\n"
        "v_mul_f32 v187, v187, v235\n"
        "v_mfma_f32_16x16x32_bf16 v[148:151], a[132:135], a[216:219], v[148:151]\n"
        "v_cvt_pk_bf16_f32 v232, v184, v185\n"
        "v_cvt_pk_bf16_f32 v233, v186, v187\n"
        "v_mfma_f32_16x16x32_bf16 v[152:155], a[136:139], a[216:219], v[152:155]\n"
        "buffer_load_dwordx4 a[216:219], v14, s[40:43], s18 offen offset:0 sc1\n"
        "s_waitcnt vmcnt(15)\n"
        "v_mfma_f32_16x16x32_bf16 v[136:139], a[140:143], a[220:223], v[136:139]\n"
        "v_lshlrev_b32 v184, 16, v232\n"
        "v_and_b32 v185, 0xffff0000, v232\n"
        "v_mfma_f32_16x16x32_bf16 v[140:143], a[144:147], a[220:223], v[140:143]\n"
        "v_lshlrev_b32 v186, 16, v233\n"
        "v_and_b32 v187, 0xffff0000, v233\n"
        "v_mfma_f32_16x16x32_bf16 v[144:147], a[148:151], a[220:223], v[144:147]\n"
        "s_waitcnt lgkmcnt(1)\n"
        "v_lshlrev_b32 v188, 16, v218\n"
        "v_and_b32 v189, 0xffff0000, v218\n"
        "v_mfma_f32_16x16x32_bf16 v[148:151], a[152:155], a[220:223], v[148:151]\n"
        "v_lshlrev_b32 v232, 16, v228\n"
        "v_and_b32 v233, 0xffff0000, v228\n"
        "v_mfma_f32_16x16x32_bf16 v[152:155], a[156:159], a[220:223], v[152:155]\n"
        "buffer_load_dwordx4 a[220:223], v14, s[40:43], s18 offen offset:1024 sc1\n"
        "s_waitcnt vmcnt(14)\n"
        "v_mfma_f32_16x16x32_bf16 v[136:139], a[160:163], v[36:39], v[136:139]\n"
        "v_lshlrev_b32 v190, 16, v219\n"
        "v_and_b32 v191, 0xffff0000, v219\n"
        "v_mfma_f32_16x16x32_bf16 v[140:143], a[164:167], v[36:39], v[140:143]\n"
        "v_lshlrev_b32 v234, 16, v229\n"
        "v_and_b32 v235, 0xffff0000, v229\n"
        "v_mfma_f32_16x16x32_bf16 v[144:147], a[168:171], v[36:39], v[144:147]\n"
        "v_mul_f32 v188, v188, v232\n"
        "v_mul_f32 v189, v189, v233\n"
        "v_mfma_f32_16x16x32_bf16 v[148:151], a[172:175], v[36:39], v[148:151]\n"
        "v_mul_f32 v190, v190, v234\n"
        "v_mul_f32 v191, v191, v235\n"
        "v_mfma_f32_16x16x32_bf16 v[152:155], v[16:19], v[36:39], v[152:155]\n"
        "buffer_load_dwordx4 v[36:39], v12, s[40:43], s19 offen offset:0 sc1\n"
        "s_waitcnt vmcnt(13)\n"
        "v_mfma_f32_16x16x32_bf16 v[136:139], v[20:23], v[40:43], v[136:139]\n"
        "v_cvt_pk_bf16_f32 v232, v188, v189\n"
        "v_cvt_pk_bf16_f32 v233, v190, v191\n"
        "v_mfma_f32_16x16x32_bf16 v[140:143], v[24:27], v[40:43], v[140:143]\n"
        "v_lshlrev_b32 v188, 16, v232\n"
        "v_and_b32 v189, 0xffff0000, v232\n"
        "v_mfma_f32_16x16x32_bf16 v[144:147], v[28:31], v[40:43], v[144:147]\n"
        "v_lshlrev_b32 v190, 16, v233\n"
        "v_and_b32 v191, 0xffff0000, v233\n"
        "v_mfma_f32_16x16x32_bf16 v[148:151], v[32:35], v[40:43], v[148:151]\n"
        "s_waitcnt lgkmcnt(0)\n"
        "v_lshlrev_b32 v192, 16, v220\n"
        "v_and_b32 v193, 0xffff0000, v220\n"
        "v_mfma_f32_16x16x32_bf16 v[152:155], a[196:199], v[40:43], v[152:155]\n"
        "buffer_load_dwordx4 v[40:43], v12, s[40:43], s19 offen offset:1024 sc1\n"
        "s_waitcnt vmcnt(12)\n"
        "v_mfma_f32_16x16x32_bf16 v[156:159], a[0:3], v[44:47], 0\n"
        "v_lshlrev_b32 v232, 16, v230\n"
        "v_and_b32 v233, 0xffff0000, v230\n"
        "v_mfma_f32_16x16x32_bf16 v[160:163], a[4:7], v[44:47], 0\n"
        "v_lshlrev_b32 v194, 16, v221\n"
        "v_and_b32 v195, 0xffff0000, v221\n"
        "v_mfma_f32_16x16x32_bf16 v[164:167], a[8:11], v[44:47], 0\n"
        "v_lshlrev_b32 v234, 16, v231\n"
        "v_and_b32 v235, 0xffff0000, v231\n"
        "v_mfma_f32_16x16x32_bf16 v[168:171], a[12:15], v[44:47], 0\n"
        "buffer_load_dwordx2 v[8:9], v7, s[40:43], 0 offen sc1\n"
        "v_mfma_f32_16x16x32_bf16 v[172:175], a[16:19], v[44:47], 0\n"
        "v_mul_f32 v192, v192, v232\n"
        "v_mul_f32 v193, v193, v233\n"
        "s_waitcnt vmcnt(11)\n"
        "v_mfma_f32_16x16x32_bf16 v[156:159], a[20:23], v[48:51], v[156:159]\n"
        "v_mul_f32 v194, v194, v234\n"
        "v_mul_f32 v195, v195, v235\n"
        "v_mfma_f32_16x16x32_bf16 v[160:163], a[24:27], v[48:51], v[160:163]\n"
        "v_cvt_pk_bf16_f32 v232, v192, v193\n"
        "v_cvt_pk_bf16_f32 v233, v194, v195\n"
        "v_mfma_f32_16x16x32_bf16 v[164:167], a[28:31], v[48:51], v[164:167]\n"
        "buffer_load_dwordx4 v[44:47], v12, s[40:43], s19 offen offset:2048 sc1\n"
        "v_mfma_f32_16x16x32_bf16 v[168:171], a[32:35], v[48:51], v[168:171]\n"
        "v_lshlrev_b32 v192, 16, v232\n"
        "v_and_b32 v193, 0xffff0000, v232\n"
        "v_mfma_f32_16x16x32_bf16 v[172:175], a[36:39], v[48:51], v[172:175]\n"
        "v_lshlrev_b32 v194, 16, v233\n"
        "v_and_b32 v195, 0xffff0000, v233\n"
        "s_waitcnt vmcnt(11)\n"
        "v_mfma_f32_16x16x32_bf16 v[156:159], a[40:43], v[52:55], v[156:159]\n"
        "v_mfma_f32_16x16x32_bf16 v[160:163], a[44:47], v[52:55], v[160:163]\n"
        "buffer_load_dwordx4 v[48:51], v12, s[40:43], s19 offen offset:3072 sc1\n"
        "v_mfma_f32_16x16x32_bf16 v[164:167], a[48:51], v[52:55], v[164:167]\n"
        "v_mfma_f32_16x16x32_bf16 v[168:171], a[52:55], v[52:55], v[168:171]\n"
        "v_mfma_f32_16x16x32_bf16 v[172:175], a[56:59], v[52:55], v[172:175]\n"
        "s_waitcnt vmcnt(11)\n"
        "v_mfma_f32_16x16x32_bf16 v[156:159], a[60:63], a[244:247], v[156:159]\n"
        "buffer_load_dwordx4 v[52:55], v13, s[40:43], s19 offen offset:0 sc1\n"
        "v_mfma_f32_16x16x32_bf16 v[160:163], a[64:67], a[244:247], v[160:163]\n"
        "v_mfma_f32_16x16x32_bf16 v[164:167], a[68:71], a[244:247], v[164:167]\n"
        "v_mfma_f32_16x16x32_bf16 v[168:171], a[72:75], a[244:247], v[168:171]\n"
        "v_mfma_f32_16x16x32_bf16 v[172:175], a[76:79], a[244:247], v[172:175]\n"
        "buffer_load_dwordx4 a[244:247], v13, s[40:43], s19 offen offset:1024 sc1\n"
        "s_waitcnt vmcnt(12)\n"
        "v_mfma_f32_16x16x32_bf16 v[156:159], a[80:83], a[200:203], v[156:159]\n"
        "v_mfma_f32_16x16x32_bf16 v[160:163], a[84:87], a[200:203], v[160:163]\n"
        "v_mfma_f32_16x16x32_bf16 v[164:167], a[88:91], a[200:203], v[164:167]\n"
        "v_mfma_f32_16x16x32_bf16 v[168:171], a[92:95], a[200:203], v[168:171]\n"
        "v_mfma_f32_16x16x32_bf16 v[172:175], a[96:99], a[200:203], v[172:175]\n"
        "buffer_load_dwordx4 a[200:203], v13, s[40:43], s19 offen offset:2048 sc1\n"
        "s_waitcnt vmcnt(12)\n"
        "v_mfma_f32_16x16x32_bf16 v[156:159], a[100:103], a[204:207], v[156:159]\n"
        "v_mfma_f32_16x16x32_bf16 v[160:163], a[104:107], a[204:207], v[160:163]\n"
        "v_mfma_f32_16x16x32_bf16 v[164:167], a[108:111], a[204:207], v[164:167]\n"
        "v_mfma_f32_16x16x32_bf16 v[168:171], a[112:115], a[204:207], v[168:171]\n"
        "v_mfma_f32_16x16x32_bf16 v[172:175], a[116:119], a[204:207], v[172:175]\n"
        "buffer_load_dwordx4 a[204:207], v13, s[40:43], s19 offen offset:3072 sc1\n"
        "s_waitcnt vmcnt(12)\n"
        "v_mfma_f32_16x16x32_bf16 v[156:159], a[120:123], a[208:211], v[156:159]\n"
        "v_mfma_f32_16x16x32_bf16 v[160:163], a[124:127], a[208:211], v[160:163]\n"
        "v_mfma_f32_16x16x32_bf16 v[164:167], a[128:131], a[208:211], v[164:167]\n"
        "v_mfma_f32_16x16x32_bf16 v[168:171], a[132:135], a[208:211], v[168:171]\n"
        "v_mfma_f32_16x16x32_bf16 v[172:175], a[136:139], a[208:211], v[172:175]\n"
        "buffer_load_dwordx4 a[208:211], v14, s[40:43], s19 offen offset:0 sc1\n"
        "s_waitcnt vmcnt(12)\n"
        "v_mfma_f32_16x16x32_bf16 v[156:159], a[140:143], a[212:215], v[156:159]\n"
        "v_mfma_f32_16x16x32_bf16 v[160:163], a[144:147], a[212:215], v[160:163]\n"
        "v_mfma_f32_16x16x32_bf16 v[164:167], a[148:151], a[212:215], v[164:167]\n"
        "v_mfma_f32_16x16x32_bf16 v[168:171], a[152:155], a[212:215], v[168:171]\n"
        "v_mfma_f32_16x16x32_bf16 v[172:175], a[156:159], a[212:215], v[172:175]\n"
        "buffer_load_dwordx4 a[212:215], v14, s[40:43], s19 offen offset:1024 sc1\n"
        "v_xor_b32 v4, s64, v197\n"
        "v_xor_b32 v5, s65, v199\n"
        "v_or_b32 v6, v4, v5\n"
        "v_xor_b32 v4, s64, v201\n"
        "v_xor_b32 v5, s65, v203\n"
        "v_or3_b32 v6, v4, v5, v6\n"
        "v_xor_b32 v4, s64, v205\n"
        "v_xor_b32 v5, s65, v207\n"
        "v_or3_b32 v6, v4, v5, v6\n"
        "v_xor_b32 v4, s64, v209\n"
        "v_xor_b32 v5, s65, v211\n"
        "v_or3_b32 v6, v4, v5, v6\n"
        "v_cmp_ne_u32 vcc, 0, v6\n"
        "s_cbranch_vccz L25_%=\n"
        "s_nop 7\n"
        "s_nop 7\n"
        "s_nop 7\n"
        "s_waitcnt vmcnt(0) lgkmcnt(0)\n"
        "s_mov_b32 s6, 65536\n"
        "L24_%=:\n"
        "s_sleep 4\n"
        "buffer_load_dwordx4 v[196:199], v254, s[40:43], s24 offen sc1\n"
        "buffer_load_dwordx4 v[200:203], v254, s[40:43], s25 offen sc1\n"
        "buffer_load_dwordx4 v[204:207], v254, s[40:43], s26 offen sc1\n"
        "buffer_load_dwordx4 v[208:211], v254, s[40:43], s27 offen sc1\n"
        "s_waitcnt vmcnt(3)\n"
        "v_xor_b32 v4, s64, v197\n"
        "v_xor_b32 v5, s65, v199\n"
        "v_or_b32 v6, v4, v5\n"
        "s_waitcnt vmcnt(2)\n"
        "v_xor_b32 v4, s64, v201\n"
        "v_xor_b32 v5, s65, v203\n"
        "v_or3_b32 v6, v4, v5, v6\n"
        "s_waitcnt vmcnt(1)\n"
        "v_xor_b32 v4, s64, v205\n"
        "v_xor_b32 v5, s65, v207\n"
        "v_or3_b32 v6, v4, v5, v6\n"
        "s_waitcnt vmcnt(0)\n"
        "v_xor_b32 v4, s64, v209\n"
        "v_xor_b32 v5, s65, v211\n"
        "v_or3_b32 v6, v4, v5, v6\n"
        "v_cmp_ne_u32 vcc, 0, v6\n"
        "s_cbranch_vccz L25_%=\n"
        "s_sub_u32 s6, s6, 1\n"
        "s_cmp_lg_u32 s6, 0\n"
        "s_cbranch_scc1 L24_%=\n"
        "L25_%=:\n"
        "s_waitcnt vmcnt(8)\n"
        "v_cmp_ne_u64 vcc, s[64:65], v[8:9]\n"
        "s_cmp_eq_u64 vcc, 0\n"
        "s_cselect_b32 s7, 0, 0x40000000\n"
        "v_mfma_f32_16x16x32_bf16 v[156:159], a[160:163], a[216:219], v[156:159]\n"
        "v_add_u32 v10, s7, v12\n"
        "v_add_u32 v6, s7, v254\n"
        "v_mfma_f32_16x16x32_bf16 v[160:163], a[164:167], a[216:219], v[160:163]\n"
        "v_add_u32 v11, s7, v13\n"
        "v_mov_b32 v237, v198\n"
        "v_mfma_f32_16x16x32_bf16 v[164:167], a[168:171], a[216:219], v[164:167]\n"
        "v_mov_b32 v239, v202\n"
        "v_mov_b32 v241, v206\n"
        "v_mfma_f32_16x16x32_bf16 v[168:171], a[172:175], a[216:219], v[168:171]\n"
        "v_mov_b32 v236, v196\n"
        "v_mov_b32 v238, v200\n"
        "v_mfma_f32_16x16x32_bf16 v[172:175], v[16:19], a[216:219], v[172:175]\n"
        "buffer_load_dwordx4 a[216:219], v10, s[40:43], s20 offen offset:0 sc1\n"
        "v_mfma_f32_16x16x32_bf16 v[156:159], v[20:23], a[220:223], v[156:159]\n"
        "v_mov_b32 v240, v204\n"
        "v_mov_b32 v242, v208\n"
        "v_mfma_f32_16x16x32_bf16 v[160:163], v[24:27], a[220:223], v[160:163]\n"
        "v_mov_b32 v243, v210\n"
        "v_mul_f32 v116, v237, v116\n"
        "v_mfma_f32_16x16x32_bf16 v[164:167], v[28:31], a[220:223], v[164:167]\n"
        "v_mul_f32 v117, v237, v117\n"
        "v_mul_f32 v118, v237, v118\n"
        "v_mfma_f32_16x16x32_bf16 v[168:171], v[32:35], a[220:223], v[168:171]\n"
        "buffer_load_dword v255, v6, s[40:43], s47 offen sc1\n"
        "v_mfma_f32_16x16x32_bf16 v[172:175], a[196:199], a[220:223], v[172:175]\n"
        "v_mul_f32 v119, v237, v119\n"
        "v_mul_f32 v120, v237, v120\n"
        "v_mfma_f32_16x16x32_bf16 v[176:179], a[0:3], v[36:39], v[176:179]\n"
        "v_mul_f32 v121, v237, v121\n"
        "v_mul_f32 v122, v237, v122\n"
        "v_mfma_f32_16x16x32_bf16 v[180:183], a[4:7], v[36:39], v[180:183]\n"
        "v_mul_f32 v123, v237, v123\n"
        "v_mul_f32 v124, v237, v124\n"
        "v_mfma_f32_16x16x32_bf16 v[184:187], a[8:11], v[36:39], v[184:187]\n"
        "buffer_load_dwordx4 a[220:223], v10, s[40:43], s20 offen offset:1024 sc1\n"
        "v_mfma_f32_16x16x32_bf16 v[188:191], a[12:15], v[36:39], v[188:191]\n"
        "v_mul_f32 v125, v237, v125\n"
        "v_mul_f32 v126, v237, v126\n"
        "v_mfma_f32_16x16x32_bf16 v[192:195], a[16:19], v[36:39], v[192:195]\n"
        "v_mul_f32 v127, v237, v127\n"
        "v_mul_f32 v128, v237, v128\n"
        "v_mfma_f32_16x16x32_bf16 v[176:179], a[20:23], v[40:43], v[176:179]\n"
        "v_mul_f32 v129, v237, v129\n"
        "v_mul_f32 v130, v237, v130\n"
        "v_mfma_f32_16x16x32_bf16 v[180:183], a[24:27], v[40:43], v[180:183]\n"
        "buffer_load_dwordx4 v[36:39], v10, s[40:43], s20 offen offset:2048 sc1\n"
        "v_mfma_f32_16x16x32_bf16 v[184:187], a[28:31], v[40:43], v[184:187]\n"
        "v_mul_f32 v131, v237, v131\n"
        "v_mul_f32 v132, v237, v132\n"
        "v_mfma_f32_16x16x32_bf16 v[188:191], a[32:35], v[40:43], v[188:191]\n"
        "v_mul_f32 v133, v237, v133\n"
        "v_mul_f32 v134, v237, v134\n"
        "v_mfma_f32_16x16x32_bf16 v[192:195], a[36:39], v[40:43], v[192:195]\n"
        "v_mul_f32 v135, v237, v135\n"
        "v_mul_f32 v136, v239, v136\n"
        "s_waitcnt vmcnt(11)\n"
        "v_mfma_f32_16x16x32_bf16 v[176:179], a[40:43], v[44:47], v[176:179]\n"
        "buffer_load_dwordx4 v[40:43], v10, s[40:43], s20 offen offset:3072 sc1\n"
        "v_mfma_f32_16x16x32_bf16 v[180:183], a[44:47], v[44:47], v[180:183]\n"
        "v_mul_f32 v137, v239, v137\n"
        "v_mul_f32 v138, v239, v138\n"
        "v_mfma_f32_16x16x32_bf16 v[184:187], a[48:51], v[44:47], v[184:187]\n"
        "v_mul_f32 v139, v239, v139\n"
        "v_mul_f32 v140, v239, v140\n"
        "v_mfma_f32_16x16x32_bf16 v[188:191], a[52:55], v[44:47], v[188:191]\n"
        "v_mul_f32 v141, v239, v141\n"
        "v_mul_f32 v142, v239, v142\n"
        "v_mfma_f32_16x16x32_bf16 v[192:195], a[56:59], v[44:47], v[192:195]\n"
        "buffer_load_dwordx4 v[44:47], v11, s[40:43], s20 offen offset:0 sc1\n"
        "s_waitcnt vmcnt(12)\n"
        "v_mfma_f32_16x16x32_bf16 v[176:179], a[60:63], v[48:51], v[176:179]\n"
        "v_mul_f32 v143, v239, v143\n"
        "v_mul_f32 v144, v239, v144\n"
        "v_mfma_f32_16x16x32_bf16 v[180:183], a[64:67], v[48:51], v[180:183]\n"
        "v_mul_f32 v145, v239, v145\n"
        "v_mul_f32 v146, v239, v146\n"
        "v_mfma_f32_16x16x32_bf16 v[184:187], a[68:71], v[48:51], v[184:187]\n"
        "v_mul_f32 v147, v239, v147\n"
        "v_mul_f32 v148, v239, v148\n"
        "v_mfma_f32_16x16x32_bf16 v[188:191], a[72:75], v[48:51], v[188:191]\n"
        "v_mul_f32 v149, v239, v149\n"
        "v_mul_f32 v150, v239, v150\n"
        "v_mfma_f32_16x16x32_bf16 v[192:195], a[76:79], v[48:51], v[192:195]\n"
        "buffer_load_dwordx4 v[48:51], v10, s[40:43], s21 offen offset:0 sc1\n"
        "s_waitcnt vmcnt(12)\n"
        "v_mfma_f32_16x16x32_bf16 v[176:179], a[80:83], v[52:55], v[176:179]\n"
        "v_mul_f32 v151, v239, v151\n"
        "v_mul_f32 v152, v239, v152\n"
        "v_mfma_f32_16x16x32_bf16 v[180:183], a[84:87], v[52:55], v[180:183]\n"
        "v_mul_f32 v153, v239, v153\n"
        "v_mul_f32 v154, v239, v154\n"
        "v_mfma_f32_16x16x32_bf16 v[184:187], a[88:91], v[52:55], v[184:187]\n"
        "v_mul_f32 v155, v239, v155\n"
        "v_mul_f32 v156, v241, v156\n"
        "v_mfma_f32_16x16x32_bf16 v[188:191], a[92:95], v[52:55], v[188:191]\n"
        "v_mul_f32 v157, v241, v157\n"
        "v_mul_f32 v158, v241, v158\n"
        "v_mfma_f32_16x16x32_bf16 v[192:195], a[96:99], v[52:55], v[192:195]\n"
        "buffer_load_dwordx4 v[52:55], v10, s[40:43], s21 offen offset:1024 sc1\n"
        "s_waitcnt vmcnt(12)\n"
        "v_mfma_f32_16x16x32_bf16 v[176:179], a[100:103], a[244:247], v[176:179]\n"
        "v_mul_f32 v159, v241, v159\n"
        "v_mul_f32 v160, v241, v160\n"
        "v_mfma_f32_16x16x32_bf16 v[180:183], a[104:107], a[244:247], v[180:183]\n"
        "v_mul_f32 v161, v241, v161\n"
        "v_mul_f32 v162, v241, v162\n"
        "v_mfma_f32_16x16x32_bf16 v[184:187], a[108:111], a[244:247], v[184:187]\n"
        "v_mul_f32 v163, v241, v163\n"
        "v_mul_f32 v164, v241, v164\n"
        "v_mfma_f32_16x16x32_bf16 v[188:191], a[112:115], a[244:247], v[188:191]\n"
        "v_mul_f32 v165, v241, v165\n"
        "v_mul_f32 v166, v241, v166\n"
        "v_mfma_f32_16x16x32_bf16 v[192:195], a[116:119], a[244:247], v[192:195]\n"
        "buffer_load_dwordx4 a[244:247], v10, s[40:43], s21 offen offset:2048 sc1\n"
        "s_waitcnt vmcnt(12)\n"
        "v_mfma_f32_16x16x32_bf16 v[176:179], a[120:123], a[200:203], v[176:179]\n"
        "v_mul_f32 v167, v241, v167\n"
        "v_mul_f32 v168, v241, v168\n"
        "v_mfma_f32_16x16x32_bf16 v[180:183], a[124:127], a[200:203], v[180:183]\n"
        "v_mul_f32 v169, v241, v169\n"
        "v_mul_f32 v170, v241, v170\n"
        "v_mfma_f32_16x16x32_bf16 v[184:187], a[128:131], a[200:203], v[184:187]\n"
        "v_mul_f32 v171, v241, v171\n"
        "v_mul_f32 v172, v241, v172\n"
        "v_mfma_f32_16x16x32_bf16 v[188:191], a[132:135], a[200:203], v[188:191]\n"
        "v_mul_f32 v173, v241, v173\n"
        "v_mul_f32 v174, v241, v174\n"
        "v_mfma_f32_16x16x32_bf16 v[192:195], a[136:139], a[200:203], v[192:195]\n"
        "buffer_load_dwordx4 a[200:203], v10, s[40:43], s21 offen offset:3072 sc1\n"
        "s_waitcnt vmcnt(12)\n"
        "v_mfma_f32_16x16x32_bf16 v[176:179], a[140:143], a[204:207], v[176:179]\n"
        "v_mul_f32 v175, v241, v175\n"
        "v_mfma_f32_16x16x32_bf16 v[180:183], a[144:147], a[204:207], v[180:183]\n"
        "v_mfma_f32_16x16x32_bf16 v[184:187], a[148:151], a[204:207], v[184:187]\n"
        "v_mfma_f32_16x16x32_bf16 v[188:191], a[152:155], a[204:207], v[188:191]\n"
        "v_mfma_f32_16x16x32_bf16 v[192:195], a[156:159], a[204:207], v[192:195]\n"
        "buffer_load_dwordx4 a[204:207], v11, s[40:43], s21 offen offset:0 sc1\n"
        "s_waitcnt vmcnt(12)\n"
        "v_mfma_f32_16x16x32_bf16 v[176:179], a[160:163], a[208:211], v[176:179]\n"
        "v_mfma_f32_16x16x32_bf16 v[180:183], a[164:167], a[208:211], v[180:183]\n"
        "v_mfma_f32_16x16x32_bf16 v[184:187], a[168:171], a[208:211], v[184:187]\n"
        "v_mfma_f32_16x16x32_bf16 v[188:191], a[172:175], a[208:211], v[188:191]\n"
        "v_mfma_f32_16x16x32_bf16 v[192:195], v[16:19], a[208:211], v[192:195]\n"
        "buffer_load_dwordx4 a[208:211], v10, s[40:43], s22 offen offset:0 sc1\n"
        "s_waitcnt vmcnt(12)\n"
        "v_mfma_f32_16x16x32_bf16 v[176:179], v[20:23], a[212:215], v[176:179]\n"
        "v_mfma_f32_16x16x32_bf16 v[180:183], v[24:27], a[212:215], v[180:183]\n"
        "v_mfma_f32_16x16x32_bf16 v[184:187], v[28:31], a[212:215], v[184:187]\n"
        "v_mfma_f32_16x16x32_bf16 v[188:191], v[32:35], a[212:215], v[188:191]\n"
        "v_mfma_f32_16x16x32_bf16 v[192:195], a[196:199], a[212:215], v[192:195]\n"
        "buffer_load_dwordx4 a[212:215], v10, s[40:43], s22 offen offset:1024 sc1\n"
        "s_cmp_eq_u32 s7, 0\n"
        "s_cbranch_scc1 L26_%=\n"
        "buffer_load_dwordx2 v[8:9], v7, s[40:43], 0 offen sc1\n"
        "s_waitcnt vmcnt(0)\n"
        "v_cmp_ne_u64 vcc, s[64:65], v[8:9]\n"
        "s_cbranch_vccz L28_%=\n"
        "s_nop 7\n"
        "s_nop 7\n"
        "s_nop 7\n"
        "s_waitcnt vmcnt(0) lgkmcnt(0)\n"
        "s_mov_b32 s6, 65536\n"
        "L27_%=:\n"
        "s_sleep 4\n"
        "buffer_load_dwordx2 v[8:9], v7, s[40:43], 0 offen sc1\n"
        "s_waitcnt vmcnt(0)\n"
        "v_cmp_ne_u64 vcc, s[64:65], v[8:9]\n"
        "s_cbranch_vccz L28_%=\n"
        "s_sub_u32 s6, s6, 1\n"
        "s_cmp_lg_u32 s6, 0\n"
        "s_cbranch_scc1 L27_%=\n"
        "L28_%=:\n"
        "buffer_load_dwordx4 a[216:219], v12, s[40:43], s20 offen offset:0 sc1\n"
        "buffer_load_dword v255, v254, s[40:43], s47 offen sc1\n"
        "buffer_load_dwordx4 a[220:223], v12, s[40:43], s20 offen offset:1024 sc1\n"
        "buffer_load_dwordx4 v[36:39], v12, s[40:43], s20 offen offset:2048 sc1\n"
        "buffer_load_dwordx4 v[40:43], v12, s[40:43], s20 offen offset:3072 sc1\n"
        "buffer_load_dwordx4 v[44:47], v13, s[40:43], s20 offen offset:0 sc1\n"
        "buffer_load_dwordx4 v[48:51], v12, s[40:43], s21 offen offset:0 sc1\n"
        "buffer_load_dwordx4 v[52:55], v12, s[40:43], s21 offen offset:1024 sc1\n"
        "buffer_load_dwordx4 a[244:247], v12, s[40:43], s21 offen offset:2048 sc1\n"
        "buffer_load_dwordx4 a[200:203], v12, s[40:43], s21 offen offset:3072 sc1\n"
        "buffer_load_dwordx4 a[204:207], v13, s[40:43], s21 offen offset:0 sc1\n"
        "buffer_load_dwordx4 a[208:211], v12, s[40:43], s22 offen offset:0 sc1\n"
        "buffer_load_dwordx4 a[212:215], v12, s[40:43], s22 offen offset:1024 sc1\n"
        "s_lshl_b32 s72, s3, 10\n"
        "s_add_u32 m0, s72, 32768\n"
        "s_and_b32 s72, s5, 7\n"
        "s_add_u32 s70, s72, 12\n"
        "s_mov_b32 s71, 0x40000000\n"
        "s_cmp_lt_u32 s70, 20\n"
        "s_cselect_b32 s71, s23, s71\n"
        "s_cselect_b32 s73, 15, s73\n"
        "s_cmp_lt_u32 s70, 15\n"
        "s_cselect_b32 s71, s22, s71\n"
        "s_cselect_b32 s73, 10, s73\n"
        "s_sub_u32 s70, s70, s73\n"
        "s_lshl_b32 s70, s70, 10\n"
        "s_and_b32 s70, s70, 0x3fff\n"
        "s_add_u32 s71, s71, s70\n"
        "buffer_load_dwordx4 v12, s[40:43], s71 offen sc1 lds\n"
        "L26_%=:\n"
        "s_waitcnt vmcnt(12)\n"
        "v_mfma_f32_16x16x32_bf16 v[196:199], a[176:179], a[216:219], 0\n"
        "s_waitcnt vmcnt(11)\n"
        "v_lshlrev_b32 v4, 16, v255\n"
        "v_mul_u32_u24 v254, 336, v1\n"
        "v_mfma_f32_16x16x32_bf16 v[200:203], a[180:183], a[216:219], 0\n"
        "v_xor_b32 v5, 0x80000000, v4\n"
        "v_mov_b32 v6, v4\n"
        "v_mfma_f32_16x16x32_bf16 v[204:207], a[184:187], a[216:219], 0\n"
        "v_mov_b32 v7, v5\n"
        "v_lshl_add_u32 v254, v2, 4, v254\n"
        "v_mfma_f32_16x16x32_bf16 v[208:211], a[188:191], a[216:219], 0\n"
        "v_add_u32 v255, s28, v254\n"
        "v_mul_f32 v176, v243, v176\n"
        "v_mfma_f32_16x16x32_bf16 v[212:215], a[192:195], a[216:219], 0\n"
        "buffer_load_dwordx4 a[216:219], v12, s[40:43], s22 offen offset:2048 sc1\n"
        "s_waitcnt vmcnt(11)\n"
        "v_mfma_f32_16x16x32_bf16 v[196:199], v[64:67], a[220:223], v[196:199]\n"
        "v_mul_f32 v177, v243, v177\n"
        "v_mul_f32 v178, v243, v178\n"
        "v_mfma_f32_16x16x32_bf16 v[200:203], v[68:71], a[220:223], v[200:203]\n"
        "v_mul_f32 v179, v243, v179\n"
        "v_mul_f32 v180, v243, v180\n"
        "v_mfma_f32_16x16x32_bf16 v[204:207], v[72:75], a[220:223], v[204:207]\n"
        "v_mul_f32 v181, v243, v181\n"
        "v_mul_f32 v182, v243, v182\n"
        "v_mfma_f32_16x16x32_bf16 v[208:211], v[76:79], a[220:223], v[208:211]\n"
        "v_mul_f32 v183, v243, v183\n"
        "v_mul_f32 v184, v243, v184\n"
        "v_mfma_f32_16x16x32_bf16 v[212:215], v[80:83], a[220:223], v[212:215]\n"
        "buffer_load_dwordx4 a[220:223], v12, s[40:43], s22 offen offset:3072 sc1\n"
        "s_waitcnt vmcnt(11)\n"
        "v_mfma_f32_16x16x32_bf16 v[196:199], v[84:87], v[36:39], v[196:199]\n"
        "v_mul_f32 v185, v243, v185\n"
        "v_mul_f32 v186, v243, v186\n"
        "v_mfma_f32_16x16x32_bf16 v[200:203], v[88:91], v[36:39], v[200:203]\n"
        "v_mul_f32 v187, v243, v187\n"
        "v_mul_f32 v188, v243, v188\n"
        "v_mfma_f32_16x16x32_bf16 v[204:207], v[92:95], v[36:39], v[204:207]\n"
        "v_mul_f32 v189, v243, v189\n"
        "v_mul_f32 v190, v243, v190\n"
        "v_mfma_f32_16x16x32_bf16 v[208:211], v[96:99], v[36:39], v[208:211]\n"
        "v_mul_f32 v191, v243, v191\n"
        "v_mul_f32 v192, v243, v192\n"
        "v_mfma_f32_16x16x32_bf16 v[212:215], v[100:103], v[36:39], v[212:215]\n"
        "buffer_load_dwordx4 v[36:39], v13, s[40:43], s22 offen offset:0 sc1\n"
        "s_waitcnt vmcnt(11)\n"
        "v_mfma_f32_16x16x32_bf16 v[196:199], a[224:227], v[40:43], v[196:199]\n"
        "v_mul_f32 v193, v243, v193\n"
        "v_mul_f32 v194, v243, v194\n"
        "v_mfma_f32_16x16x32_bf16 v[200:203], a[228:231], v[40:43], v[200:203]\n"
        "v_mul_f32 v195, v243, v195\n"
        "v_mfma_f32_16x16x32_bf16 v[204:207], a[232:235], v[40:43], v[204:207]\n"
        "v_mfma_f32_16x16x32_bf16 v[208:211], a[236:239], v[40:43], v[208:211]\n"
        "v_mfma_f32_16x16x32_bf16 v[212:215], a[240:243], v[40:43], v[212:215]\n"
        "buffer_load_dwordx4 v[40:43], v12, s[40:43], s23 offen offset:0 sc1\n"
        "s_waitcnt vmcnt(11)\n"
        "v_mfma_f32_16x16x32_bf16 v[196:199], v[56:59], v[44:47], v[196:199]\n"
        "v_mfma_f32_16x16x32_bf16 v[200:203], v[60:63], v[44:47], v[200:203]\n"
        "v_mfma_f32_16x16x32_bf16 v[204:207], v[104:107], v[44:47], v[204:207]\n"
        "s_nop 5\n"
        "v_fma_f32 v116, v236, v196, v116\n"
        "v_fma_f32 v117, v236, v197, v117\n"
        "v_mfma_f32_16x16x32_bf16 v[208:211], v[108:111], v[44:47], v[208:211]\n"
        "v_fma_f32 v118, v236, v198, v118\n"
        "v_fma_f32 v119, v236, v199, v119\n"
        "v_mfma_f32_16x16x32_bf16 v[212:215], v[112:115], v[44:47], v[212:215]\n"
        "buffer_load_dwordx4 v[44:47], v12, s[40:43], s23 offen offset:1024 sc1\n"
        "s_waitcnt vmcnt(11)\n"
        "v_mfma_f32_16x16x32_bf16 v[216:219], a[176:179], v[48:51], 0\n"
        "v_fma_f32 v120, v236, v200, v120\n"
        "v_fma_f32 v121, v236, v201, v121\n"
        "v_mfma_f32_16x16x32_bf16 v[220:223], a[180:183], v[48:51], 0\n"
        "v_fma_f32 v122, v236, v202, v122\n"
        "v_fma_f32 v123, v236, v203, v123\n"
        "v_mfma_f32_16x16x32_bf16 v[224:227], a[184:187], v[48:51], 0\n"
        "v_fma_f32 v124, v236, v204, v124\n"
        "v_fma_f32 v125, v236, v205, v125\n"
        "v_mfma_f32_16x16x32_bf16 v[228:231], a[188:191], v[48:51], 0\n"
        "v_fma_f32 v126, v236, v206, v126\n"
        "v_fma_f32 v127, v236, v207, v127\n"
        "v_mfma_f32_16x16x32_bf16 v[232:235], a[192:195], v[48:51], 0\n"
        "buffer_load_dwordx4 v[48:51], v12, s[40:43], s23 offen offset:2048 sc1\n"
        "s_waitcnt vmcnt(11)\n"
        "v_mfma_f32_16x16x32_bf16 v[216:219], v[64:67], v[52:55], v[216:219]\n"
        "v_fma_f32 v128, v236, v208, v128\n"
        "v_fma_f32 v129, v236, v209, v129\n"
        "v_mfma_f32_16x16x32_bf16 v[220:223], v[68:71], v[52:55], v[220:223]\n"
        "v_fma_f32 v130, v236, v210, v130\n"
        "v_fma_f32 v131, v236, v211, v131\n"
        "v_mfma_f32_16x16x32_bf16 v[224:227], v[72:75], v[52:55], v[224:227]\n"
        "v_fma_f32 v132, v236, v212, v132\n"
        "v_fma_f32 v133, v236, v213, v133\n"
        "v_mfma_f32_16x16x32_bf16 v[228:231], v[76:79], v[52:55], v[228:231]\n"
        "v_fma_f32 v134, v236, v214, v134\n"
        "v_fma_f32 v135, v236, v215, v135\n"
        "v_mfma_f32_16x16x32_bf16 v[232:235], v[80:83], v[52:55], v[232:235]\n"
        "buffer_load_dwordx4 v[52:55], v12, s[40:43], s23 offen offset:3072 sc1\n"
        "s_waitcnt vmcnt(8)\n"
        "v_mfma_f32_16x16x32_bf16 v[196:199], a[176:179], a[208:211], 0\n"
        "ds_write_b128 v255, v[116:119] offset:0\n"
        "v_mfma_f32_16x16x32_bf16 v[200:203], a[180:183], a[208:211], 0\n"
        "ds_write_b128 v255, v[120:123] offset:64\n"
        "v_mfma_f32_16x16x32_bf16 v[204:207], a[184:187], a[208:211], 0\n"
        "ds_write_b128 v255, v[124:127] offset:128\n"
        "v_mfma_f32_16x16x32_bf16 v[208:211], a[188:191], a[208:211], 0\n"
        "ds_write_b128 v255, v[128:131] offset:192\n"
        "v_mfma_f32_16x16x32_bf16 v[212:215], a[192:195], a[208:211], 0\n"
        "ds_write_b128 v255, v[132:135] offset:256\n"
        "v_mfma_f32_16x16x32_bf16 v[216:219], v[84:87], a[244:247], v[216:219]\n"
        "v_add_u32 v255, s29, v254\n"
        "v_mfma_f32_16x16x32_bf16 v[220:223], v[88:91], a[244:247], v[220:223]\n"
        "v_mfma_f32_16x16x32_bf16 v[224:227], v[92:95], a[244:247], v[224:227]\n"
        "v_mfma_f32_16x16x32_bf16 v[228:231], v[96:99], a[244:247], v[228:231]\n"
        "v_mfma_f32_16x16x32_bf16 v[232:235], v[100:103], a[244:247], v[232:235]\n"
        "buffer_load_dwordx4 a[244:247], v13, s[40:43], s23 offen offset:0 sc1\n"
        "s_waitcnt vmcnt(8)\n"
        "v_mfma_f32_16x16x32_bf16 v[196:199], v[64:67], a[212:215], v[196:199]\n"
        "v_mfma_f32_16x16x32_bf16 v[200:203], v[68:71], a[212:215], v[200:203]\n"
        "v_mfma_f32_16x16x32_bf16 v[204:207], v[72:75], a[212:215], v[204:207]\n"
        "v_mfma_f32_16x16x32_bf16 v[208:211], v[76:79], a[212:215], v[208:211]\n"
        "v_mfma_f32_16x16x32_bf16 v[212:215], v[80:83], a[212:215], v[212:215]\n"
        "v_mfma_f32_16x16x32_bf16 v[216:219], a[224:227], a[200:203], v[216:219]\n"
        "v_mfma_f32_16x16x32_bf16 v[220:223], a[228:231], a[200:203], v[220:223]\n"
        "v_mfma_f32_16x16x32_bf16 v[224:227], a[232:235], a[200:203], v[224:227]\n"
        "v_mfma_f32_16x16x32_bf16 v[228:231], a[236:239], a[200:203], v[228:231]\n"
        "v_mfma_f32_16x16x32_bf16 v[232:235], a[240:243], a[200:203], v[232:235]\n"
        "s_waitcnt vmcnt(7)\n"
        "v_mfma_f32_16x16x32_bf16 v[196:199], v[84:87], a[216:219], v[196:199]\n"
        "v_mfma_f32_16x16x32_bf16 v[200:203], v[88:91], a[216:219], v[200:203]\n"
        "v_mfma_f32_16x16x32_bf16 v[204:207], v[92:95], a[216:219], v[204:207]\n"
        "v_mfma_f32_16x16x32_bf16 v[208:211], v[96:99], a[216:219], v[208:211]\n"
        "v_mfma_f32_16x16x32_bf16 v[212:215], v[100:103], a[216:219], v[212:215]\n"
        "v_mfma_f32_16x16x32_bf16 v[216:219], v[56:59], a[204:207], v[216:219]\n"
        "v_mfma_f32_16x16x32_bf16 v[220:223], v[60:63], a[204:207], v[220:223]\n"
        "v_mfma_f32_16x16x32_bf16 v[224:227], v[104:107], a[204:207], v[224:227]\n"
        "s_nop 5\n"
        "v_fma_f32 v136, v238, v216, v136\n"
        "v_fma_f32 v137, v238, v217, v137\n"
        "v_mfma_f32_16x16x32_bf16 v[228:231], v[108:111], a[204:207], v[228:231]\n"
        "v_fma_f32 v138, v238, v218, v138\n"
        "v_fma_f32 v139, v238, v219, v139\n"
        "v_mfma_f32_16x16x32_bf16 v[232:235], v[112:115], a[204:207], v[232:235]\n"
        "v_fma_f32 v140, v238, v220, v140\n"
        "v_fma_f32 v141, v238, v221, v141\n"
        "s_waitcnt vmcnt(6)\n"
        "v_mfma_f32_16x16x32_bf16 v[196:199], a[224:227], a[220:223], v[196:199]\n"
        "v_fma_f32 v142, v238, v222, v142\n"
        "v_fma_f32 v143, v238, v223, v143\n"
        "v_mfma_f32_16x16x32_bf16 v[200:203], a[228:231], a[220:223], v[200:203]\n"
        "v_fma_f32 v144, v238, v224, v144\n"
        "v_fma_f32 v145, v238, v225, v145\n"
        "v_mfma_f32_16x16x32_bf16 v[204:207], a[232:235], a[220:223], v[204:207]\n"
        "v_fma_f32 v146, v238, v226, v146\n"
        "v_fma_f32 v147, v238, v227, v147\n"
        "v_mfma_f32_16x16x32_bf16 v[208:211], a[236:239], a[220:223], v[208:211]\n"
        "v_fma_f32 v148, v238, v228, v148\n"
        "v_fma_f32 v149, v238, v229, v149\n"
        "v_mfma_f32_16x16x32_bf16 v[212:215], a[240:243], a[220:223], v[212:215]\n"
        "v_fma_f32 v150, v238, v230, v150\n"
        "v_fma_f32 v151, v238, v231, v151\n"
        "s_waitcnt vmcnt(5)\n"
        "v_mfma_f32_16x16x32_bf16 v[196:199], v[56:59], v[36:39], v[196:199]\n"
        "v_fma_f32 v152, v238, v232, v152\n"
        "v_fma_f32 v153, v238, v233, v153\n"
        "v_mfma_f32_16x16x32_bf16 v[200:203], v[60:63], v[36:39], v[200:203]\n"
        "v_fma_f32 v154, v238, v234, v154\n"
        "v_fma_f32 v155, v238, v235, v155\n"
        "v_mfma_f32_16x16x32_bf16 v[204:207], v[104:107], v[36:39], v[204:207]\n"
        "ds_write_b128 v255, v[136:139] offset:0\n"
        "v_mfma_f32_16x16x32_bf16 v[208:211], v[108:111], v[36:39], v[208:211]\n"
        "ds_write_b128 v255, v[140:143] offset:64\n"
        "v_mfma_f32_16x16x32_bf16 v[212:215], v[112:115], v[36:39], v[212:215]\n"
        "ds_write_b128 v255, v[144:147] offset:128\n"
        "s_waitcnt vmcnt(4)\n"
        "v_mfma_f32_16x16x32_bf16 v[216:219], a[176:179], v[40:43], v[4:7]\n"
        "ds_write_b128 v255, v[148:151] offset:192\n"
        "v_mfma_f32_16x16x32_bf16 v[220:223], a[180:183], v[40:43], v[4:7]\n"
        "ds_write_b128 v255, v[152:155] offset:256\n"
        "v_mfma_f32_16x16x32_bf16 v[224:227], a[184:187], v[40:43], v[4:7]\n"
        "v_add_u32 v255, s30, v254\n"
        "v_fma_f32 v156, v240, v196, v156\n"
        "v_mfma_f32_16x16x32_bf16 v[228:231], a[188:191], v[40:43], v[4:7]\n"
        "v_fma_f32 v157, v240, v197, v157\n"
        "v_fma_f32 v158, v240, v198, v158\n"
        "v_mfma_f32_16x16x32_bf16 v[232:235], a[192:195], v[40:43], v[4:7]\n"
        "v_fma_f32 v159, v240, v199, v159\n"
        "v_fma_f32 v160, v240, v200, v160\n"
        "s_waitcnt vmcnt(3)\n"
        "v_mfma_f32_16x16x32_bf16 v[216:219], v[64:67], v[44:47], v[216:219]\n"
        "v_fma_f32 v161, v240, v201, v161\n"
        "v_fma_f32 v162, v240, v202, v162\n"
        "v_mfma_f32_16x16x32_bf16 v[220:223], v[68:71], v[44:47], v[220:223]\n"
        "v_fma_f32 v163, v240, v203, v163\n"
        "v_fma_f32 v164, v240, v204, v164\n"
        "v_mfma_f32_16x16x32_bf16 v[224:227], v[72:75], v[44:47], v[224:227]\n"
        "v_fma_f32 v165, v240, v205, v165\n"
        "v_fma_f32 v166, v240, v206, v166\n"
        "v_mfma_f32_16x16x32_bf16 v[228:231], v[76:79], v[44:47], v[228:231]\n"
        "v_fma_f32 v167, v240, v207, v167\n"
        "v_fma_f32 v168, v240, v208, v168\n"
        "v_mfma_f32_16x16x32_bf16 v[232:235], v[80:83], v[44:47], v[232:235]\n"
        "v_fma_f32 v169, v240, v209, v169\n"
        "v_fma_f32 v170, v240, v210, v170\n"
        "s_waitcnt vmcnt(2)\n"
        "v_mfma_f32_16x16x32_bf16 v[216:219], v[84:87], v[48:51], v[216:219]\n"
        "v_fma_f32 v171, v240, v211, v171\n"
        "v_fma_f32 v172, v240, v212, v172\n"
        "v_mfma_f32_16x16x32_bf16 v[220:223], v[88:91], v[48:51], v[220:223]\n"
        "v_fma_f32 v173, v240, v213, v173\n"
        "v_fma_f32 v174, v240, v214, v174\n"
        "v_mfma_f32_16x16x32_bf16 v[224:227], v[92:95], v[48:51], v[224:227]\n"
        "v_fma_f32 v175, v240, v215, v175\n"
        "ds_write_b128 v255, v[156:159] offset:0\n"
        "v_mfma_f32_16x16x32_bf16 v[228:231], v[96:99], v[48:51], v[228:231]\n"
        "ds_write_b128 v255, v[160:163] offset:64\n"
        "v_mfma_f32_16x16x32_bf16 v[232:235], v[100:103], v[48:51], v[232:235]\n"
        "ds_write_b128 v255, v[164:167] offset:128\n"
        "ds_write_b128 v255, v[168:171] offset:192\n"
        "ds_write_b128 v255, v[172:175] offset:256\n"
        "s_waitcnt lgkmcnt(0)\n"
        "s_barrier\n"
        "s_waitcnt vmcnt(1)\n"
        "v_mfma_f32_16x16x32_bf16 v[216:219], a[224:227], v[52:55], v[216:219]\n"
        "ds_read_b128 v[116:119], v244 offset:0\n"
        "ds_read_b128 v[120:123], v245 offset:0\n"
        "v_mfma_f32_16x16x32_bf16 v[220:223], a[228:231], v[52:55], v[220:223]\n"
        "ds_read_b128 v[124:127], v246 offset:0\n"
        "ds_read_b128 v[128:131], v247 offset:0\n"
        "v_mfma_f32_16x16x32_bf16 v[224:227], a[232:235], v[52:55], v[224:227]\n"
        "ds_read_b128 v[132:135], v248 offset:0\n"
        "ds_read_b128 v[136:139], v244 offset:5376\n"
        "v_mfma_f32_16x16x32_bf16 v[228:231], a[236:239], v[52:55], v[228:231]\n"
        "ds_read_b128 v[140:143], v245 offset:5376\n"
        "ds_read_b128 v[144:147], v246 offset:5376\n"
        "v_mfma_f32_16x16x32_bf16 v[232:235], a[240:243], v[52:55], v[232:235]\n"
        "ds_read_b128 v[148:151], v247 offset:5376\n"
        "ds_read_b128 v[152:155], v248 offset:5376\n"
        "s_waitcnt vmcnt(0)\n"
        "v_mfma_f32_16x16x32_bf16 v[216:219], v[56:59], a[244:247], v[216:219]\n"
        "ds_read_b128 v[156:159], v244 offset:10752\n"
        "v_add_u32 v255, s31, v254\n"
        "v_mfma_f32_16x16x32_bf16 v[220:223], v[60:63], a[244:247], v[220:223]\n"
        "ds_read_b128 v[160:163], v245 offset:10752\n"
        "ds_read_b128 v[164:167], v246 offset:10752\n"
        "v_mfma_f32_16x16x32_bf16 v[224:227], v[104:107], a[244:247], v[224:227]\n"
        "s_nop 1\n"
        "v_fma_f32 v176, v242, v216, v176\n"
        "v_fma_f32 v177, v242, v217, v177\n"
        "v_mfma_f32_16x16x32_bf16 v[228:231], v[108:111], a[244:247], v[228:231]\n"
        "v_fma_f32 v178, v242, v218, v178\n"
        "v_fma_f32 v179, v242, v219, v179\n"
        "v_mfma_f32_16x16x32_bf16 v[232:235], v[112:115], a[244:247], v[232:235]\n"
        "v_fma_f32 v180, v242, v220, v180\n"
        "v_fma_f32 v181, v242, v221, v181\n"
        "v_fma_f32 v182, v242, v222, v182\n"
        "v_fma_f32 v183, v242, v223, v183\n"
        "v_fma_f32 v184, v242, v224, v184\n"
        "v_fma_f32 v185, v242, v225, v185\n"
        "v_fma_f32 v186, v242, v226, v186\n"
        "v_fma_f32 v187, v242, v227, v187\n"
        "ds_read_b128 v[168:171], v247 offset:10752\n"
        "v_fma_f32 v188, v242, v228, v188\n"
        "v_fma_f32 v189, v242, v229, v189\n"
        "v_fma_f32 v190, v242, v230, v190\n"
        "v_fma_f32 v191, v242, v231, v191\n"
        "ds_read_b128 v[172:175], v248 offset:10752\n"
        "v_fma_f32 v192, v242, v232, v192\n"
        "v_fma_f32 v193, v242, v233, v193\n"
        "v_fma_f32 v194, v242, v234, v194\n"
        "v_fma_f32 v195, v242, v235, v195\n"
        "ds_write_b128 v255, v[176:179] offset:0\n"
        "ds_write_b128 v255, v[180:183] offset:64\n"
        "ds_write_b128 v255, v[184:187] offset:128\n"
        "ds_write_b128 v255, v[188:191] offset:192\n"
        "ds_write_b128 v255, v[192:195] offset:256\n"
        "ds_read_b128 v[196:199], v244 offset:16128\n"
        "ds_read_b128 v[200:203], v245 offset:16128\n"
        "ds_read_b128 v[204:207], v246 offset:16128\n"
        "ds_read_b128 v[208:211], v247 offset:16128\n"
        "ds_read_b128 v[212:215], v248 offset:16128\n"
        "s_waitcnt lgkmcnt(15)\n"
        "v_add_f32 v116, v116, v136\n"
        "v_add_f32 v117, v117, v137\n"
        "v_add_f32 v118, v118, v138\n"
        "v_add_f32 v119, v119, v139\n"
        "v_add_f32 v120, v120, v140\n"
        "v_add_f32 v121, v121, v141\n"
        "v_add_f32 v122, v122, v142\n"
        "v_add_f32 v123, v123, v143\n"
        "v_add_f32 v124, v124, v144\n"
        "v_add_f32 v125, v125, v145\n"
        "v_add_f32 v126, v126, v146\n"
        "v_add_f32 v127, v127, v147\n"
        "v_add_f32 v128, v128, v148\n"
        "v_add_f32 v129, v129, v149\n"
        "v_add_f32 v130, v130, v150\n"
        "v_add_f32 v131, v131, v151\n"
        "v_add_f32 v132, v132, v152\n"
        "v_add_f32 v133, v133, v153\n"
        "v_add_f32 v134, v134, v154\n"
        "v_add_f32 v135, v135, v155\n"
        "s_waitcnt lgkmcnt(14)\n"
        "v_add_f32 v116, v116, v156\n"
        "v_add_f32 v117, v117, v157\n"
        "v_add_f32 v118, v118, v158\n"
        "v_add_f32 v119, v119, v159\n"
        "s_waitcnt lgkmcnt(13)\n"
        "v_add_f32 v120, v120, v160\n"
        "v_add_f32 v121, v121, v161\n"
        "v_add_f32 v122, v122, v162\n"
        "v_add_f32 v123, v123, v163\n"
        "s_waitcnt lgkmcnt(12)\n"
        "v_add_f32 v124, v124, v164\n"
        "v_add_f32 v125, v125, v165\n"
        "v_add_f32 v126, v126, v166\n"
        "v_add_f32 v127, v127, v167\n"
        "s_waitcnt lgkmcnt(11)\n"
        "v_add_f32 v128, v128, v168\n"
        "v_add_f32 v129, v129, v169\n"
        "v_add_f32 v130, v130, v170\n"
        "v_add_f32 v131, v131, v171\n"
        "s_waitcnt lgkmcnt(10)\n"
        "v_add_f32 v132, v132, v172\n"
        "v_add_f32 v133, v133, v173\n"
        "v_add_f32 v134, v134, v174\n"
        "v_add_f32 v135, v135, v175\n"
        "s_waitcnt lgkmcnt(4)\n"
        "v_pk_add_f32 v[116:117], v[116:117], v[196:197]\n"
        "v_pk_add_f32 v[118:119], v[118:119], v[198:199]\n"
        "s_waitcnt lgkmcnt(3)\n"
        "v_pk_add_f32 v[120:121], v[120:121], v[200:201]\n"
        "v_pk_add_f32 v[122:123], v[122:123], v[202:203]\n"
        "s_waitcnt lgkmcnt(2)\n"
        "v_pk_add_f32 v[124:125], v[124:125], v[204:205]\n"
        "v_pk_add_f32 v[126:127], v[126:127], v[206:207]\n"
        "s_waitcnt lgkmcnt(1)\n"
        "v_pk_add_f32 v[128:129], v[128:129], v[208:209]\n"
        "v_pk_add_f32 v[130:131], v[130:131], v[210:211]\n"
        "s_waitcnt lgkmcnt(0)\n"
        "v_pk_add_f32 v[132:133], v[132:133], v[212:213]\n"
        "v_pk_add_f32 v[134:135], v[134:135], v[214:215]\n"
        "buffer_store_dwordx4 v[116:119], v249, s[36:39], s46 offen sc1\n"
        "buffer_store_dwordx4 v[120:123], v250, s[36:39], s46 offen sc1\n"
        "buffer_store_dwordx4 v[124:127], v251, s[36:39], s46 offen sc1\n"
        "buffer_store_dwordx4 v[128:131], v252, s[36:39], s46 offen sc1\n"
        "buffer_store_dwordx4 v[132:135], v253, s[36:39], s46 offen sc1\n"
        "END_%=:\n"
        : "+{v0}"(tid), "+{s3}"(lb)
        : "{s[0:1]}"(ka), "{s2}"(bid)
        : "v1", "v2", "v3", "v4", "v5", "v6", "v7", "v8", "v9", "v10", "v11", "v12", "v13", "v14", "v15", "v16", "v17", "v18", "v19", "v20", "v21", "v22", "v23", "v24", "v25", "v26", "v27", "v28", "v29", "v30", "v31", "v32", "v33", "v34", "v35", "v36", "v37", "v38", "v39", "v40", "v41", "v42", "v43", "v44", "v45", "v46", "v47", "v48", "v49", "v50", "v51", "v52", "v53", "v54", "v55", "v56", "v57", "v58", "v59", "v60", "v61", "v62", "v63", "v64", "v65", "v66", "v67", "v68", "v69", "v70", "v71", "v72", "v73", "v74", "v75", "v76", "v77", "v78", "v79", "v80", "v81", "v82", "v83", "v84", "v85", "v86", "v87", "v88", "v89", "v90", "v91", "v92", "v93", "v94", "v95", "v96", "v97", "v98", "v99", "v100", "v101", "v102", "v103", "v104", "v105", "v106", "v107", "v108", "v109", "v110", "v111", "v112", "v113", "v114", "v115", "v116", "v117", "v118", "v119", "v120", "v121", "v122", "v123", "v124", "v125", "v126", "v127", "v128", "v129", "v130", "v131", "v132", "v133", "v134", "v135", "v136", "v137", "v138", "v139", "v140", "v141", "v142", "v143", "v144", "v145", "v146", "v147", "v148", "v149", "v150", "v151", "v152", "v153", "v154", "v155", "v156", "v157", "v158", "v159", "v160", "v161", "v162", "v163", "v164", "v165", "v166", "v167", "v168", "v169", "v170", "v171", "v172", "v173", "v174", "v175", "v176", "v177", "v178", "v179", "v180", "v181", "v182", "v183", "v184", "v185", "v186", "v187", "v188", "v189", "v190", "v191", "v192", "v193", "v194", "v195", "v196", "v197", "v198", "v199", "v200", "v201", "v202", "v203", "v204", "v205", "v206", "v207", "v208", "v209", "v210", "v211", "v212", "v213", "v214", "v215", "v216", "v217", "v218", "v219", "v220", "v221", "v222", "v223", "v224", "v225", "v226", "v227", "v228", "v229", "v230", "v231", "v232", "v233", "v234", "v235", "v236", "v237", "v238", "v239", "v240", "v241", "v242", "v243", "v244", "v245", "v246", "v247", "v248", "v249", "v250", "v251", "v252", "v253", "v254", "v255", "a0", "a1", "a2", "a3", "a4", "a5", "a6", "a7", "a8", "a9", "a10", "a11", "a12", "a13", "a14", "a15", "a16", "a17", "a18", "a19", "a20", "a21", "a22", "a23", "a24", "a25", "a26", "a27", "a28", "a29", "a30", "a31", "a32", "a33", "a34", "a35", "a36", "a37", "a38", "a39", "a40", "a41", "a42", "a43", "a44", "a45", "a46", "a47", "a48", "a49", "a50", "a51", "a52", "a53", "a54", "a55", "a56", "a57", "a58", "a59", "a60", "a61", "a62", "a63", "a64", "a65", "a66", "a67", "a68", "a69", "a70", "a71", "a72", "a73", "a74", "a75", "a76", "a77", "a78", "a79", "a80", "a81", "a82", "a83", "a84", "a85", "a86", "a87", "a88", "a89", "a90", "a91", "a92", "a93", "a94", "a95", "a96", "a97", "a98", "a99", "a100", "a101", "a102", "a103", "a104", "a105", "a106", "a107", "a108", "a109", "a110", "a111", "a112", "a113", "a114", "a115", "a116", "a117", "a118", "a119", "a120", "a121", "a122", "a123", "a124", "a125", "a126", "a127", "a128", "a129", "a130", "a131", "a132", "a133", "a134", "a135", "a136", "a137", "a138", "a139", "a140", "a141", "a142", "a143", "a144", "a145", "a146", "a147", "a148", "a149", "a150", "a151", "a152", "a153", "a154", "a155", "a156", "a157", "a158", "a159", "a160", "a161", "a162", "a163", "a164", "a165", "a166", "a167", "a168", "a169", "a170", "a171", "a172", "a173", "a174", "a175", "a176", "a177", "a178", "a179", "a180", "a181", "a182", "a183", "a184", "a185", "a186", "a187", "a188", "a189", "a190", "a191", "a192", "a193", "a194", "a195", "a196", "a197", "a198", "a199", "a200", "a201", "a202", "a203", "a204", "a205", "a206", "a207", "a208", "a209", "a210", "a211", "a212", "a213", "a214", "a215", "a216", "a217", "a218", "a219", "a220", "a221", "a222", "a223", "a224", "a225", "a226", "a227", "a228", "a229", "a230", "a231", "a232", "a233", "a234", "a235", "a236", "a237", "a238", "a239", "a240", "a241", "a242", "a243", "a244", "a245", "a246", "a247", "a248", "a249", "a250", "a251", "a252", "a253", "a254", "a255", "s4", "s5", "s6", "s7", "s8", "s9", "s10", "s11", "s12", "s13", "s14", "s15", "s16", "s17", "s18", "s19", "s20", "s21", "s22", "s23", "s24", "s25", "s26", "s27", "s28", "s29", "s30", "s31", "s33", "s34", "s35", "s36", "s37", "s38", "s39", "s40", "s41", "s42", "s43", "s44", "s45", "s46", "s47", "s48", "s49", "s50", "s51", "s52", "s53", "s54", "s55", "s56", "s57", "s58", "s59", "s60", "s61", "s62", "s63", "s64", "s65", "s66", "s67", "s68", "s69", "s70", "s71", "s72", "s73", "s74", "s75", "s76", "s77", "s78", "s79", "s80", "s81", "s82", "s83", "s84", "s85", "s86", "s87", "s88", "s89", "s90", "s91", "s92", "s93", "s94", "s95", "s96", "s97", "s98", "s99", "vcc", "scc", "memory");
}

extern "C" void kernel_launch(void* const* d_in, const int* in_sizes, int n_in, void* d_out, int out_size, void* d_ws,
                              size_t ws_size, hipStream_t stream) {
    Args a;
    const int idx[14] = {0, 1, 2, 6, 7, 8, 9, -1, -2, 4, 5, 10, 11, 12};
    for (int i = 0; i < 14; ++i) {
        const int k = idx[i];
        a.d[i].p = k >= 0 ? d_in[k] : (k == -1 ? d_out : d_ws);
        a.d[i].n = k >= 0 ? 2u * (unsigned)in_sizes[k] : (k == -1 ? 4u * (unsigned)out_size : (unsigned)18874368);
        a.d[i].f = 0x00020000u;
    }
    gmix_kernel<<<256, 256, 0, stream>>>(a);
}
```
